```python
import torch

def _softmax(x, axis=-1):
    return torch.softmax(x, dim=axis)
def _gelu(x):
    return torch.nn.functional.gelu(x, approximate="tanh")
def _silu(x):
    return torch.nn.functional.silu(x)
def _sigmoid(x):
    return torch.sigmoid(x)
def _layernorm(x, axis=-1, eps=1e-5):
    m = x.mean(dim=axis, keepdim=True)
    v = x.var(dim=axis, keepdim=True, unbiased=False)
    return (x - m) / torch.sqrt(v + eps)
def _scatter_add(tgt, idx, src, axis=0):
    return tgt.clone().index_add_(axis, idx, src)
def _segment_sum(src, seg, n):
    out = torch.zeros((n,) + src.shape[1:], dtype=src.dtype)
    return out.index_add_(0, seg, src)
def _pad(x, pw):
    flat = tuple(v for pair in reversed(pw) for v in pair)
    return torch.nn.functional.pad(x, flat)

kernel_name = 'recurrent_scan__compressed_kv_cla__mixer_s_mlp__prompt'

def setup_inputs():

    _w = (lambda a: (lambda t: t.to(torch.bfloat16) if t.is_floating_point() else t)(torch.as_tensor(a)))
    def _A_setup():
        B, H, Sq, Sk, D, M = 1, 32, 2048, 2048, 128, 640
        Dc = 4 * D
        return {
            "x": _w((lambda *s: torch.randn(*s, dtype=torch.bfloat16) * 0.05)(B, Sq, M)),
            "c_kv": _w((lambda *s: torch.randn(*s, dtype=torch.bfloat16) * 0.2)(B, Sk, Dc)),
            "w_q": _w((lambda *s: torch.randn(*s, dtype=torch.bfloat16) * 3.2)(M, H, D) * (M ** -0.5)),
            "w_uk": _w((lambda *s: torch.randn(*s, dtype=torch.bfloat16) * 3.2)(Dc, H, D) * (Dc ** -0.5)),
            "w_uv": _w((lambda *s: torch.randn(*s, dtype=torch.bfloat16) * 0.2)(Dc, H, D) * (Dc ** -0.5)),
            "w_o": _w((lambda *s: torch.randn(*s, dtype=torch.bfloat16) * 0.2)(H, D, M) * ((H * D) ** -0.5)),
        }
    _A_in = _A_setup()

    def _B_setup():
        B, S, D, F = 1, 2048, 640, 1728
        return {
            "x":  _w((lambda *s: torch.randn(*s, dtype=torch.bfloat16) * 0.05)(B, S, D)),
            "w1": _w((lambda *s: torch.randn(*s, dtype=torch.bfloat16) * 3.2)(D, D) * (D ** -0.5)),
            "w2": _w((lambda *s: torch.randn(*s, dtype=torch.bfloat16) * 3.2)(D, D) * (D ** -0.5)),
            "w3": _w((lambda *s: torch.randn(*s, dtype=torch.bfloat16) * 3.2)(D, F) * (D ** -0.5)),
            "w4": _w((lambda *s: torch.randn(*s, dtype=torch.bfloat16) * 1.6)(F, D) * (F ** -0.5)),
        }
    _B_in = _B_setup()

    return {
        "A_x": _A_in["x"],
        "A_c_kv": _A_in["c_kv"],
        "A_w_q": _A_in["w_q"],
        "A_w_uk": _A_in["w_uk"],
        "A_w_uv": _A_in["w_uv"],
        "A_w_o": _A_in["w_o"],
        "B_x": _B_in["x"],
        "B_w1": _B_in["w1"],
        "B_w2": _B_in["w2"],
        "B_w3": _B_in["w3"],
        "B_w4": _B_in["w4"],
    }

def reference(A_x, A_c_kv, A_w_q, A_w_uk, A_w_uv, A_w_o, B_x, B_w1, B_w2, B_w3, B_w4):
    def _A_ref(x, c_kv, w_q, w_uk, w_uv, w_o):
        B, Sq, M = x.shape[0], x.shape[1], x.shape[2]
        Sk = c_kv.shape[1]
        H, D = w_q.shape[1], w_q.shape[2]
        q = torch.einsum("bqm,mhd->bhqd", x, w_q)
        k = torch.einsum("bkc,chd->bhkd", c_kv, w_uk)
        v = torch.einsum("bkc,chd->bhkd", c_kv, w_uv)
        scores = torch.einsum("bhqd,bhkd->bhqk", q, k) * (D ** -0.5)
        off = Sk - Sq
        qi = torch.arange(Sq)[:, None]
        kj = torch.arange(Sk)[None, :]
        cmask = kj <= (qi + off)
        scores = torch.where(cmask[None, None], scores, -1e9)
        p = _softmax(scores, axis=-1)
        ctx = torch.einsum("bhqk,bhkd->bhqd", p, v)
        return torch.einsum("bhqd,hdm->bqm", ctx, w_o)

    def _B_ref(x, w1, w2, w3, w4):
        B, S, D = x.shape
        h = _gelu(_layernorm(x, axis=-1) @ w1)
        c = D // 4
        g0 = _pad(h[:, :, 0*c:1*c], ((0, 0), (1, 0), (0, 0)))[:, :S, :]
        g1 = _pad(h[:, :, 1*c:2*c], ((0, 0), (0, 1), (0, 0)))[:, 1:, :]
        g2 = _pad(h[:, :, 2*c:3*c], ((0, 0), (2, 0), (0, 0)))[:, :S, :]
        g3 = _pad(h[:, :, 3*c:   ], ((0, 0), (0, 2), (0, 0)))[:, 2:, :]
        hs = torch.concatenate([g0, g1, g2, g3], axis=-1) @ w2
        x1 = x + hs
        h2 = _gelu(_layernorm(x1, axis=-1) @ w3) @ w4
        return x1 + h2

    x = A_x
    xn = _layernorm(x, axis=-1)
    s = _A_ref(xn, A_c_kv, A_w_q, A_w_uk, A_w_uv, A_w_o)
    g = _silu(_B_ref(xn, B_w1, B_w2, B_w3, B_w4))
    return x + s * g
```

```cpp
#include <hip/hip_runtime.h>
#include <hip/hip_bf16.h>
#include <stdint.h>
typedef unsigned short u16;
static __device__ __forceinline__ float bf2f(u16 b){ return __uint_as_float(((unsigned)b)<<16); }
static __device__ __forceinline__ u16 f2bf(float f){ unsigned u=__float_as_uint(f); u += 0x7FFFu + ((u>>16)&1u); return (u16)(u>>16); }
static __device__ __forceinline__ float rbf(float f){ return bf2f(f2bf(f)); }

__global__ void nv_ln(const u16* x, u16* y, int D){
  int row=blockIdx.x; __shared__ float red[256];
  float s=0; for(int i=threadIdx.x;i<D;i+=256) s+=bf2f(x[row*D+i]);
  red[threadIdx.x]=s; __syncthreads();
  for(int o=128;o>0;o>>=1){ if(threadIdx.x<o) red[threadIdx.x]+=red[threadIdx.x+o]; __syncthreads(); }
  float mean=red[0]/D; __syncthreads();
  float q=0; for(int i=threadIdx.x;i<D;i+=256){ float d=bf2f(x[row*D+i])-mean; q+=d*d; }
  red[threadIdx.x]=q; __syncthreads();
  for(int o=128;o>0;o>>=1){ if(threadIdx.x<o) red[threadIdx.x]+=red[threadIdx.x+o]; __syncthreads(); }
  float var=red[0]/D;
  float m=rbf(mean), v=rbf(var); float den=rbf(sqrtf(rbf(v+1e-5f)));
  for(int i=threadIdx.x;i<D;i+=256){ float d=rbf(bf2f(x[row*D+i])-m); y[row*D+i]=f2bf(d/den); }
}
__global__ void nv_gemm(const u16* A, const u16* B, u16* C, int M,int N,int K,int act){
  __shared__ float As[16][17], Bs[16][17];
  int r=blockIdx.y*16+threadIdx.y, c=blockIdx.x*16+threadIdx.x; float acc=0;
  for(int k0=0;k0<K;k0+=16){
    As[threadIdx.y][threadIdx.x]=bf2f(A[(size_t)r*K+k0+threadIdx.x]);
    Bs[threadIdx.y][threadIdx.x]=bf2f(B[(size_t)(k0+threadIdx.y)*N+c]);
    __syncthreads();
    for(int k=0;k<16;k++) acc+=As[threadIdx.y][k]*Bs[k][threadIdx.x];
    __syncthreads();
  }
  float v=rbf(acc);
  if(act==1){ float t=0.7978845608028654f*(v+0.044715f*v*v*v); v=0.5f*v*(1.f+tanhf(t)); }
  C[(size_t)r*N+c]=f2bf(v);
}
__global__ void nv_attn(const u16* q,const u16* k,const u16* v,u16* ctx,int S,int H){
  int row=blockIdx.x,h=blockIdx.y; __shared__ float p[2048]; __shared__ float qs[128]; __shared__ float red[128];
  int t=threadIdx.x; qs[t]=bf2f(q[(size_t)row*H*128+h*128+t]); __syncthreads();
  float mx=-1e30f;
  for(int j=t;j<=row;j+=128){ float s=0; const u16* kk=k+(size_t)j*H*128+h*128; for(int d=0;d<128;d++) s+=qs[d]*bf2f(kk[d]);
    s=rbf(rbf(s)*rbf(0.08838834764831845f)); p[j]=s; mx=fmaxf(mx,s);}
  red[t]=mx; __syncthreads(); for(int o=64;o>0;o>>=1){ if(t<o) red[t]=fmaxf(red[t],red[t+o]); __syncthreads(); } mx=red[0]; __syncthreads();
  float sum=0; for(int j=t;j<=row;j+=128){ float e=__expf(p[j]-mx); p[j]=e; sum+=e; }
  red[t]=sum; __syncthreads(); for(int o=64;o>0;o>>=1){ if(t<o) red[t]+=red[t+o]; __syncthreads(); } sum=red[0]; __syncthreads();
  float acc=0; for(int j=0;j<=row;j++) acc+=rbf(p[j]/sum)*bf2f(v[(size_t)j*H*128+h*128+t]);
  ctx[(size_t)row*H*128+h*128+t]=f2bf(acc);
}
__global__ void nv_shift(const u16* h,u16* o,int S,int D){
  int i=blockIdx.x*256+threadIdx.x; if(i>=S*D) return; int r=i/D,c=i%D; int g=c/(D/4); int sr = g==0? r-1 : g==1? r+1 : g==2? r-2 : r+2;
  o[i]=(sr>=0&&sr<S)? h[sr*D+c] : (u16)0;
}
__global__ void nv_add(const u16* a,const u16* b,u16* o,int n){ int i=blockIdx.x*256+threadIdx.x; if(i<n) o[i]=f2bf(bf2f(a[i])+bf2f(b[i])); }
__global__ void nv_final(const u16* x,const u16* s,const u16* b,u16* o,int n){ int i=blockIdx.x*256+threadIdx.x; if(i<n){ float bb=bf2f(b[i]); float g=rbf(bb/(1.f+__expf(-bb))); o[i]=f2bf(bf2f(x[i])+rbf(bf2f(s[i])*g)); } }

struct NvBufs { u16 *xn,*xnn,*q,*k,*v,*ctx,*s,*h,*hs,*t,*x1,*x1n,*h3,*h2,*bo; };
static void naive_launch(void* const* d_in, void* d_out, void* d_ws, hipStream_t st, NvBufs* ob=nullptr){
  const int S=2048,M=640,Dc=512,HD=4096,F=1728;
  const u16 *x=(const u16*)d_in[0],*ckv=(const u16*)d_in[1],*wq=(const u16*)d_in[2],*wuk=(const u16*)d_in[3],*wuv=(const u16*)d_in[4],*wo=(const u16*)d_in[5];
  const u16 *w1=(const u16*)d_in[7],*w2=(const u16*)d_in[8],*w3=(const u16*)d_in[9],*w4=(const u16*)d_in[10];
  u16* w=(u16*)d_ws; NvBufs b;
  b.xn=w; w+=S*M; b.xnn=w; w+=S*M; b.q=w; w+=S*HD; b.k=w; w+=S*HD; b.v=w; w+=S*HD; b.ctx=w; w+=S*HD; b.s=w; w+=S*M; b.h=w; w+=S*M; b.hs=w; w+=S*M; b.t=w; w+=S*M; b.x1=w; w+=S*M; b.x1n=w; w+=S*M; b.h3=w; w+=S*F; b.h2=w; w+=S*M; b.bo=w; w+=S*M;
  dim3 tb(16,16);
  nv_ln<<<S,256,0,st>>>(x,b.xn,M); nv_ln<<<S,256,0,st>>>(b.xn,b.xnn,M);
  nv_gemm<<<dim3(HD/16,S/16),tb,0,st>>>(b.xn,wq,b.q,S,HD,M,0);
  nv_gemm<<<dim3(HD/16,S/16),tb,0,st>>>(ckv,wuk,b.k,S,HD,Dc,0);
  nv_gemm<<<dim3(HD/16,S/16),tb,0,st>>>(ckv,wuv,b.v,S,HD,Dc,0);
  nv_attn<<<dim3(S,32),128,0,st>>>(b.q,b.k,b.v,b.ctx,S,32);
  nv_gemm<<<dim3(M/16,S/16),tb,0,st>>>(b.ctx,wo,b.s,S,M,HD,0);
  nv_gemm<<<dim3(M/16,S/16),tb,0,st>>>(b.xnn,w1,b.h,S,M,M,1);
  nv_shift<<<S*M/256,256,0,st>>>(b.h,b.hs,S,M);
  nv_gemm<<<dim3(M/16,S/16),tb,0,st>>>(b.hs,w2,b.t,S,M,M,0);
  nv_add<<<S*M/256,256,0,st>>>(b.xn,b.t,b.x1,S*M);
  nv_ln<<<S,256,0,st>>>(b.x1,b.x1n,M);
  nv_gemm<<<dim3(F/16,S/16),tb,0,st>>>(b.x1n,w3,b.h3,S,F,M,1);
  nv_gemm<<<dim3(M/16,S/16),tb,0,st>>>(b.h3,w4,b.h2,S,M,F,0);
  nv_add<<<S*M/256,256,0,st>>>(b.x1,b.h2,b.bo,S*M);
  nv_final<<<S*M/256,256,0,st>>>(x,b.s,b.bo,(u16*)d_out,S*M);
  if(ob) *ob=b;
}
extern "C" void kernel_launch(void* const* d_in, const int* in_sizes, int n_in, void* d_out, int out_size, void* d_ws, size_t ws_size, hipStream_t stream){
  naive_launch(d_in,d_out,d_ws,stream);
}
```

```cpp
#include <hip/hip_runtime.h>
#include <stdint.h>
constexpr size_t WS_KV=0, WS_KV_SZ=33554432, WS_CTX=33554432, WS_CTX_SZ=16777216, WS_XN=50331648, WS_XN_SZ=2621440, WS_XNN=52953088, WS_XNN_SZ=2621440, WS_H=55574528, WS_H_SZ=2621440, WS_X1=58195968, WS_X1_SZ=2621440, WS_H3=60817408, WS_H3_SZ=7340032, WS_W1=68157440, WS_W1_SZ=819200, WS_W2=68976640, WS_W2_SZ=819200, WS_W3=69795840, WS_W3_SZ=2293760, WS_W4=72089600, WS_W4_SZ=2293760, WS_WO=74383360, WS_WO_SZ=5242880, WS_ST=79626240, WS_ST_SZ=131072, WS_PART=79757312, WS_PART_SZ=20971520, WS_FLAG=100728832, WS_FLAG_SZ=36864, WS_DBG=100765696, WS_DBG_SZ=2097152;

__global__ __launch_bounds__(256,1) void k_mega(char* ws, const char* x, const char* ckv, const char* wq, const char* wuk, const char* wuv, char* out,
    const char* w1, const char* w2, const char* w3, const char* w4, const char* wo){
  extern __shared__ char lds[];
  const int wid=__builtin_amdgcn_readfirstlane(threadIdx.x>>6);
  asm volatile("s_waitcnt lgkmcnt(0)\n"
".macro QA x0,x1,x2\nv_mfma_f32_16x16x32_bf16 v[\\x0:\\x0+3], a[\\x1:\\x1+3], a[\\x2:\\x2+3], v[\\x0:\\x0+3]\n.endm\n.macro QB x0,x1,x2\nds_read_b128 a[\\x0:\\x0+3], v[\\x1] offset:\\x2\n.endm\n"
".macro QC x0,x1,x2\nv_mfma_f32_16x16x32_bf16 v[\\x0:\\x0+3], a[\\x1:\\x1+3], v[\\x2:\\x2+3], v[\\x0:\\x0+3]\n.endm\n.macro QD x0,x1,x2\n"
"v_mfma_f32_16x16x32_bf16 v[\\x0:\\x0+3], v[\\x1:\\x1+3], a[\\x2:\\x2+3], v[\\x0:\\x0+3]\n.endm\n.macro QE x0,x1,x2\nds_read_b64_tr_b16 a[\\x0:\\x0+1], v[\\x1] offset:\\x2\n.endm\n.macro QF x0,x1,x2\n"
"v_cvt_pk_bf16_f32 v[\\x0], v[\\x1], v[\\x2]\n.endm\n.macro QG x0\ns_waitcnt lgkmcnt(\\x0)\n.endm\n.macro QH x0,x1,x2\nds_read_b128 v[\\x0:\\x0+3], v[\\x1] offset:\\x2\n.endm\n.macro QI x0,x1\n"
"v_accvgpr_read_b32 v[\\x0], a[\\x1]\n.endm\n.macro QJ x0,x1,x2,x3,x4\nbuffer_load_dwordx4 a[\\x0:\\x0+3], v[\\x2], s[\\x1:\\x1+3], s[\\x3] offen offset:\\x4 sc1\n.endm\n.macro QK x0,x1\n"
"v_mul_f32 v[\\x0], s[\\x1], v[\\x0]\n.endm\n.macro QL x0,x1,x2,x3\nbuffer_load_dwordx4 v[\\x1], s[\\x0:\\x0+3], s[\\x2] offen offset:\\x3 sc1 lds\n.endm\n.macro QM x0,x1\n"
"v_mul_f32 v[\\x0], v[\\x0], v[\\x1]\n.endm\n.macro QN x0,x1\nv_mov_b32 v[\\x0], v[\\x1]\n.endm\n.macro QO x0,x1\nv_accvgpr_write_b32 a[\\x0], v[\\x1]\n.endm\n.macro QP x0,x1\n"
"v_exp_f32 v[\\x0], v[\\x1]\n.endm\n.macro QR x0,x1,x2\nv_mfma_f32_16x16x32_bf16 a[\\x0:\\x0+3], a[\\x1:\\x1+3], v[\\x2:\\x2+3], a[\\x0:\\x0+3]\n.endm\n.macro QS x0,x1,x2,x3\n"
"buffer_load_dwordx4 v[\\x1], s[\\x0:\\x0+3], s[\\x2] offen offset:\\x3 lds\n.endm\n.macro QT x0,x1\nv_sub_f32 v[\\x0], v[\\x0], v[\\x1]\n.endm\n.macro QU x0,x1\nv_lshlrev_b32 v[\\x0], 16, v[\\x1]\n"
".endm\n.macro QV x0\nv_rcp_f32 v[\\x0], v[\\x0]\n.endm\n.macro QW x0\nv_exp_f32 v[\\x0], v[\\x0]\n.endm\n.macro QX x0,x1\nv_pk_mul_f32 v[\\x0:\\x0+1], v[\\x0:\\x0+1], v[\\x1:\\x1+1]\n.endm\n"
".macro QY x0,x1\nv_and_b32 v[\\x0], 0xffff0000, v[\\x1]\n.endm\n.macro QZ x0,x1\ns_add_u32 s[\\x0], s[\\x0], s[\\x1]\n.endm\n.macro QAA x0\ns_add_u32 s[\\x0], s[\\x0], 4096\n.endm\n"
".macro QAB x0,x1,x2,x3,x4\nbuffer_store_dwordx4 v[\\x0:\\x0+3], v[\\x2], s[\\x1:\\x1+3], s[\\x3] offen offset:\\x4 sc1\n.endm\n.macro QAC x0,x1\n"
"v_pk_mul_f32 v[\\x0:\\x0+1], v[\\x0:\\x0+1], v[\\x1:\\x1+1] op_sel_hi:[1,0]\n.endm\n.macro QAD x0\nv_accvgpr_write_b32 a[\\x0], 0\n.endm\n.macro QAE x0,x1,x2\n"
"v_mfma_f32_16x16x32_bf16 v[\\x0:\\x0+3], a[\\x1:\\x1+3], a[\\x2:\\x2+3], 0\n.endm\n.macro QAF x0,x1,x2\nv_pk_fma_f32 v[\\x0:\\x0+1], v[\\x1:\\x1+1], v[\\x2:\\x2+1], v[\\x0:\\x0+1] op_sel_hi:[1,0,1]\n"
".endm\n.macro QAG x0\nv_mov_b32 v[\\x0], 0\n.endm\n.macro QAH x0\ns_waitcnt vmcnt(\\x0)\n.endm\n.macro QAI x0,x1\nv_pk_add_f32 v[\\x0:\\x0+1], v[\\x0:\\x0+1], v[\\x1:\\x1+1]\n.endm\n.macro QAJ x0,x1\n"
"v_add_f32 v[\\x0], v[\\x0], v[\\x1]\n.endm\n.macro QAK x0\nv_pk_add_f32 v[\\x0:\\x0+1], v[\\x0:\\x0+1], 1.0 op_sel_hi:[1,0]\n.endm\n.macro QAL x0,x1,x2\n"
"v_cndmask_b32 v[\\x1], v[\\x1], v[\\x2], s[\\x0:\\x0+1]\n.endm\n.macro QAM x0,x1\ns_waitcnt vmcnt(\\x0) lgkmcnt(\\x1)\n.endm\n.macro QAN x0,x1,x2\n"
"v_mfma_f32_16x16x32_bf16 v[\\x0:\\x0+3], a[\\x1:\\x1+3], v[\\x2:\\x2+3], 0\n.endm\n.macro QAO x0\ns_add_u32 s[\\x0], s[\\x0], 262144\n.endm\n.macro QAP x0,x1\n"
"v_pk_mul_f32 v[\\x0:\\x0+1], v[\\x1:\\x1+1], v[\\x1:\\x1+1]\n.endm\n.macro QAR x0,x1,x2,x3\nv_mfma_f32_16x16x32_bf16 v[\\x0:\\x0+3], a[\\x1:\\x1+3], v[\\x2:\\x2+3], v[\\x3:\\x3+3]\n.endm\n"
".macro QAS x0,x1,x2\nv_mfma_f32_16x16x32_bf16 v[\\x0:\\x0+3], v[\\x1:\\x1+3], a[\\x2:\\x2+3], 0\n.endm\n.macro QAT x0,x1,x2,x3\nv_cndmask_b32 v[\\x1], v[\\x2], v[\\x3], s[\\x0:\\x0+1]\n.endm\n"
".macro QAU x0,x1\nv_pk_mul_f32 v[\\x0:\\x0+1], v[\\x0:\\x0+1], v[\\x1:\\x1+1] op_sel:[0,1] op_sel_hi:[1,1]\n.endm\n.macro QAV x0,x1\n"
"v_pk_fma_f32 v[\\x0:\\x0+1], v[\\x0:\\x0+1], v[\\x1:\\x1+1], v[\\x1:\\x1+1] op_sel:[0,0,1] op_sel_hi:[1,0,1]\n.endm\n.macro QAW x0,x1,x2,x3,x4\n"
"buffer_load_dwordx4 v[\\x0:\\x0+3], v[\\x2], s[\\x1:\\x1+3], s[\\x3] offen offset:\\x4 sc1\n.endm\n.macro QAX x0\ns_addc_u32 s[\\x0], s[\\x0], 0\n.endm\n.macro QAY x0,x1\n"
"v_cvt_pk_bf16_f32 v[\\x0], v[\\x0], v[\\x1]\n.endm\n.macro QAZ x0,x1\nv_max_f32 v[\\x0], v[\\x0], v[\\x1]\n.endm\n.macro QBA x0\ns_mov_b32 s[\\x0], 0x20000\n.endm\n.macro QBB x0,x1,x2\n"
"buffer_load_dwordx4 v[\\x1], s[\\x0:\\x0+3], s[\\x2] offen lds\n.endm\n.macro QBC x0,x1\nv_mul_f32 v[\\x0], 0xbfb8aa3b, v[\\x1]\n.endm\n.macro QBD \ns_nop 1\n.endm\n.macro QBE \ns_nop 0\n.endm\n"
".macro QBF x0,x1\nv_pk_fma_f32 v[\\x0:\\x0+1], v[\\x1:\\x1+1], v[\\x1:\\x1+1], v[\\x0:\\x0+1]\n.endm\n.macro QBG x0\nv_and_b32 v[\\x0], 0xffff0000, v[\\x0]\n.endm\n.macro QBH x0\n"
"s_lshl_b32 s[\\x0], s[\\x0], 2\n.endm\n.macro QBI x0,x1\ns_cselect_b32 s[\\x0], s[\\x1], s[\\x0]\n.endm\n.macro QBJ x0,x1\nv_mov_b32 v[\\x0], s[\\x1]\n.endm\n.macro QBK x0,x1,x2\n"
"v_max3_f32 v[\\x0], v[\\x0], v[\\x1], v[\\x2]\n.endm\n.macro QBL x0,x1,x2\nds_write_b128 v[\\x1], a[\\x0:\\x0+3] offset:\\x2\n.endm\n.macro QBM x0,x1,x2\nv_add_u32 v[\\x0], s[\\x1], v[\\x2]\n.endm\n"
".macro QBN x0,x1\nv_permlane16_swap_b32 v[\\x0], v[\\x1]\n.endm\n.macro QBO x0,x1,x2,x3\nv_mfma_f32_16x16x32_bf16 v[\\x0:\\x0+3], v[\\x1:\\x1+3], v[\\x2:\\x2+3], v[\\x3:\\x3+3]\n.endm\n.macro QBP x0\n"
"s_add_u32 s[\\x0], s[\\x0], 16384\n.endm\n.macro QBR x0\ns_and_b32 s[\\x0], s[\\x0], 4194303\n.endm\n.macro QBS x0,x1\nv_permlane32_swap_b32 v[\\x0], v[\\x1]\n.endm\n.macro QBT x0,x1,x2\n"
"s_add_u32 s[\\x0], s[\\x1], s[\\x2]\n.endm\n.macro QBU x0\ns_lshl_b32 s[\\x0], s[\\x0], 7\n.endm\n.macro QBV x0\ns_add_u32 m0, s[\\x0], 0\n.endm\n.macro QBW x0\ns_xor_b32 s[\\x0], s[\\x0], 0x4000\n"
".endm\n.macro QBX x0,x1\ns_and_b32 s[\\x0], s[\\x1], 1\n.endm\n.macro QBY x0,x1,x2,x3\nv_max3_f32 v[\\x0], v[\\x1], v[\\x2], v[\\x3]\n.endm\n.macro QBZ x0\ns_cmp_eq_u32 s[\\x0], 0\n.endm\n"
".macro QCA x0,x1,x2\nv_pk_add_f32 v[\\x0:\\x0+1], v[\\x1:\\x1+1], v[\\x2:\\x2+1] op_sel_hi:[1,0]\n.endm\n.macro QCB x0,x1\nv_pk_add_f32 v[\\x0:\\x0+1], v[\\x0:\\x0+1], v[\\x1:\\x1+1] op_sel_hi:[1,0]\n"
".endm\n.macro QCC x0,x1\nv_cvt_pk_bf16_f32 v[\\x0], v[\\x1], v[\\x0]\n.endm\n.macro QCD x0,x1,x2\nds_write_b64 v[\\x1], v[\\x0:\\x0+1] offset:\\x2\n.endm\n.macro QCE x0\n"
"s_add_u32 s[\\x0], s[\\x0], 1\n.endm\n.macro QCF x0,x1\nv_pk_add_f32 v[\\x0:\\x0+1], v[\\x0:\\x0+1], v[\\x1:\\x1+1] op_sel_hi:[1,0] neg_lo:[0,1] neg_hi:[0,1]\n.endm\n.macro QCG x0\n"
"s_getpc_b64 s[\\x0:\\x0+1]\n.endm\n.macro QCH x0,x1,x2\nds_write_b128 v[\\x1], v[\\x0:\\x0+3] offset:\\x2\n.endm\n.macro QCI x0\ns_mov_b32 m0, s[\\x0]\n.endm\n.macro QCJ x0,x1\n"
"s_lshr_b32 s[\\x0], s[\\x1], 1\n.endm\n.macro QCK x0\ns_lshl_b32 s[\\x0], s[\\x0], 3\n.endm\n.macro QCL x0,x1\nv_lshrrev_b32 v[\\x0], 1, v[\\x1]\n.endm\n.macro QCM x0\n"
"v_cvt_pk_bf16_f32 v[\\x0], v[\\x0], v[\\x0]\n.endm\n.macro QCN x0\nv_mov_b32 v[\\x0], 0x3f803f80\n.endm\n.macro QCO x0,x1,x2\nv_readlane_b32 s[\\x0], v[\\x1], s[\\x2]\n.endm\n.macro QCP x0,x1,x2\n"
"v_mfma_f32_32x32x16_bf16 a[\\x0:\\x0+15], v[\\x1:\\x1+3], v[\\x2:\\x2+3], 0\n.endm\n.macro QCR x0\nv_mov_b32 v[\\x0], 1\n.endm\n.macro QCS x0\ns_sub_u32 s[\\x0], s[\\x0], 1\n.endm\n.macro QCT x0\n"
"s_add_u32 m0, s[\\x0], 4096\n.endm\n.macro QCU x0,x1\ns_add_u32 s[\\x0], s[\\x1], 98304\n.endm\n.macro QCV x0,x1\nv_and_b32 v[\\x0], 0xf0, v[\\x1]\n.endm\n.macro QCW x0\n"
"v_lshlrev_b32 v[\\x0], 3, v[\\x0]\n.endm\n.macro QCX x0\ns_add_u32 s[\\x0], s[\\x0], 128\n.endm\n.macro QCY x0\ns_and_b32 s[\\x0], s[\\x0], 1023\n.endm\n.macro QCZ x0,x1,x2\n"
"buffer_load_dword v[\\x1], off, s[\\x0:\\x0+3], s[\\x2] sc1\n.endm\n.macro QDA x0\nv_lshrrev_b32 v[\\x0], 16, v[\\x0]\n.endm\n.macro QDB x0\ns_mov_b32 s[\\x0], 0x40000\n.endm\n.macro QDC x0\n"
"s_mov_b32 s[\\x0], 0\n.endm\n.macro QDD x0\ns_add_u32 m0, s[\\x0], 122880\n.endm\n.macro QDE x0,x1\ns_add_u32 s[\\x0], s[\\x1], 65536\n.endm\n.macro QDF x0\ns_add_u32 m0, s[\\x0], 32768\n.endm\n"
".macro QDG x0,x1\nv_cndmask_b32 v[\\x0], v[\\x0], v[\\x1], vcc\n.endm\n.macro QDH x0\nv_add_u32 v[\\x0], 32768, v[\\x0]\n.endm\n.macro QDI x0,x1,x2,x3\n"
"buffer_load_dwordx2 v[\\x0:\\x0+1], v[\\x2], s[\\x1:\\x1+3], s[\\x3] offen sc1\n.endm\n.macro QDJ x0\ns_add_u32 s[\\x0], s[\\x0], 0x8000\n.endm\n.macro QDK x0\ns_sub_u32 s[\\x0], s[\\x0], 0x8000\n"
".endm\n.macro QDL x0\ns_mov_b32 s[\\x0], 2621440\n.endm\n.macro QDM x0,x1\nv_add_u32 v[\\x0], v[\\x0], v[\\x1]\n.endm\n.macro QDN x0\ns_add_u32 m0, s[\\x0], 106496\n.endm\n.macro QDO \ns_nop 7\n"
".endm\n.macro QDP \ns_sleep 3\n.endm\n.macro QDR \ns_nop 4\n.endm\n.macro QDS x0,x1\nv_lshrrev_b32 v[\\x0], 5, v[\\x1]\n.endm\n.macro QDT x0\nv_lshlrev_b32 v[\\x0], 4, v[\\x0]\n.endm\n"
".macro QDU x0,x1\nv_lshrrev_b32 v[\\x0], 4, v[\\x1]\n.endm\n.macro QDV x0\ns_add_u32 m0, s[\\x0], 8192\n.endm\n.macro QDW x0\ns_add_u32 m0, s[\\x0], 16384\n.endm\n.macro QDX x0,x1,x2\n"
"buffer_store_dword v[\\x1], off, s[\\x0:\\x0+3], s[\\x2] sc1\n.endm\n.macro QDY x0\ns_add_u32 m0, s[\\x0], 65536\n.endm\n.macro QDZ x0,x1,x2\nv_mul_u32_u24 v[\\x0], v[\\x1], v[\\x2]\n.endm\n"
".macro QEA x0,x1\ns_add_u32 s[\\x0], s[\\x1], 32768\n.endm\n.macro QEB x0,x1,x2,x3\nbuffer_load_dword v[\\x1], v[\\x2], s[\\x0:\\x0+3], s[\\x3] offen sc1\n.endm\n.macro QEC x0\n"
"s_add_u32 s[\\x0], s[\\x0], 131072\n.endm\n.macro QED x0,x1,x2\nv_mad_u32_u24 v[\\x0], v[\\x1], s[\\x2], v[\\x0]\n.endm\n.macro QEE x0\nv_xor_b32 v[\\x0], 0x4000, v[\\x0]\n.endm\n.macro QEF x0,x1\n"
"v_sub_f32 v[\\x0], 0, v[\\x1]\n.endm\n.macro QEG x0,x1\ns_lshl_b32 s[\\x0], s[\\x1], 2\n.endm\n.macro QEH x0,x1\nv_and_b32 v[\\x0], 15, v[\\x1]\n.endm\n.macro QEI \ns_mov_b64 exec, 1\n.endm\n"
".macro QEJ x0,x1,x2\nbuffer_atomic_add v[\\x1], off, s[\\x0:\\x0+3], s[\\x2] sc1\n.endm\n.macro QEK \ns_mov_b64 exec, -1\n.endm\n.macro QEL x0,x1\ns_add_u32 s[\\x0], s[\\x1], 40960\n.endm\n"
".macro QEM x0\ns_add_u32 m0, s[\\x0], 24576\n.endm\n.macro QEN x0\ns_add_u32 m0, s[\\x0], 40960\n.endm\n.macro QEO x0,x1\nv_cmp_nge_f32 vcc, s[\\x0], v[\\x1]\n.endm\n.macro QEP x0\n"
"s_add_u32 s[\\x0], s[\\x0], 14336\n.endm\n.macro QER x0\ns_lshl_b32 s[\\x0], s[\\x0], 8\n.endm\n.macro QES x0,x1\nv_lshrrev_b32 v[\\x0], 2, v[\\x1]\n.endm\n.macro QET x0,x1\n"
"v_xor_b32 v[\\x0], v[\\x0], v[\\x1]\n.endm\n.macro QEU x0,x1\nv_add_u32 v[\\x0], 0, v[\\x1]\n.endm\n.macro QEV x0\nv_lshrrev_b32 v[\\x0], 1, v[\\x0]\n.endm\n.macro QEW x0,x1,x2\n"
"v_add_f32 v[\\x0], v[\\x1], v[\\x2]\n.endm\n.macro QEX x0\ns_add_u32 m0, s[\\x0], 20480\n.endm\n.macro QEY x0,x1,x2\nds_read_b64_tr_b16 v[\\x0:\\x0+1], v[\\x1] offset:\\x2\n.endm\n.macro QEZ x0,x1\n"
"s_add_u32 s[\\x0], s[\\x1], 0\n.endm\n.macro QFA x0\nv_mov_b32 v[\\x0], 5\n.endm\n.macro QFB x0\nv_mov_b32 v[\\x0], 10\n.endm\n.macro QFC x0,x1\ns_add_u32 s[\\x0], s[\\x1], 8192\n.endm\n"
".macro QFD x0,x1,x2\nv_sub_u32 v[\\x0], v[\\x1], v[\\x2]\n.endm\n.macro QFE x0,x1\ns_lshl_b32 s[\\x0], s[\\x1], 3\n.endm\n.macro QFF x0\nv_cmp_ne_u32 vcc, 16, v[\\x0]\n.endm\n.macro QFG x0,x1,x2\n"
"global_load_dwordx2 v[\\x0:\\x0+1], v[\\x2], s[\\x1:\\x1+1]\n.endm\n.macro QFH x0,x1\ns_add_u32 s[\\x0], s[\\x1], 73728\n.endm\n.macro QFI x0\ns_add_u32 m0, s[\\x0], 147456\n.endm\n.macro QFJ x0\n"
"s_add_u32 m0, s[\\x0], 81920\n.endm\n.macro QFK x0,x1\ns_add_u32 s[\\x0], s[\\x1], 106496\n.endm\n.macro QFL x0\ns_add_u32 m0, s[\\x0], 61440\n.endm\n.macro QFM x0,x1,x2,x3\n"
"buffer_store_dwordx2 v[\\x0:\\x0+1], v[\\x2], s[\\x1:\\x1+3], s[\\x3] offen sc1\n.endm\n.macro QFN x0\nv_add_u32 v[\\x0], 0x10000, v[\\x0]\n.endm\n.macro QFO x0,x1\n"
"v_lshl_add_u32 v[\\x0], v[\\x0], 4, v[\\x1]\n.endm\n.macro QFP x0\nv_bfe_u32 v[\\x0], v[\\x0], 4, 12\n.endm\n.macro QFR x0,x1,x2\nbuffer_load_dword a[\\x1], off, s[\\x0:\\x0+3], s[\\x2]\n.endm\n"
".macro QFS x0,x1,x2\nv_mfma_f32_16x16x32_bf16 v[\\x0:\\x0+3], v[\\x1:\\x1+3], v[\\x2:\\x2+3], 0\n.endm\n.macro QFT x0,x1\nv_max3_f32 v[\\x0], v[\\x0], v[\\x1], 0\n.endm\n.macro QFU x0,x1\n"
"v_exp_f32 v[\\x0], -v[\\x1]\n.endm\n.macro QFV x0,x1,x2\nv_sub_f32 v[\\x0], v[\\x1], v[\\x2]\n.endm\n.macro QFW x0,x1,x2,x3,x4\n"
"buffer_load_dwordx2 v[\\x0:\\x0+1], v[\\x2], s[\\x1:\\x1+3], s[\\x3] offen offset:\\x4 sc1\n.endm\n.macro QFX x0,x1\nv_lshl_add_u32 v[\\x0], v[\\x1], 3, v[\\x0]\n.endm\n.macro QFY x0\n"
"s_cmp_lt_u32 s[\\x0], 2\n.endm\n.macro QFZ x0,x1\ns_lshl_b32 s[\\x0], s[\\x1], 11\n.endm\n.macro QGA x0\ns_lshl_b32 s[\\x0], s[\\x0], 14\n.endm\n.macro QGB x0\ns_andn2_b32 s[\\x0], s[\\x0], 1\n.endm\n"
".macro QGC x0\ns_addc_u32 s[\\x0], s[\\x0], 4\n.endm\n.macro QGD x0,x1,x2\nglobal_load_dword v[\\x1], v[\\x2], s[\\x0:\\x0+1]\n.endm\n.macro QGE x0,x1\ns_mul_i32 s[\\x0], s[\\x1], 20480\n.endm\n"
".macro QGF x0\ns_add_u32 m0, s[\\x0], 102400\n.endm\n.macro QGG x0\ns_add_u32 m0, s[\\x0], 126976\n.endm\n.macro QGH x0\ns_add_u32 m0, s[\\x0], 86016\n.endm\n.macro QGI x0,x1\n"
"s_lshl_b32 s[\\x0], s[\\x1], 12\n.endm\n.macro QGJ x0,x1\nv_xor_b32 v[\\x0], 64, v[\\x1]\n.endm\n.macro QGK x0\ns_mov_b32 s[\\x0], 32768\n.endm\n.macro QGL x0,x1\nv_add_u32 v[\\x0], 131072, v[\\x1]\n"
".endm\n.macro QGM x0\ns_lshl_b32 s[\\x0], s[\\x0], 10\n.endm\n.macro QGN x0\ns_add_u32 s[\\x0], s[\\x0], 50331648\n.endm\n.macro QGO x0,x1\ns_add_u32 s[\\x0], s[\\x1], 131072\n.endm\n"
".macro QGP x0,x1,x2\nv_pk_add_f32 v[\\x0:\\x0+1], v[\\x1:\\x1+1], v[\\x2:\\x2+1]\n.endm\n.macro QGR x0\ns_mov_b32 s[\\x0], 5242880\n.endm\n.macro QGS x0,x1\nv_readfirstlane_b32 s[\\x0], v[\\x1]\n"
".endm\n.macro QGT x0,x1\nglobal_load_lds_dwordx4 v[\\x1], s[\\x0:\\x0+1]\n.endm\n.macro QGU x0,x1\nv_and_b32 v[\\x0], 16, v[\\x1]\n.endm\n.macro QGV x0\nv_mov_b32 v[\\x0], 1280\n.endm\n.macro QGW x0\n"
"v_mov_b32 v[\\x0], 6554\n.endm\n.macro QGX x0\nv_mov_b32 v[\\x0], 13108\n.endm\n.macro QGY x0,x1\ns_sub_u32 s[\\x0], 15, s[\\x1]\n.endm\n.macro QGZ x0\ns_add_u32 m0, s[\\x0], 143360\n.endm\n"
".macro QHA x0,x1\ns_lshl_b32 s[\\x0], 1, s[\\x1]\n.endm\n.macro QHB x0,x1\ns_add_u32 s[\\x0], s[\\x1], 14336\n.endm\n.macro QHC x0,x1\ns_add_u32 s[\\x0], s[\\x1], 28672\n.endm\n.macro QHD x0\n"
"s_cmp_ge_u32 s[\\x0], 15\n.endm\n.macro QHE x0\ns_cmp_ge_u32 s[\\x0], 16\n.endm\n.macro QHF x0\ns_cselect_b32 s[\\x0], 0x3c000, s[\\x0]\n.endm\n.macro QHG x0\ns_add_u32 s[\\x0], s[\\x0], 6144\n.endm\n"
".macro QHH x0\nv_cmp_ne_u32 vcc, 4161009664, v[\\x0]\n.endm\nv_mbcnt_lo_u32_b32 v3, -1, 0\nv_mbcnt_hi_u32_b32 v3, -1, v3\nv_lshlrev_b32 v241, 4, v3\nQCG 50\nL_pc1:\ns_lshr_b32 s52, %1, 3\nQBH 52\n"
"s_add_u32 s52, s52, %0\ns_mul_i32 s52, s52, ((L_roleS-L_st-1024)/127+63)&-64\ns_min_u32 s52, s52, L_roleS-L_st-1024\ns_add_u32 s52, s52, L_st-L_pc1\nQZ 50,52\nQAX 51\n"
"global_load_dwordx4 v[250:253], v241, s[50:51]\nL_st:\ns_mov_b32 s44, %0\ns_and_b32 s75, %1, 7\ns_lshr_b32 s76, %1, 3\ns_and_b32 s46, s76, 7\ns_lshr_b32 s76, s76, 3\nQEG 45,75\nQZ 45,76\n"
"QBT 47,76,46\ns_and_b32 s47, s47, 3\ns_mul_i32 s47, s47, 2\ns_mov_b64 s[36:37], %3\nQDL 38\nQBA 39\ns_and_b32 s48, %1, 7\nQER 48\ns_andn2_b32 s49, %1, 7\nQZ 48,49\ns_lshl_b32 s49, s44, 1\nQZ 48,49\n"
"s_mul_i32 s49, s48, 1280\nQDS 0,3\nv_and_b32 v1, 31, v3\nv_mul_u32_u24 v248, 1280, v0\nQFX 248,1\nbuffer_load_dwordx2 v[96:97], v248, s[36:39], s49 offen offset:0\n"
"buffer_load_dwordx2 v[98:99], v248, s[36:39], s49 offen offset:256\nbuffer_load_dwordx2 v[100:101], v248, s[36:39], s49 offen offset:512\n"
"buffer_load_dwordx2 v[102:103], v248, s[36:39], s49 offen offset:768\nbuffer_load_dwordx2 v[104:105], v248, s[36:39], s49 offen offset:1024\nQFY 44\ns_cselect_b64 s[56:57], %6, %7\n"
"s_cselect_b32 s78, 0x3e0293ee, 1.0\ns_lshl_b32 s75, s45, 8\nQBX 76,44\nQBU 76\nQZ 75,76\nQZ 56,75\nQAX 57\ns_sub_u32 s58, 4194304, s75\nQBA 59\ns_lshl_b32 s68, s47, 19\ns_mul_i32 s69, s44, 24576\n"
"v_lshrrev_b32 v0, 3, v3\nQES 1,0\nQCW 1\nv_and_b32 v2, 3, v0\nQDM 1,2\nv_lshlrev_b32 v1, 13, v1\nQCL 2,0\nv_lshlrev_b32 v2, 5, v2\nv_and_b32 v4, 0x70, v241\nQET 2,4\nQDM 1,2\nQEU 232,1\n"
"v_add_u32 v233, 130048, v1\nv_add_u32 v234, 30720, v1\nv_add_u32 v235, 160768, v1\nQBV 69\nQBE \nQS 56,232,68,0\nQS 56,233,68,1024\nQS 56,234,68,2048\nQS 56,235,68,3072\nQAO 68\nQBR 68\nQCT 69\nQBE \n"
"QS 56,232,68,0\nQS 56,233,68,1024\nQS 56,234,68,2048\nQS 56,235,68,3072\nQAO 68\nQBR 68\ns_mov_b64 s[60:61], %4\ns_lshl_b32 s75, s46, 18\nQZ 60,75\nQAX 61\ns_mov_b32 s62, 262144\nQBA 63\n"
"s_lshl_b32 s70, s47, 7\ns_lshl_b32 s71, s44, 13\nQFZ 79,44\ns_lshl_b32 s72, s44, 14\nQCL 2,0\nQDT 2\nQET 2,4\nv_lshl_add_u32 v2, v0, 10, v2\nv_xor_b32 v236, 0, v2\nv_add_u32 v236, 0, v236\nQGJ 237,2\n"
"v_add_u32 v237, 7168, v237\nv_xor_b32 v238, 0, v2\nv_add_u32 v238, 14336, v238\nQGJ 239,2\nv_add_u32 v239, 21504, v239\ns_add_u32 m0, s79, 98304\nQBT 75,72,70\nQS 60,236,75,0\nQS 60,237,75,1024\n"
"QCX 70\nQCY 70\ns_mov_b64 s[92:93], %2\ns_add_u32 s92, s92, 100728832\nQAX 93\ns_mov_b32 s94, 36864\nQBA 95\ns_cmp_eq_u32 %0, 0\ns_cselect_b32 s96, 0, 1048576\ns_or_b32 s97, s96, %1\nQBZ 97\n"
"s_cselect_b32 s97, 0, 1048576\ns_add_u32 s97, s97, 32768\nQGK 99\nQCZ 92,126,99\ns_cmp_lg_u32 s97, 32768\ns_cbranch_scc1 L_nolead\nQAG 6\nv_lshlrev_b32 v7, 3, v241\nQDC 99\n"
"buffer_store_dword v6, v7, s[92:95], s99 offen sc1\ns_mov_b32 s99, 8192\nbuffer_store_dword v6, v7, s[92:95], s99 offen sc1\ns_mov_b32 s99, 16384\nbuffer_store_dword v6, v7, s[92:95], s99 offen sc1\n"
"s_mov_b32 s99, 24576\nbuffer_store_dword v6, v7, s[92:95], s99 offen sc1\nL_nolead:\nQCZ 92,250,99\ns_lshr_b32 s49, s48, 4\ns_mul_i32 s49, s49, 20480\ns_and_b32 s76, s48, 8\ns_lshl_b32 s76, s76, 4\n"
"QZ 49,76\nQBX 76,44\ns_mul_i32 s76, s76, 10240\nQZ 49,76\nQCJ 76,44\ns_mul_i32 s76, s76, 2621440\nQZ 49,76\nQDS 0,3\nv_and_b32 v1, 31, v3\ns_lshl_b32 s76, s44, 1\nQBM 249,76,0\nv_and_b32 v2, 3, v1\n"
"QET 249,2\nv_bfe_u32 v0, v1, 3, 1\nv_lshlrev_b32 v0, 2, v0\nQET 249,0\nQDT 249\nv_lshl_add_u32 v249, v2, 7, v249\nv_lshrrev_b32 v2, 3, v1\nv_lshl_add_u32 v249, v2, 9, v249\nv_bfe_u32 v2, v1, 2, 1\n"
"QFX 249,2\nv_add_u32 v249, 139264, v249\ns_mov_b64 s[64:65], %2\ns_lshl_b32 s75, s45, 20\nQZ 64,75\nQAX 65\ns_mov_b32 s66, 0x100000\nQBA 67\ns_lshl_b32 s74, s46, 17\nQDS 0,3\n"
"v_lshlrev_b32 v0, 10, v0\nv_bfe_u32 v1, v3, 2, 3\nv_lshl_add_u32 v0, v1, 7, v0\nv_and_b32 v2, 3, v3\nQFX 0,2\nQEV 1\nv_lshl_add_u32 v0, v1, 5, v0\nv_add_u32 v0, s69, v0\nv_xor_b32 v224, 0, v0\n"
"v_xor_b32 v225, 32, v0\nQGJ 226,0\nv_xor_b32 v227, 96, v0\nQEH 0,3\nQCL 1,0\nQDU 2,3\nQET 1,2\nQDT 1\nv_lshl_add_u32 v0, v0, 7, v1\nQGJ 1,0\nv_add_u32 v228, 98304, v0\nv_add_u32 v229, 98304, v1\n"
"QGL 230,0\nQGL 231,1\nQBX 76,44\ns_lshl_b32 s76, s76, 12\nQCJ 77,44\nQGA 77\nQZ 76,77\nQBM 240,76,241\ns_lshl_b32 s76, s44, 1\nQZ 76,47\ns_add_u32 s77, s76, 1\ns_and_b32 s76, s76, 7\n"
"s_and_b32 s77, s77, 7\nQBU 76\nQBU 77\ns_cmp_lt_u32 %1, 200\ns_cselect_b32 s42, %1, 0\ns_cselect_b32 s86, 0, 0x40000000\ns_mul_i32 s40, s42, 6554\ns_lshr_b32 s40, s40, 16\ns_mul_i32 s43, s40, 10\n"
"s_sub_u32 s41, s42, s43\nQBH 41\ns_add_u32 s41, s41, %0\ns_mul_i32 s42, s41, 13108\ns_lshr_b32 s42, s42, 16\ns_mul_i32 s42, s42, 95\ns_mul_i32 s43, s40, 5\nQZ 42,43\nQZ 42,41\nQGM 42\nQZ 86,42\n"
"s_add_u32 s87, s86, 18644992\ns_add_u32 s86, s86, 17825792\ns_lshl_b32 s41, s41, 5\ns_mul_i32 s42, s40, 40960\nQZ 41,42\ns_mov_b64 s[80:81], %9\nQZ 80,41\nQAX 81\ns_mul_i32 s42, s40, 13108\n"
"s_lshr_b32 s42, s42, 16\ns_mul_i32 s42, s42, 5\ns_sub_u32 s42, s40, s42\ns_cmp_ge_u32 s42, 2\ns_cselect_b32 s43, 20480, 0\ns_cmp_eq_u32 s42, 4\ns_cselect_b32 s43, -81920, s43\n"
"s_cselect_b32 s42, 5120, 0\nQZ 41,43\ns_mov_b64 s[82:83], %10\nQZ 82,41\nQAX 83\nQDS 118,241\nv_mul_u32_u24 v116, 1280, v118\nv_and_or_b32 v116, v241, 16, v116\nv_and_b32 v118, 16, v118\n"
"v_mad_u32_u24 v117, v118, s42, v116\ns_mov_b64 s[88:89], %2\nQGN 88\nQAX 89\ns_mov_b32 s90, 29294592\nQBA 91\nQGO 84,79\nQCL 118,241\nv_add_u32 v118, s84, v118\nQAH 12\nQDV 69\nQBE \nQS 56,232,68,0\n"
"QS 56,233,68,1024\nQS 56,234,68,2048\nQS 56,235,68,3072\nQAO 68\nQBR 68\nQDN 79\nQBT 75,72,70\nQS 60,236,75,0\nQS 60,237,75,1024\nQCX 70\nQCY 70\ns_add_u32 m0, s69, 12288\nQBE \nQS 56,232,68,0\n"
"QS 56,233,68,1024\nQS 56,234,68,2048\nQS 56,235,68,3072\nQAO 68\nQBR 68\nQU 64,96\nQY 65,96\nQU 66,97\nQY 67,97\nQU 68,98\nQY 69,98\nQU 70,99\nQY 71,99\nQU 72,100\nQY 73,100\nQU 74,101\nQY 75,101\n"
"QU 76,102\nQY 77,102\nQU 78,103\nQY 79,103\nQU 80,104\nQY 81,104\nQU 82,105\nQY 83,105\nQGP 242,64,66\nQGP 244,68,70\nQAI 242,72\nQAI 244,74\nQAI 242,76\nQAI 244,78\nQAI 242,80\nQAI 244,82\n"
"QAI 242,244\nQAJ 242,243\nQBD \nv_add_f32_dpp v242, v242, v242 row_ror:8 row_mask:0xf bank_mask:0xf\nQBD \nv_add_f32_dpp v242, v242, v242 row_ror:4 row_mask:0xf bank_mask:0xf\nQBD \n"
"v_add_f32_dpp v242, v242, v242 row_ror:2 row_mask:0xf bank_mask:0xf\nQBD \nv_add_f32_dpp v242, v242, v242 row_ror:1 row_mask:0xf bank_mask:0xf\nQN 244,242\nQBD \nQBN 242,244\nQAJ 242,244\n"
"v_mul_f32 v242, 0xbacccccd, v242\nQCA 8,64,242\nQCA 10,66,242\nQCA 12,68,242\nQCA 14,70,242\nQCA 16,72,242\nQCA 18,74,242\nQCA 20,76,242\nQCA 22,78,242\nQCA 24,80,242\nQCA 26,82,242\nQAP 244,8\n"
"QBF 244,10\nQBF 244,12\nQBF 244,14\nQBF 244,16\nQBF 244,18\nQBF 244,20\nQBF 244,22\nQBF 244,24\nQBF 244,26\nQEW 243,244,245\nQCM 242\nQBG 242\nQCB 64,242\nQCB 66,242\n"
"v_add_f32_dpp v243, v243, v243 row_ror:8 row_mask:0xf bank_mask:0xf\nQCB 68,242\nQCB 70,242\nv_add_f32_dpp v243, v243, v243 row_ror:4 row_mask:0xf bank_mask:0xf\nQCB 72,242\nQCB 74,242\n"
"v_add_f32_dpp v243, v243, v243 row_ror:2 row_mask:0xf bank_mask:0xf\nQCB 76,242\nQCB 78,242\nv_add_f32_dpp v243, v243, v243 row_ror:1 row_mask:0xf bank_mask:0xf\nQCB 80,242\nQCB 82,242\nQN 244,243\n"
"QCC 65,64\nQCC 67,66\nQBN 243,244\nQCC 69,68\nQCC 71,70\nQAJ 243,244\nQCC 73,72\nQCC 75,74\nv_mul_f32 v243, 0x3acccccd, v243\nQCC 77,76\nQCC 79,78\nQCM 243\nQCC 81,80\nQCC 83,82\nQBG 243\nQU 64,65\n"
"QBG 65\nv_add_f32 v243, 0x3727c5ac, v243\nQU 66,67\nQBG 67\nQCM 243\nQU 68,69\nQBG 69\nQBG 243\nQU 70,71\nQBG 71\nv_sqrt_f32 v243, v243\nQU 72,73\nQBG 73\nQCM 243\nQU 74,75\nQBG 75\nQBG 243\n"
"QU 76,77\nQBG 77\nQV 243\nQU 78,79\nQBG 79\nQU 80,81\nQBG 81\nQU 82,83\nQBG 83\nQAU 64,242\nQAU 66,242\nQAU 68,242\nQAU 70,242\nQAU 72,242\nQAU 74,242\nQAU 76,242\nQAU 78,242\nQAU 80,242\nQAU 82,242\n"
"QF 106,64,65\nQF 107,66,67\nQF 108,68,69\nQF 109,70,71\nQF 110,72,73\nQF 111,74,75\nQF 112,76,77\nQF 113,78,79\nQF 114,80,81\nQF 115,82,83\nQU 64,106\nQY 65,106\nQU 66,107\nQY 67,107\nQU 68,108\n"
"QY 69,108\nQU 70,109\nQY 71,109\nQU 72,110\nQY 73,110\nQU 74,111\nQY 75,111\nQU 76,112\nQY 77,112\nQU 78,113\nQY 79,113\nQU 80,114\nQY 81,114\nQU 82,115\nQY 83,115\nQGP 242,64,66\nQGP 244,68,70\n"
"QAI 242,72\nQAI 244,74\nQAI 242,76\nQAI 244,78\nQAI 242,80\nQAI 244,82\nQAI 242,244\nQAJ 242,243\nQBD \nv_add_f32_dpp v242, v242, v242 row_ror:8 row_mask:0xf bank_mask:0xf\nQBD \n"
"v_add_f32_dpp v242, v242, v242 row_ror:4 row_mask:0xf bank_mask:0xf\nQBD \nv_add_f32_dpp v242, v242, v242 row_ror:2 row_mask:0xf bank_mask:0xf\nQBD \n"
"v_add_f32_dpp v242, v242, v242 row_ror:1 row_mask:0xf bank_mask:0xf\nQN 244,242\nQBD \nQBN 242,244\nQAJ 242,244\nv_mul_f32 v242, 0xbacccccd, v242\nQCA 8,64,242\nQCA 10,66,242\nQCA 12,68,242\n"
"QCA 14,70,242\nQCA 16,72,242\nQCA 18,74,242\nQCA 20,76,242\nQCA 22,78,242\nQCA 24,80,242\nQCA 26,82,242\nQAP 244,8\nQBF 244,10\nQBF 244,12\nQBF 244,14\nQBF 244,16\nQBF 244,18\nQBF 244,20\nQBF 244,22\n"
"QBF 244,24\nQBF 244,26\nQEW 243,244,245\nQCM 242\nQBG 242\nQCB 64,242\nQCB 66,242\nv_add_f32_dpp v243, v243, v243 row_ror:8 row_mask:0xf bank_mask:0xf\nQCB 68,242\nQCB 70,242\n"
"v_add_f32_dpp v243, v243, v243 row_ror:4 row_mask:0xf bank_mask:0xf\nQCB 72,242\nQCB 74,242\nv_add_f32_dpp v243, v243, v243 row_ror:2 row_mask:0xf bank_mask:0xf\nQCB 76,242\nQCB 78,242\n"
"v_add_f32_dpp v243, v243, v243 row_ror:1 row_mask:0xf bank_mask:0xf\nQCB 80,242\nQCB 82,242\nQN 244,243\nQCC 65,64\nQCC 67,66\nQBN 243,244\nQCC 69,68\nQCC 71,70\nQAJ 243,244\nQCC 73,72\nQCC 75,74\n"
"v_mul_f32 v243, 0x3acccccd, v243\nQCC 77,76\nQCC 79,78\nQCM 243\nQCC 81,80\nQCC 83,82\nQBG 243\nQU 64,65\nQBG 65\nv_add_f32 v243, 0x3727c5ac, v243\nQU 66,67\nQBG 67\nQCM 243\nQU 68,69\nQBG 69\n"
"QBG 243\nQU 70,71\nQBG 71\nv_sqrt_f32 v243, v243\nQU 72,73\nQBG 73\nQCM 243\nQU 74,75\nQBG 75\nQBG 243\nQU 76,77\nQBG 77\nQV 243\nQU 78,79\nQBG 79\nQU 80,81\nQBG 81\nQU 82,83\nQBG 83\nQAU 64,242\n"
"QAU 66,242\nQAU 68,242\nQAU 70,242\nQAU 72,242\nQAU 74,242\nQAU 76,242\nQAU 78,242\nQAU 80,242\nQAU 82,242\nQF 96,64,65\nQF 97,66,67\nQF 98,68,69\nQF 99,70,71\nQF 100,72,73\nQF 101,74,75\n"
"QF 102,76,77\nQF 103,78,79\nQF 104,80,81\nQF 105,82,83\ns_mul_i32 s48, s44, 5120\ns_add_u32 s48, s48, 139264\nQCD 106,249,0\nQCD 108,249,2048\nQCD 110,249,4096\nQCD 112,249,6144\nQCD 114,249,8192\n"
"QCD 96,249,10240\nQCD 98,249,12288\nQCD 100,249,14336\nQCD 102,249,16384\nQCD 104,249,18432\nQBM 248,48,241\nv_lshrrev_b32 v242, 3, v241\nQET 242,241\nv_and_b32 v242, 0x70, v242\n"
"v_and_b32 v249, 0x380, v241\nv_lshl_add_u32 v249, v249, 1, v242\nQDW 69\nQBE \nQS 56,232,68,0\nQS 56,233,68,1024\nQS 56,234,68,2048\nQS 56,235,68,3072\nQAO 68\nQBR 68\ns_add_u32 m0, s79, 114688\n"
"QBT 75,72,70\nQS 60,236,75,0\nQS 60,237,75,1024\nQCX 70\nQCY 70\nQEX 69\nQBE \nQS 56,232,68,0\nQS 56,233,68,1024\nQS 56,234,68,2048\nQS 56,235,68,3072\nQAO 68\nQBR 68\ns_mov_b64 s[52:53], %5\nQGR 54\n"
"QBA 55\ns_cmp_eq_u32 s44, 1\ns_cselect_b64 s[52:53], %13, s[52:53]\ns_cselect_b32 s54, 5242880, s54\ns_cmp_eq_u32 s44, 2\ns_cselect_b64 s[52:53], %11, s[52:53]\ns_cselect_b32 s54, 2211840, s54\n"
"s_cmp_eq_u32 s44, 3\ns_cselect_b64 s[52:53], %12, s[52:53]\ns_cselect_b32 s54, 2211840, s54\ns_lshr_b32 s50, s54, 7\nv_mul_lo_u32 v120, v241, s50\nbuffer_load_dword v121, v120, s[52:55], 0 offen\n"
"s_lshl_b32 s42, s45, 13\nQZ 42,79\nbuffer_load_dwordx4 v[250:253], v241, s[60:63], s42 offen offset:0\nbuffer_load_dwordx4 v[250:253], v241, s[60:63], s42 offen offset:1024\ns_cmp_ge_u32 s44, 2\n"
"s_cbranch_scc1 L_kv_v\nL_kv_k:\nQBD \nQAM 23,0\ns_barrier\nQGS 98,126\nQE 0,224,0\nQE 2,224,2048\nQE 4,225,0\nQE 6,225,2048\nQE 8,226,0\nQE 10,226,2048\nQE 12,227,0\nQE 14,227,2048\nQH 128,228,0\n"
"QH 132,228,2048\nQH 136,228,4096\nQH 140,228,6144\nQE 16,224,4096\nQE 18,224,6144\nQE 20,225,4096\nQE 22,225,6144\nQE 24,226,4096\nQE 26,226,6144\nQE 28,227,4096\nQE 30,227,6144\nQH 144,229,0\n"
"QH 148,229,2048\nQH 152,229,4096\nQH 156,229,6144\nQBV 84\nQBE \nQGT 80,116\ns_add_u32 m0, s84, 1024\nQBE \nQGT 82,117\nQAM 15,0\ns_barrier\nQAN 0,0,128\nQE 32,224,8192\nQC 0,16,144\nQE 34,224,10240\n"
"QBV 69\nQAN 4,4,128\nQS 56,232,68,0\nQC 4,20,144\nQE 36,225,8192\nQAN 8,8,128\nQE 38,225,10240\nQC 8,24,144\nQS 56,233,68,1024\nQAN 12,12,128\nQE 40,226,8192\nQC 12,28,144\nQE 42,226,10240\n"
"QAN 16,0,132\nQS 56,234,68,2048\nQC 16,16,148\nQE 44,227,8192\nQAN 20,4,132\nQE 46,227,10240\nQC 20,20,148\nQS 56,235,68,3072\nQAN 24,8,132\nQH 160,228,8192\nQC 24,24,148\nQH 164,228,10240\nQAO 68\n"
"QBR 68\nQDD 79\nQBT 75,72,70\nQAN 28,12,132\nQS 60,236,75,0\nQC 28,28,148\nQH 168,228,12288\nQAN 32,0,136\nQH 172,228,14336\nQC 32,16,152\nQS 60,237,75,1024\nQAN 36,4,136\nQH 176,229,8192\n"
"QC 36,20,152\nQH 180,229,10240\nQCX 70\nQCY 70\nQCT 69\nQAN 40,8,136\nQS 56,232,68,0\nQC 40,24,152\nQH 184,229,12288\nQAN 44,12,136\nQH 188,229,14336\nQC 44,28,152\nQS 56,233,68,1024\nQAN 48,0,140\n"
"QE 48,224,12288\nQC 48,16,156\nQE 50,224,14336\nQAN 52,4,140\nQS 56,234,68,2048\nQC 52,20,156\nQE 52,225,12288\nQAN 56,8,140\nQE 54,225,14336\nQC 56,24,156\nQS 56,235,68,3072\nQAN 60,12,140\n"
"QE 56,226,12288\nQC 60,28,156\nQE 58,226,14336\nQE 60,227,12288\nQE 62,227,14336\nQAO 68\nQBR 68\nQCE 98\nQBJ 127,98\nQDX 92,127,97\nQAM 16,0\ns_barrier\nQC 0,32,160\nQE 64,224,16384\nQC 0,48,176\n"
"QE 66,224,18432\nQDV 69\nQC 4,36,160\nQS 56,232,68,0\nQC 4,52,176\nQE 68,225,16384\nQC 8,40,160\nQE 70,225,18432\nQC 8,56,176\nQS 56,233,68,1024\nQC 12,44,160\nQE 72,226,16384\nQC 12,60,176\n"
"QE 74,226,18432\nQC 16,32,164\nQS 56,234,68,2048\nQC 16,48,180\nQE 76,227,16384\nQC 20,36,164\nQE 78,227,18432\nQC 20,52,180\nQS 56,235,68,3072\nQC 24,40,164\nQH 128,228,16384\nQC 24,56,180\n"
"QH 132,228,18432\nQAO 68\nQBR 68\ns_add_u32 m0, s79, 98304\nQBT 75,72,70\nQC 28,44,164\nQS 60,236,75,0\nQC 28,60,180\nQH 136,228,20480\nQC 32,32,168\nQH 140,228,22528\nQC 32,48,184\n"
"QS 60,237,75,1024\nQC 36,36,168\nQH 144,229,16384\nQC 36,52,184\nQH 148,229,18432\nQCX 70\nQCY 70\ns_add_u32 m0, s69, 12288\nQC 40,40,168\nQS 56,232,68,0\nQC 40,56,184\nQH 152,229,20480\n"
"QC 44,44,168\nQH 156,229,22528\nQC 44,60,184\nQS 56,233,68,1024\nQC 48,32,172\nQE 80,224,20480\nQC 48,48,188\nQE 82,224,22528\nQC 52,36,172\nQS 56,234,68,2048\nQC 52,52,188\nQE 84,225,20480\n"
"QC 56,40,172\nQE 86,225,22528\nQC 56,56,188\nQS 56,235,68,3072\nQC 60,44,172\nQE 88,226,20480\nQC 60,60,188\nQE 90,226,22528\nQE 92,227,20480\nQE 94,227,22528\nQAO 68\nQBR 68\nQAM 11,0\ns_barrier\n"
"QC 0,64,128\nQE 96,224,0\nQC 0,80,144\nQE 98,224,2048\nQDW 69\nQC 4,68,128\nQS 56,232,68,0\nQC 4,84,144\nQE 100,225,0\nQC 8,72,128\nQE 102,225,2048\nQC 8,88,144\nQS 56,233,68,1024\nQC 12,76,128\n"
"QE 104,226,0\nQC 12,92,144\nQE 106,226,2048\nQC 16,64,132\nQS 56,234,68,2048\nQC 16,80,148\nQE 108,227,0\nQC 20,68,132\nQE 110,227,2048\nQC 20,84,148\nQS 56,235,68,3072\nQC 24,72,132\n"
"QH 160,228,24576\nQC 24,88,148\nQH 164,228,26624\nQAO 68\nQBR 68\nQDN 79\nQBT 75,72,70\nQC 28,76,132\nQS 60,236,75,0\nQC 28,92,148\nQH 168,228,28672\nQC 32,64,136\nQH 172,228,30720\nQC 32,80,152\n"
"QS 60,237,75,1024\nQC 36,68,136\nQH 176,229,24576\nQC 36,84,152\nQH 180,229,26624\nQCX 70\nQCY 70\nQEX 69\nQC 40,72,136\nQS 56,232,68,0\nQC 40,88,152\nQH 184,229,28672\nQC 44,76,136\n"
"QH 188,229,30720\nQC 44,92,152\nQS 56,233,68,1024\nQC 48,64,140\nQE 112,224,4096\nQC 48,80,156\nQE 114,224,6144\nQC 52,68,140\nQS 56,234,68,2048\nQC 52,84,156\nQE 116,225,4096\nQC 56,72,140\n"
"QE 118,225,6144\nQC 56,88,156\nQS 56,235,68,3072\nQC 60,76,140\nQE 120,226,4096\nQC 60,92,156\nQE 122,226,6144\nQE 124,227,4096\nQE 126,227,6144\nQAO 68\nQBR 68\nQCE 98\nQBJ 127,98\nQDX 92,127,97\n"
"QAM 11,0\ns_barrier\nQC 0,96,160\nQE 128,224,8192\nQC 0,112,176\nQE 130,224,10240\nQBV 69\nQC 4,100,160\nQS 56,232,68,0\nQC 4,116,176\nQE 132,225,8192\nQC 8,104,160\nQE 134,225,10240\nQC 8,120,176\n"
"QS 56,233,68,1024\nQC 12,108,160\nQE 136,226,8192\nQC 12,124,176\nQE 138,226,10240\nQC 16,96,164\nQS 56,234,68,2048\nQC 16,112,180\nQE 140,227,8192\nQC 20,100,164\nQE 142,227,10240\nQC 20,116,180\n"
"QS 56,235,68,3072\nQC 24,104,164\nQH 128,228,0\nQC 24,120,180\nQH 132,228,2048\nQAO 68\nQBR 68\ns_add_u32 m0, s79, 114688\nQBT 75,72,70\nQC 28,108,164\nQS 60,236,75,0\nQC 28,124,180\nQH 136,228,4096\n"
"QC 32,96,168\nQH 140,228,6144\nQC 32,112,184\nQS 60,237,75,1024\nQC 36,100,168\nQH 144,229,0\nQC 36,116,184\nQH 148,229,2048\nQCX 70\nQCY 70\nQCT 69\nQC 40,104,168\nQS 56,232,68,0\nQC 40,120,184\n"
"QH 152,229,4096\nQC 44,108,168\nQH 156,229,6144\nQC 44,124,184\nQS 56,233,68,1024\nQC 48,96,172\nQE 144,224,12288\nQC 48,112,188\nQE 146,224,14336\nQC 52,100,172\nQS 56,234,68,2048\nQC 52,116,188\n"
"QE 148,225,12288\nQC 56,104,172\nQE 150,225,14336\nQC 56,120,188\nQS 56,235,68,3072\nQC 60,108,172\nQE 152,226,12288\nQC 60,124,188\nQE 154,226,14336\nQE 156,227,12288\nQE 158,227,14336\n"
"QEY 84,118,0\nQEY 86,118,512\nQEY 88,118,1024\nQEY 90,118,1536\nQH 96,248,0\nQH 100,248,1024\nQH 104,248,2048\nQH 108,248,3072\nQH 112,248,4096\nQAO 68\nQBR 68\nQAM 11,0\ns_barrier\nQC 0,128,128\n"
"QE 160,224,16384\nQC 0,144,144\nQE 162,224,18432\nQDV 69\nQC 4,132,128\nQS 56,232,68,0\nQC 4,148,144\nQE 164,225,16384\nQC 8,136,128\nQE 166,225,18432\nQC 8,152,144\nQS 56,233,68,1024\nQC 12,140,128\n"
"QE 168,226,16384\nQC 12,156,144\nQE 170,226,18432\nQC 16,128,132\nQS 56,234,68,2048\nQC 16,144,148\nQE 172,227,16384\nQC 20,132,132\nQE 174,227,18432\nQC 20,148,148\nQS 56,235,68,3072\nQC 24,136,132\n"
"QH 160,228,8192\nQC 24,152,148\nQH 164,228,10240\nQAO 68\nQBR 68\nQDD 79\nQBT 75,72,70\nQC 28,140,132\nQS 60,236,75,0\nQC 28,156,148\nQH 168,228,12288\nQC 32,128,136\nQH 172,228,14336\nQC 32,144,152\n"
"QS 60,237,75,1024\nQC 36,132,136\nQH 176,229,8192\nQC 36,148,152\nQH 180,229,10240\nQCX 70\nQCY 70\ns_add_u32 m0, s69, 12288\nQC 40,136,136\nQS 56,232,68,0\nQC 40,152,152\nQH 184,229,12288\n"
"QC 44,140,136\nQH 188,229,14336\nQC 44,156,152\nQS 56,233,68,1024\nQC 48,128,140\nQE 176,224,20480\nQC 48,144,156\nQE 178,224,22528\nQC 52,132,140\nQS 56,234,68,2048\nQC 52,148,156\nQE 180,225,20480\n"
"QC 56,136,140\nQE 182,225,22528\nQC 56,152,156\nQS 56,235,68,3072\nQC 60,140,140\nQE 184,226,20480\nQC 60,156,156\nQE 186,226,22528\nQE 188,227,20480\nQE 190,227,22528\nQAO 68\nQBR 68\nQCE 98\n"
"QBJ 127,98\nQDX 92,127,97\nQAM 11,0\ns_barrier\nQC 0,160,160\nQE 192,224,0\nQC 0,176,176\nQE 194,224,2048\ns_add_u32 m0, s71, 131072\nQDE 75,76\nQC 4,164,160\nQS 60,236,75,0\nQC 4,180,176\n"
"QE 196,225,0\nQC 8,168,160\nQE 198,225,2048\nQC 8,184,176\nQS 60,237,75,1024\nQC 12,172,160\nQE 200,226,0\nQC 12,188,176\nQE 202,226,2048\nQC 16,160,164\nQS 60,238,75,2048\nQC 16,176,180\n"
"QE 204,227,0\nQC 20,164,164\nQE 206,227,2048\nQC 20,180,180\nQS 60,239,75,3072\nQC 24,168,164\nQH 128,228,16384\nQC 24,184,180\nQH 132,228,18432\ns_add_u32 m0, s71, 135168\nQDE 75,77\nQC 28,172,164\n"
"QS 60,236,75,0\nQC 28,188,180\nQH 136,228,20480\nQC 32,160,168\nQH 140,228,22528\nQC 32,176,184\nQS 60,237,75,1024\nQC 36,164,168\nQH 144,229,16384\nQC 36,180,184\nQH 148,229,18432\nQC 40,168,168\n"
"QS 60,238,75,2048\nQC 40,184,184\nQH 152,229,20480\nQC 44,172,168\nQH 156,229,22528\nQC 44,188,184\nQS 60,239,75,3072\nQC 48,160,172\nQE 208,224,4096\nQC 48,176,188\nQE 210,224,6144\nQC 52,164,172\n"
"QE 212,225,4096\nQC 52,180,188\nQE 214,225,6144\nQC 56,168,172\nQE 216,226,4096\nQC 56,184,188\nQE 218,226,6144\nQC 60,172,172\nQE 220,227,4096\nQC 60,188,188\nQE 222,227,6144\nQAM 9,0\ns_barrier\n"
"QC 0,192,128\nQE 224,224,8192\nQC 0,208,144\nQE 226,224,10240\nQC 4,196,128\nQAB 96,88,249,49,0\nQC 4,212,144\nQE 228,225,8192\nQC 8,200,128\nQE 230,225,10240\nQC 8,216,144\nQAB 100,88,249,49,2048\n"
"QC 12,204,128\nQE 232,226,8192\nQC 12,220,144\nQE 234,226,10240\nQAA 49\nQC 16,192,132\nQAB 104,88,249,49,0\nQC 16,208,148\nQE 236,227,8192\nQC 20,196,132\nQE 238,227,10240\nQC 20,212,148\n"
"QAB 108,88,249,49,2048\nQC 24,200,132\nQH 160,228,24576\nQC 24,216,148\nQH 164,228,26624\nQAA 49\nQC 28,204,132\nQAB 112,88,249,49,0\nQC 28,220,148\nQH 168,228,28672\nQC 32,192,136\nQH 172,228,30720\n"
"QC 32,208,152\nbuffer_store_dwordx4 v[84:87], v241, s[88:91], s86 offen sc1\nQC 36,196,136\nQH 176,229,24576\nQC 36,212,152\nQH 180,229,26624\nQC 40,200,136\n"
"buffer_store_dwordx4 v[88:91], v241, s[88:91], s87 offen sc1\nQC 40,216,152\nQH 184,229,28672\nQC 44,204,136\nQH 188,229,30720\nQC 44,220,152\nQE 240,224,12288\nQC 48,192,140\nQE 242,224,14336\n"
"QC 48,208,156\nQE 244,225,12288\nQC 52,196,140\nQE 246,225,14336\nQC 52,212,156\nQE 248,226,12288\nQC 56,200,140\nQE 250,226,14336\nQC 56,216,156\nQE 252,227,12288\nQC 60,204,140\nQE 254,227,14336\n"
"QC 60,220,156\nQAM 7,0\ns_barrier\nQC 0,224,160\nQH 128,230,0\nQC 0,240,176\nQH 132,231,0\nQBV 71\nQCU 75,76\nQC 4,228,160\nQS 60,236,75,0\nQC 4,244,176\nQH 136,230,2048\nQC 8,232,160\n"
"QH 140,231,2048\nQC 8,248,176\nQS 60,237,75,1024\nQC 12,236,160\nQH 144,230,4096\nQC 12,252,176\nQH 148,231,4096\nQC 16,224,164\nQS 60,238,75,2048\nQC 16,240,180\nQH 152,230,6144\nQC 20,228,164\n"
"QH 156,231,6144\nQC 20,244,180\nQS 60,239,75,3072\nQCT 71\nQCU 75,77\nQC 24,232,164\nQS 60,236,75,0\nQC 24,248,180\nQS 60,237,75,1024\nQC 28,236,164\nQS 60,238,75,2048\nQC 28,252,180\n"
"QS 60,239,75,3072\nQGK 99\nQC 32,224,168\nQCZ 92,127,99\nQC 32,240,184\nQC 36,228,168\nQC 36,244,184\nQC 40,232,168\nQC 40,248,184\nQC 44,236,168\nQC 44,252,184\nQC 48,224,172\nQC 48,240,188\n"
"QC 52,228,172\nQC 52,244,188\nQC 56,232,172\nQC 56,248,188\nQC 60,236,172\nQC 60,252,188\nQG 4\nQAN 192,0,128\nQC 192,16,132\nQAN 196,4,128\nQH 160,230,8192\nQC 196,20,132\nQDF 71\nQGO 75,76\n"
"QK 0,78\nQAN 200,8,128\nQS 60,236,75,0\nQC 200,24,132\nQH 164,231,8192\nQK 1,78\nQAN 204,12,128\nQK 2,78\nQK 3,78\nQC 204,28,132\nQK 4,78\nQK 5,78\nQAN 208,0,136\nQH 168,230,10240\nQK 6,78\n"
"QC 208,16,140\nQK 7,78\nQAY 0,1\nQAN 212,4,136\nQS 60,237,75,1024\nQC 212,20,140\nQH 172,231,10240\nQF 1,2,3\nQAN 216,8,136\nQF 2,4,5\nQF 3,6,7\nQC 216,24,140\nQK 8,78\nQK 9,78\nQAN 220,12,136\n"
"QS 60,238,75,2048\nQC 220,28,140\nQG 4\nQK 10,78\nQK 11,78\nQC 192,32,144\nQK 12,78\nQK 13,78\nQC 192,48,148\nQK 14,78\nQK 15,78\nQC 196,36,144\nQH 176,230,12288\nQF 4,8,9\nQC 196,52,148\nQF 5,10,11\n"
"QF 6,12,13\nQC 200,40,144\nQS 60,239,75,3072\nQC 200,56,148\nQH 180,231,12288\nQF 7,14,15\nQC 204,44,144\nQK 16,78\nQK 17,78\nQC 204,60,148\nQK 18,78\nQK 19,78\nQC 208,32,152\nQH 184,230,14336\n"
"QK 20,78\nQC 208,48,156\ns_add_u32 m0, s71, 36864\nQGO 75,77\nQK 21,78\nQC 212,36,152\nQS 60,236,75,0\nQC 212,52,156\nQH 188,231,14336\nQK 22,78\nQC 216,40,152\nQK 23,78\nQF 8,16,17\nQC 216,56,156\n"
"QF 9,18,19\nQF 10,20,21\nQC 220,44,152\nQS 60,237,75,1024\nQC 220,60,156\nQG 4\nQF 11,22,23\nQK 24,78\nQC 192,64,160\nQK 25,78\nQK 26,78\nQC 192,80,164\nQK 27,78\nQK 28,78\nQC 196,68,160\n"
"QH 128,230,16384\nQK 29,78\nQC 196,84,164\nQK 30,78\nQK 31,78\nQC 200,72,160\nQS 60,238,75,2048\nQC 200,88,164\nQH 132,231,16384\nQF 12,24,25\nQC 204,76,160\nQF 13,26,27\nQF 14,28,29\nQC 204,92,164\n"
"QF 15,30,31\nQK 32,78\nQC 208,64,168\nQH 136,230,18432\nQK 33,78\nQC 208,80,172\nQK 34,78\nQK 35,78\nQC 212,68,168\nQS 60,239,75,3072\nQC 212,84,172\nQH 140,231,18432\nQK 36,78\nQC 216,72,168\n"
"QEZ 73,74\nQCG 100\nL_pc2:\ns_lshr_b32 s99, %1, 2\nQGB 99\ns_bitcmp1_b32 %0, 0\nQGC 99\nQER 99\ns_min_u32 s99, s99, L_done-L_pc2-512\nQZ 100,99\nQAX 101\nQES 252,241\nQGD 100,250,252\nQC 216,88,172\n"
"QAB 0,64,240,73,0\nQC 220,76,168\nQK 37,78\nQK 38,78\nQC 220,92,172\nQG 4\nQK 39,78\nQF 16,32,33\nQC 192,96,176\nQF 17,34,35\nQF 18,36,37\nQC 192,112,180\nQF 19,38,39\nQK 40,78\nQC 196,100,176\n"
"QH 144,230,20480\nQK 41,78\nQC 196,116,180\nQK 42,78\nQK 43,78\nQC 200,104,176\nQK 44,78\nQK 45,78\nQC 200,120,180\nQH 148,231,20480\nQK 46,78\nQC 204,108,176\nQK 47,78\nQF 20,40,41\nQC 204,124,180\n"
"QF 21,42,43\nQF 22,44,45\nQC 208,96,184\nQH 152,230,22528\nQF 23,46,47\nQC 208,112,188\nQEZ 73,74\nQK 48,78\nQK 49,78\nQC 212,100,184\nQAB 4,64,240,73,1024\nQC 212,116,188\nQH 156,231,22528\n"
"QK 50,78\nQC 216,104,184\nQK 51,78\nQK 52,78\nQC 216,120,188\nQK 53,78\nQK 54,78\nQC 220,108,184\nQK 55,78\nQF 24,48,49\nQC 220,124,188\nQG 4\nQF 25,50,51\nQF 26,52,53\nQC 192,128,128\nQF 27,54,55\n"
"QK 56,78\nQC 192,144,132\nQK 57,78\nQK 58,78\nQC 196,132,128\nQH 160,230,24576\nQK 59,78\nQC 196,148,132\nQK 60,78\nQK 61,78\nQC 200,136,128\nQK 62,78\nQK 63,78\nQC 200,152,132\nQH 164,231,24576\n"
"QF 28,56,57\nQC 204,140,128\nQF 29,58,59\nQF 30,60,61\nQC 204,156,132\nQF 31,62,63\nQEG 50,44\nQC 208,128,136\nQH 168,230,26624\ns_lshl_b32 s85, s44, 5\nQC 208,144,140\nQEZ 73,74\n"
"s_mov_b64 s[52:53], %5\nQGR 54\nQBA 55\ns_mul_i32 s50, s46, 80\nQC 212,132,136\nQAB 8,64,240,73,2048\nQC 212,148,140\nQH 172,231,26624\ns_mul_i32 s51, s44, 20\nQC 216,136,136\nQZ 50,51\n"
"s_lshl_b32 s50, s50, 13\ns_lshl_b32 s51, s45, 8\nQC 216,152,140\nQZ 50,51\nQDS 124,241\nv_min_u32 v124, 19, v124\nQC 220,140,136\nv_lshlrev_b32 v124, 13, v124\nQGU 122,241\nQC 220,156,140\nQG 4\n"
"QFX 124,122\nQC 192,160,144\nv_lshrrev_b32 v96, 6, v241\nv_cmp_le_u32 s[80:81], 1, v96\nQC 192,176,148\nv_cmp_le_u32 s[82:83], 2, v96\nv_cmp_le_u32 s[84:85], 5, v96\nQC 196,164,144\nQH 176,230,28672\n"
"v_cmp_le_u32 s[86:87], 10, v96\nQC 196,180,148\ns_mov_b64 s[88:89], %9\nQBJ 98,88\nQBJ 99,89\nQC 200,168,144\ns_mov_b64 s[88:89], %10\nQBJ 115,88\nQBJ 116,89\nQC 200,184,148\nQH 180,231,28672\n"
"QAL 80,98,115\nQC 204,172,144\nQAL 80,99,116\ns_mov_b64 s[88:89], %11\nQC 204,188,148\nQBJ 115,88\nQBJ 116,89\nQC 208,160,152\nQH 184,230,30720\nQAL 82,98,115\nQC 208,176,156\nQEZ 73,74\n"
"QAL 82,99,116\ns_mov_b64 s[88:89], %13\nQC 212,164,152\nQAB 12,64,240,73,3072\nQC 212,180,156\nQH 188,231,30720\nQBJ 115,88\nQC 216,168,152\nQBJ 116,89\nQAL 84,98,115\nQC 216,184,156\nQAL 84,99,116\n"
"s_mov_b64 s[88:89], %12\nQC 220,172,152\nQBJ 115,88\nQBJ 116,89\nQC 220,188,156\nQG 4\nQAL 86,98,115\nQAL 86,99,116\nQC 192,192,160\nQAM 13,0\ns_barrier\nv_xor_b32 v242, v127, v126\nQBE \n"
"QGS 100,242\nQBE \nQBZ 100\ns_cbranch_scc1 L_ps3\nL_pr3:\nQCR 244\ns_bfe_u32 s99, %1, 262144\nQBU 99\ns_add_u32 s99, s99, 0\nQZ 99,96\nQEI \nQEJ 92,244,99\nQEK \nQC 192,208,164\n"
"v_add_u32 v230, 4294836224, v230\nv_add_u32 v231, 4294836224, v231\nQC 196,196,160\nQH 128,230,0\nQAG 97\nQC 196,212,164\nQCR 115\nQAL 80,97,115\nQC 200,200,160\nv_mov_b32 v115, 2\nQAL 82,97,115\n"
"QC 200,216,164\nQH 132,231,0\nQFA 115\nQC 204,204,160\nQAL 84,97,115\nQFB 115\nQC 204,220,164\nQAL 86,97,115\nv_mov_b32 v109, 200\nQC 208,192,168\nQH 136,230,2048\nv_mov_b32 v115, 540\n"
"QC 208,208,172\nQFC 73,74\nQAL 82,109,115\nQGV 115\nQC 212,196,168\nQAB 16,64,240,73,0\nQC 212,212,172\nQH 140,231,2048\nQAL 84,109,115\nQC 216,200,168\nv_mov_b32 v115, 540\nQAL 86,109,115\n"
"QC 216,216,172\nQGV 100\nv_mov_b32 v115, 3456\nQC 220,204,168\nQAL 82,100,115\nQGV 115\nQC 220,220,172\nQG 4\nQAL 84,100,115\nQFB 101\nQC 192,224,176\nv_mov_b32 v115, 27\nQAL 82,101,115\n"
"QC 192,240,180\nQFB 115\nQAL 84,101,115\nQC 196,228,176\nQH 144,230,4096\nQGW 102\nQC 196,244,180\nv_mov_b32 v115, 2428\nQAL 82,102,115\nQC 200,232,176\nQGW 115\nQAL 84,102,115\nQC 200,248,180\n"
"QH 148,231,4096\nQCR 103\nQC 204,236,176\nQFA 115\nQAL 80,103,115\nQC 204,252,180\nQCR 115\nQAL 84,103,115\nQC 208,224,184\nQH 152,230,6144\nv_mov_b32 v115, 7\nQC 208,240,188\nQAL 86,103,115\n"
"v_mov_b32 v104, 65536\nQC 212,228,184\nQGX 115\nQAL 80,104,115\nQC 212,244,188\nQH 156,231,6144\nv_mov_b32 v115, 65536\nQC 216,232,184\nQAL 84,104,115\nv_mov_b32 v115, 9363\nQC 216,248,188\n"
"QAL 86,104,115\nQFA 105\nQC 220,236,184\nv_mov_b32 v115, 7\nQAL 82,105,115\nQC 220,252,188\nQG 4\nQFB 115\nQAL 84,105,115\nQAN 64,0,128\nQC 64,16,132\nQAN 68,4,128\nQH 160,230,8192\nQC 68,20,132\n"
"QDY 71\ns_add_u32 s75, s76, 163840\nQK 192,78\nQAN 72,8,128\nQS 60,236,75,0\nQC 72,24,132\nQH 164,231,8192\nQK 193,78\nQAN 76,12,128\nQK 194,78\nQK 195,78\nQC 76,28,132\nQK 196,78\nQK 197,78\n"
"QAN 80,0,136\nQH 168,230,10240\nQK 198,78\nQC 80,16,140\nQK 199,78\nQF 0,192,193\nQAN 84,4,136\nQS 60,237,75,1024\nQC 84,20,140\nQH 172,231,10240\nQF 1,194,195\nQAN 88,8,136\nQF 2,196,197\n"
"QF 3,198,199\nQC 88,24,140\nQK 200,78\nQK 201,78\nQAN 92,12,136\nQS 60,238,75,2048\nQC 92,28,140\nQG 4\nQK 202,78\nQK 203,78\nQC 64,32,144\nQK 204,78\nQK 205,78\nQC 64,48,148\nQK 206,78\nQK 207,78\n"
"QC 68,36,144\nQH 176,230,12288\nQF 4,200,201\nQC 68,52,148\nQF 5,202,203\nQF 6,204,205\nQC 72,40,144\nQS 60,239,75,3072\nQC 72,56,148\nQH 180,231,12288\nQF 7,206,207\nQC 76,44,144\nQK 208,78\n"
"QK 209,78\nQC 76,60,148\nQK 210,78\nQK 211,78\nQC 80,32,152\nQH 184,230,14336\nQK 212,78\nQC 80,48,156\ns_add_u32 m0, s71, 69632\ns_add_u32 s75, s77, 163840\nQK 213,78\nQC 84,36,152\nQS 60,236,75,0\n"
"QC 84,52,156\nQH 188,231,14336\nQK 214,78\nQC 88,40,152\nQK 215,78\nQF 8,208,209\nQC 88,56,156\nQF 9,210,211\nQF 10,212,213\nQC 92,44,152\nQS 60,237,75,1024\nQC 92,60,156\nQG 4\nQF 11,214,215\n"
"QK 216,78\nQC 64,64,160\nQK 217,78\nQK 218,78\nQC 64,80,164\nQK 219,78\nQK 220,78\nQC 68,68,160\nQH 128,230,16384\nQK 221,78\nQC 68,84,164\nQK 222,78\nQK 223,78\nQC 72,72,160\nQS 60,238,75,2048\n"
"QC 72,88,164\nQH 132,231,16384\nQF 12,216,217\nQC 76,76,160\nQF 13,218,219\nQF 14,220,221\nQC 76,92,164\nQF 15,222,223\nQFA 115\nQC 80,64,168\nQH 136,230,18432\nQAL 86,105,115\nQC 80,80,172\nQGX 106\n"
"v_mov_b32 v115, 9363\nQC 84,68,168\nQS 60,239,75,3072\nQC 84,84,172\nQH 140,231,18432\nQAL 82,106,115\nQC 88,72,168\nQFC 73,74\nQGW 115\nQAL 84,106,115\nQC 88,88,172\nQAB 20,64,240,73,1024\n"
"QC 92,76,168\nQGX 115\nQAL 86,106,115\nQC 92,92,172\nQG 4\nv_mov_b32 v107, 20\nv_mov_b32 v115, 128\nQC 64,96,176\nQAL 84,107,115\nv_mov_b32 v115, 56\nQC 64,112,180\nQAL 86,107,115\nQAG 108\n"
"QC 68,100,176\nQH 144,230,20480\nv_mov_b32 v115, 819200\nQC 68,116,180\nQAL 80,108,115\nv_mov_b32 v115, 1638400\nQC 72,104,176\nQAL 82,108,115\nv_mov_b32 v115, 6225920\nQC 72,120,180\n"
"QH 148,231,20480\nQAL 84,108,115\nQC 76,108,176\nv_mov_b32 v115, 3932160\nQAL 86,108,115\nQC 76,124,180\nv_sub_u32 v97, v96, v97\nv_lshlrev_b32 v97, 8, v97\nQC 80,96,184\nQH 152,230,22528\n"
"v_add_u32 v97, %1, v97\nQC 80,112,188\nQFC 73,74\nv_cmp_lt_u32 s[88:89], v97, v109\nv_mov_b32 v115, 0x40000000\nQC 84,100,184\nQAB 24,64,240,73,2048\nQC 84,116,188\nQH 156,231,22528\n"
"v_cndmask_b32 v108, v115, v108, s[88:89]\nQC 88,104,184\nv_cndmask_b32 v97, 0, v97, s[88:89]\nQDZ 110,97,102\nQC 88,120,188\nQDA 110\nQDZ 115,110,101\nQC 92,108,184\nQFD 111,97,115\nQDZ 115,110,104\n"
"QC 92,124,188\nQG 4\nQDA 115\nv_mul_u32_u24 v115, v115, v103\nQC 64,128,128\nQFD 112,110,115\nv_cmp_gt_u32 vcc, 42, v110\nQC 64,144,132\nQBE \nv_cndmask_b32 v112, 0, v112, vcc\n"
"v_max_u32 v103, 3, v103\nQC 68,132,128\nQH 160,230,24576\nv_add_u32 v103, -1, v103\nQC 68,148,132\nv_lshlrev_b32 v113, 1, v110\nQCL 115,103\nQC 72,136,128\nv_cmp_ge_u32 vcc, v112, v115\n"
"QFD 116,113,103\nQC 72,152,132\nQH 164,231,24576\nv_cndmask_b32 v115, 0, 1, vcc\nQC 76,140,128\nQDM 113,115\nv_cmp_eq_u32 vcc, v112, v103\nQC 76,156,132\nv_lshlrev_b32 v117, 2, v111\n"
"v_add_u32 v117, %0, v117\nQC 80,128,136\nQH 168,230,26624\nQDG 113,116\nQC 80,144,140\nQFC 73,74\nv_cndmask_b32 v114, 0, v103, vcc\nv_mul_u32_u24 v113, v113, v100\nQC 84,132,136\n"
"QAB 28,64,240,73,3072\nQC 84,148,140\nQH 172,231,26624\nQDT 113\nQC 88,136,136\nv_lshl_add_u32 v113, v117, 5, v113\nv_add_co_u32 v98, vcc, v98, v113\nQC 88,152,140\nQBE \n"
"v_addc_co_u32 v99, vcc, 0, v99, vcc\nv_lshl_add_u32 v100, v114, 16, v100\nQC 92,140,136\nQDZ 118,117,106\nQDA 118\nQC 92,156,140\nQG 4\nQDZ 115,118,105\nQFD 111,117,115\nQC 64,160,144\n"
"v_subrev_u32 v115, 84, v117\nv_mul_u32_u24 v116, 10923, v115\nQC 64,176,148\nQDA 116\nv_mul_u32_u24 v112, 6, v116\nQC 68,164,144\nQH 176,230,28672\nv_sub_u32 v115, v115, v112\nQC 68,180,148\n"
"v_add_u32 v116, 12, v116\nv_cmp_lt_u32 vcc, 83, v117\nQC 72,168,144\nQBE \nQDG 118,116\nQDG 111,115\nQC 72,184,148\nQH 180,231,28672\nv_mad_u32_u24 v118, v118, v107, v110\nQC 76,172,144\n"
"v_mad_u32_u24 v118, v118, v105, v111\nv_lshl_add_u32 v108, v118, 10, v108\nQC 76,188,148\nv_bfe_u32 v115, v241, 4, 2\nv_cmp_eq_u32 vcc, 1, v115\nQC 80,160,152\nQH 184,230,30720\n"
"v_cndmask_b32 v254, v98, v99, vcc\nQC 80,176,156\nQEA 73,74\nv_cmp_eq_u32 vcc, 2, v115\nQBD \nQDG 254,100\nQC 84,164,152\nQAB 0,64,240,73,0\nQC 84,180,156\nQH 188,231,30720\n"
"v_cmp_eq_u32 vcc, 3, v115\nQC 88,168,152\nQBE \nQDG 254,108\nQC 88,184,156\nQC 92,172,152\nQC 92,188,156\nQG 4\nQC 64,192,160\nQAM 19,0\ns_barrier\nQC 64,208,164\nQDH 230\nQDH 231\nQC 68,196,160\n"
"QH 128,230,0\nQC 68,212,164\nQC 72,200,160\nQC 72,216,164\nQH 132,231,0\nQC 76,204,160\nQC 76,220,164\nQC 80,192,168\nQH 136,230,2048\nQC 80,208,172\nQEA 73,74\nQC 84,196,168\nQAB 4,64,240,73,1024\n"
"QC 84,212,172\nQH 140,231,2048\nQC 88,200,168\nQC 88,216,172\nQC 92,204,168\nQC 92,220,172\nQG 4\nQC 64,224,176\nQC 64,240,180\nQC 68,228,176\nQH 144,230,4096\nQC 68,244,180\nQC 72,232,176\n"
"QC 72,248,180\nQH 148,231,4096\nQC 76,236,176\nQC 76,252,180\nQC 80,224,184\nQH 152,230,6144\nQC 80,240,188\nQC 84,228,184\nQC 84,244,188\nQH 156,231,6144\nQC 88,232,184\nQC 88,248,188\n"
"QC 92,236,184\nQC 92,252,188\nQG 4\nQAN 192,0,128\nQC 192,16,132\nQAN 196,4,128\nQH 160,230,8192\nQC 196,20,132\nQBV 71\ns_add_u32 s75, s76, 196608\nQK 64,78\nQAN 200,8,128\nQS 60,236,75,0\n"
"QC 200,24,132\nQH 164,231,8192\nQK 65,78\nQAN 204,12,128\nQK 66,78\nQK 67,78\nQC 204,28,132\nQK 68,78\nQK 69,78\nQAN 208,0,136\nQH 168,230,10240\nQK 70,78\nQC 208,16,140\nQK 71,78\nQF 16,64,65\n"
"QAN 212,4,136\nQS 60,237,75,1024\nQC 212,20,140\nQH 172,231,10240\nQF 17,66,67\nQAN 216,8,136\nQF 18,68,69\nQF 19,70,71\nQC 216,24,140\nQK 72,78\nQK 73,78\nQAN 220,12,136\nQS 60,238,75,2048\n"
"QC 220,28,140\nQG 4\nQK 74,78\nQK 75,78\nQC 192,32,144\nQK 76,78\nQK 77,78\nQC 192,48,148\nQK 78,78\nQK 79,78\nQC 196,36,144\nQH 176,230,12288\nQF 20,72,73\nQC 196,52,148\nQF 21,74,75\nQF 22,76,77\n"
"QC 200,40,144\nQS 60,239,75,3072\nQC 200,56,148\nQH 180,231,12288\nQF 23,78,79\nQC 204,44,144\nQK 80,78\nQK 81,78\nQC 204,60,148\nQK 82,78\nQK 83,78\nQC 208,32,152\nQH 184,230,14336\nQK 84,78\n"
"QC 208,48,156\nQCT 71\ns_add_u32 s75, s77, 196608\nQK 85,78\nQC 212,36,152\nQS 60,236,75,0\nQC 212,52,156\nQH 188,231,14336\nQK 86,78\nQC 216,40,152\nQK 87,78\nQF 24,80,81\nQC 216,56,156\n"
"QF 25,82,83\nQF 26,84,85\nQC 220,44,152\nQS 60,237,75,1024\nQC 220,60,156\nQG 4\nQF 27,86,87\nQK 88,78\nQC 192,64,160\nQK 89,78\nQK 90,78\nQC 192,80,164\nQK 91,78\nQK 92,78\nQC 196,68,160\n"
"QH 128,230,16384\nQK 93,78\nQC 196,84,164\nQK 94,78\nQK 95,78\nQC 200,72,160\nQS 60,238,75,2048\nQC 200,88,164\nQH 132,231,16384\nQF 28,88,89\nQC 204,76,160\nQF 29,90,91\nQF 30,92,93\nQC 204,92,164\n"
"QF 31,94,95\nQC 208,64,168\nQH 136,230,18432\nQC 208,80,172\nQC 212,68,168\nQS 60,239,75,3072\nQC 212,84,172\nQH 140,231,18432\nQC 216,72,168\nQC 216,88,172\n"
"buffer_load_dword v123, v124, s[52:55], s50 offen offset:0\nQC 220,76,168\nQC 220,92,172\nQG 4\nQC 192,96,176\nQEA 73,74\nQC 192,112,180\nQAB 8,64,240,73,2048\nQC 196,100,176\nQH 144,230,20480\n"
"QC 196,116,180\nQC 200,104,176\nQC 200,120,180\nQH 148,231,20480\nQC 204,108,176\nQC 204,124,180\nQC 208,96,184\nQH 152,230,22528\nQC 208,112,188\nQEA 73,74\nQC 212,100,184\nQAB 12,64,240,73,3072\n"
"QC 212,116,188\nQH 156,231,22528\nQC 216,104,184\nQC 216,120,188\nQC 220,108,184\nQC 220,124,188\nQG 4\nQC 192,128,128\nQC 192,144,132\nQC 196,132,128\nQH 160,230,24576\nQC 196,148,132\n"
"QC 200,136,128\nQC 200,152,132\nQH 164,231,24576\nQC 204,140,128\nQC 204,156,132\nQC 208,128,136\nQH 168,230,26624\nQC 208,144,140\nQEL 73,74\nQC 212,132,136\nQAB 16,64,240,73,0\nQC 212,148,140\n"
"QH 172,231,26624\nQC 216,136,136\nQC 216,152,140\nQC 220,140,136\nQC 220,156,140\nQG 4\nQC 192,160,144\nQC 192,176,148\nQC 196,164,144\nQH 176,230,28672\nQC 196,180,148\nQC 200,168,144\n"
"QC 200,184,148\nQH 180,231,28672\nQC 204,172,144\nQC 204,188,148\nQC 208,160,152\nQH 184,230,30720\nQC 208,176,156\nQEL 73,74\nQC 212,164,152\nQAB 20,64,240,73,1024\nQC 212,180,156\nQH 188,231,30720\n"
"QC 216,168,152\nQC 216,184,156\nQC 220,172,152\nQC 220,188,156\nQG 4\nQC 192,192,160\nQAM 18,0\ns_barrier\nQC 192,208,164\nQDH 230\nQDH 231\nQC 196,196,160\nQH 128,230,0\nQC 196,212,164\n"
"QC 200,200,160\nQC 200,216,164\nQH 132,231,0\nQC 204,204,160\nQC 204,220,164\nQC 208,192,168\nQH 136,230,2048\nQC 208,208,172\nQEL 73,74\nQC 212,196,168\nQAB 24,64,240,73,2048\nQC 212,212,172\n"
"QH 140,231,2048\nQC 216,200,168\nQCG 36\nL_pc4:\ns_lshr_b32 s38, %1, 3\nQBH 38\ns_add_u32 s38, s38, %0\ns_mul_i32 s38, s38, ((L_done-L_roleS-1024)/127+63)&-64\n"
"s_min_u32 s38, s38, L_done-L_roleS-1024\ns_add_u32 s38, s38, L_roleS-L_pc4\nQZ 36,38\nQAX 37\nglobal_load_dwordx4 v[120:123], v241, s[36:37]\nQC 216,216,172\nQC 220,204,168\nQC 220,220,172\nQG 4\n"
"QC 192,224,176\nQC 192,240,180\nQC 196,228,176\nQH 144,230,4096\nQC 196,244,180\nQC 200,232,176\nQC 200,248,180\nQH 148,231,4096\nQC 204,236,176\nQC 204,252,180\nQC 208,224,184\nQH 152,230,6144\n"
"QC 208,240,188\nQC 212,228,184\nQC 212,244,188\nQH 156,231,6144\nQC 216,232,184\nQC 216,248,188\nQC 220,236,184\nQC 220,252,188\nQG 4\nQAN 64,0,128\nQC 64,16,132\nQAN 68,4,128\nQH 160,230,8192\n"
"QC 68,20,132\nQDF 71\ns_add_u32 s75, s76, 229376\nQK 192,78\nQAN 72,8,128\nQS 60,236,75,0\nQC 72,24,132\nQH 164,231,8192\nQK 193,78\nQAN 76,12,128\nQCV 243,241\nQCW 243\nQDC 99\nQC 76,28,132\n"
"QEB 92,242,243,99\nQAN 80,0,136\nQH 168,230,10240\nQK 194,78\nQC 80,16,140\nQK 195,78\nQK 196,78\nQAN 84,4,136\nQS 60,237,75,1024\nQC 84,20,140\nQH 172,231,10240\nQK 197,78\nQAN 88,8,136\nQK 198,78\n"
"QK 199,78\nQC 88,24,140\nQF 0,192,193\nQF 1,194,195\nQAN 92,12,136\nQS 60,238,75,2048\nQC 92,28,140\nQG 4\nQF 2,196,197\nQF 3,198,199\nQC 64,32,144\nQK 200,78\nQK 201,78\nQC 64,48,148\nQK 202,78\n"
"QK 203,78\nQC 68,36,144\nQH 176,230,12288\nQK 204,78\nQC 68,52,148\nQK 205,78\nQK 206,78\nQC 72,40,144\nQS 60,239,75,3072\nQC 72,56,148\nQH 180,231,12288\nQK 207,78\nQC 76,44,144\nQF 4,200,201\n"
"QF 5,202,203\nQC 76,60,148\nQF 6,204,205\nQF 7,206,207\nQC 80,32,152\nQH 184,230,14336\nQK 208,78\nQC 80,48,156\ns_add_u32 m0, s71, 36864\ns_add_u32 s75, s77, 229376\nQK 209,78\nQC 84,36,152\n"
"QS 60,236,75,0\nQC 84,52,156\nQH 188,231,14336\nQK 210,78\nQC 88,40,152\nQK 211,78\nQK 212,78\nQC 88,56,156\nQK 213,78\nQK 214,78\nQC 92,44,152\nQS 60,237,75,1024\nQC 92,60,156\nQG 4\nQK 215,78\n"
"QF 8,208,209\nQC 64,64,160\nQF 9,210,211\nQF 10,212,213\nQC 64,80,164\nQF 11,214,215\nQK 216,78\nQC 68,68,160\nQH 128,230,16384\nQK 217,78\nQC 68,84,164\nQK 218,78\nQK 219,78\nQC 72,72,160\n"
"QS 60,238,75,2048\nQC 72,88,164\nQH 132,231,16384\nQK 220,78\nQC 76,76,160\nQK 221,78\nQK 222,78\nQC 76,92,164\nQK 223,78\nQF 12,216,217\nQC 80,64,168\nQH 136,230,18432\nQF 13,218,219\nQC 80,80,172\n"
"QF 14,220,221\nQF 15,222,223\nQC 84,68,168\nQS 60,239,75,3072\nQC 84,84,172\nQH 140,231,18432\ns_mov_b32 s80, %0\nQC 88,72,168\nQEL 73,74\ns_and_b32 s83, %1, 7\ns_lshr_b32 s84, %1, 3\nQC 88,88,172\n"
"QAB 28,64,240,73,3072\nQC 92,76,168\ns_and_b32 s81, s84, 7\ns_lshr_b32 s84, s84, 3\nQEG 82,83\nQC 92,92,172\nQG 4\nQZ 82,84\ns_mov_b64 s[88:89], %5\nQC 64,96,176\ns_lshl_b32 s83, s82, 8\nQZ 88,83\n"
"QAX 89\ns_sub_u32 s88, s88, 1024\ns_subb_u32 s89, s89, 0\nQC 64,112,180\ns_sub_u32 s90, 5243904, s83\nQBA 91\nQGY 83,81\nQC 68,100,176\nQH 144,230,20480\nQFY 80\ns_cselect_b32 s84, s83, s81\n"
"QC 68,116,180\nQCK 84\nQBX 85,80\nQBH 85\nQC 72,104,176\nQZ 84,85\nQGE 87,84\nv_lshrrev_b32 v252, 7, v241\nQC 72,120,180\nQH 148,231,20480\nQFE 83,80\nQC 76,108,176\nQBM 253,83,252\n"
"v_lshlrev_b32 v253, 13, v253\nQC 76,124,180\nQEV 252\nv_lshlrev_b32 v252, 5, v252\nQC 80,96,184\nQH 152,230,22528\nv_and_b32 v250, 0x70, v241\nQC 80,112,188\nQDE 73,74\nQET 252,250\nQDM 253,252\n"
"QC 84,100,184\nQAB 0,64,240,73,0\nQC 84,116,188\nQH 156,231,22528\nv_add_u32 v250, 1024, v253\nQC 88,104,184\nv_add_u32 v251, 128, v253\nQFZ 86,80\nQC 88,120,188\nQDC 85\ns_bfe_u32 s55, %1, 196611\n"
"s_bfe_u32 s54, %1, 131072\nQC 92,108,184\nQBH 54\ns_bfe_u32 s83, %1, 131078\nQZ 54,83\nQC 92,124,188\nQG 4\nQBH 54\ns_add_u32 s54, s54, %0\nQC 64,128,128\ns_lshl_b32 s54, s54, 1\nQCJ 83,55\n"
"s_mul_i32 s83, s83, 5120\nQC 64,144,132\nQGE 50,54\nQBT 52,50,83\nQBX 84,55\nQC 68,132,128\nQH 160,230,24576\nQFE 83,84\nQC 68,148,132\nQBT 53,52,83\nQAA 53\nQFZ 83,84\nQC 72,136,128\nQZ 52,83\n"
"s_mov_b64 s[36:37], %2\ns_add_u32 s36, s36, 68157440\nQAX 37\nQC 72,152,132\nQH 164,231,24576\ns_mov_b32 s38, 1638400\nQC 76,140,128\nQBA 39\ns_mov_b64 s[40:41], %2\nQGN 40\nQAX 41\nQC 76,156,132\n"
"QGR 42\nQBA 43\ns_add_u32 s50, s50, 2621440\nQC 80,128,136\nQH 168,230,26624\ns_mov_b32 s44, 4096\nQC 80,144,140\nQDE 73,74\ns_mul_i32 s49, %0, 5120\ns_mul_i32 s48, s55, 102400\nQC 84,132,136\n"
"QAB 4,64,240,73,1024\nQC 84,148,140\nQH 172,231,26624\ns_bitcmp1_b32 %1, 2\ns_cselect_b32 s83, 819200, 0\nQBI 50,87\ns_cselect_b32 s44, 40960, s44\nQC 88,136,136\nQZ 48,83\ns_add_u32 s51, s50, 20480\n"
"QBM 249,49,241\nQC 88,152,140\nQC 92,140,136\nQC 92,156,140\nQG 4\nQC 64,160,144\nQAH 10\nQFF 242\nQBD \ns_cbranch_vccnz L_ps5\nL_pr5:\nQC 64,176,148\nQGF 49\nQC 68,164,144\nQH 176,230,28672\n"
"QC 68,180,148\nQL 36,249,48,0\nQC 72,168,144\nQC 72,184,148\nQH 180,231,28672\nQC 76,172,144\nQC 76,188,148\nQC 80,160,152\nQH 184,230,30720\nQC 80,176,156\nQDE 73,74\nQC 84,164,152\n"
"QAB 8,64,240,73,2048\nQC 84,180,156\nQH 188,231,30720\nQC 88,168,152\nQL 36,249,48,1024\nQC 88,184,156\nQC 92,172,152\nQC 92,188,156\nQG 4\nQC 64,192,160\nQAM 13,0\ns_barrier\nQC 64,208,164\n"
"v_add_u32 v230, 4294901760, v230\nv_add_u32 v231, 4294901760, v231\nQC 68,196,160\nQH 128,230,0\nQC 68,212,164\nQL 36,249,48,2048\nQC 72,200,160\nQC 72,216,164\nQH 132,231,0\nQC 76,204,160\n"
"QC 76,220,164\nQC 80,192,168\nQH 136,230,2048\nQC 80,208,172\nQDE 73,74\nQC 84,196,168\nQAB 12,64,240,73,3072\nQC 84,212,172\nQH 140,231,2048\nQC 88,200,168\nQL 36,249,48,3072\nQC 88,216,172\nQDN 49\n"
"QC 92,204,168\nQAA 48\nQC 92,220,172\nQG 4\nQC 64,224,176\nQC 64,240,180\nQC 68,228,176\nQH 144,230,4096\nQC 68,244,180\nQL 36,249,48,0\nQC 72,232,176\nQBP 48\nQC 72,248,180\nQH 148,231,4096\n"
"QC 76,236,176\nQC 76,252,180\nQC 80,224,184\nQH 152,230,6144\nQC 80,240,188\nQAW 96,40,241,50,0\nQC 84,228,184\nQC 84,244,188\nQH 156,231,6144\nQC 88,232,184\nQC 88,248,188\nQC 92,236,184\n"
"QC 92,252,188\nQG 4\nQAN 192,0,128\nQAW 100,40,241,51,0\nQC 192,16,132\nQAN 196,4,128\nQH 160,230,8192\nQC 196,20,132\nQK 64,78\nQK 65,78\nQAN 200,8,128\nQK 66,78\nQK 67,78\nQC 200,24,132\n"
"QH 164,231,8192\nQK 68,78\nQAN 204,12,128\nQAW 104,40,241,50,1024\nQC 204,28,132\nQK 69,78\nQK 70,78\nQAN 208,0,136\nQH 168,230,10240\nQK 71,78\nQC 208,16,140\nQF 16,64,65\nQF 17,66,67\n"
"QAN 212,4,136\nQF 18,68,69\nQF 19,70,71\nQC 212,20,140\nQH 172,231,10240\nQK 72,78\nQAN 216,8,136\nQCG 68\nL_pc6:\ns_lshr_b32 s70, %1, 3\nQBH 70\ns_add_u32 s70, s70, %0\n"
"s_mul_i32 s70, s70, ((L_att-L_kv_end-1024)/127+63)&-64\ns_min_u32 s70, s70, L_att-L_kv_end-1024\ns_add_u32 s70, s70, L_kv_end-L_pc6\nQZ 68,70\nQAX 69\nQCL 246,241\nQFG 244,68,246\nQC 216,24,140\n"
"QAW 108,40,241,51,1024\nQAN 220,12,136\nQK 73,78\nQK 74,78\nQC 220,28,140\nQG 4\nQK 75,78\nQK 76,78\nQC 192,32,144\nQK 77,78\nQK 78,78\nQC 192,48,148\nQK 79,78\nQF 20,72,73\nQC 196,36,144\n"
"QH 176,230,12288\nQF 21,74,75\nQC 196,52,148\nQAW 112,40,241,50,2048\nQC 200,40,144\nQF 22,76,77\nQF 23,78,79\nQC 200,56,148\nQH 180,231,12288\nQK 80,78\nQC 204,44,144\nQK 81,78\nQK 82,78\n"
"QC 204,60,148\nQK 83,78\nQK 84,78\nQC 208,32,152\nQH 184,230,14336\nQK 85,78\nQC 208,48,156\nQAW 116,40,241,51,2048\nQC 212,36,152\nQK 86,78\nQK 87,78\nQC 212,52,156\nQH 188,231,14336\nQF 24,80,81\n"
"QC 216,40,152\nQF 25,82,83\nQF 26,84,85\nQC 216,56,156\nQF 27,86,87\nQK 88,78\nQC 220,44,152\nQK 89,78\nQK 90,78\nQC 220,60,156\nQG 4\nQK 91,78\nQK 92,78\nQC 192,64,160\nQAW 120,40,241,50,3072\n"
"QC 192,80,164\nQK 93,78\nQK 94,78\nQC 196,68,160\nQH 128,230,16384\nQK 95,78\nQC 196,84,164\nQF 28,88,89\nQF 29,90,91\nQC 200,72,160\nQF 30,92,93\nQF 31,94,95\nQC 200,88,164\nQH 132,231,16384\n"
"QC 204,76,160\nQAW 124,40,241,51,3072\nQC 204,92,164\nQDD 49\nQC 208,64,168\nQH 136,230,18432\nQC 208,80,172\nQFH 73,74\nQC 212,68,168\nQAB 16,64,240,73,0\nQC 212,84,172\nQH 140,231,18432\n"
"QC 216,72,168\nQL 36,249,48,0\nQC 216,88,172\nQC 220,76,168\nQC 220,92,172\nQG 4\nQC 192,96,176\nQC 192,112,180\nQC 196,100,176\nQH 144,230,20480\nQC 196,116,180\nQL 36,249,48,1024\nQC 200,104,176\n"
"QC 200,120,180\nQH 148,231,20480\nQC 204,108,176\nQC 204,124,180\nQC 208,96,184\nQH 152,230,22528\nQC 208,112,188\nQL 36,249,48,2048\nQC 212,100,184\nQC 212,116,188\nQH 156,231,22528\nQC 216,104,184\n"
"QFH 73,74\nQC 216,120,188\nQAB 20,64,240,73,1024\nQC 220,108,184\nQC 220,124,188\nQG 4\nQC 192,128,128\nQL 36,249,48,3072\nQC 192,144,132\nQGG 49\nQC 196,132,128\nQH 160,230,24576\nQC 196,148,132\n"
"QAA 48\nQC 200,136,128\nQC 200,152,132\nQH 164,231,24576\nQC 204,140,128\nQL 36,249,48,0\nQC 204,156,132\nQBP 48\nQC 208,128,136\nQH 168,230,26624\nQC 208,144,140\nQZ 50,44\nQC 212,132,136\n"
"QC 212,148,140\nQH 172,231,26624\nQC 216,136,136\nQAW 32,40,241,50,0\nQC 216,152,140\nQZ 51,44\nQC 220,140,136\nQC 220,156,140\nQG 4\nQC 192,160,144\nQC 192,176,148\nQC 196,164,144\nQH 176,230,28672\n"
"QC 196,180,148\nQFH 73,74\nQC 200,168,144\nQAB 24,64,240,73,2048\nQC 200,184,148\nQH 180,231,28672\nQC 204,172,144\nQAW 36,40,241,51,0\nQC 204,188,148\nQC 208,160,152\nQH 184,230,30720\n"
"QC 208,176,156\nQC 212,164,152\nQC 212,180,156\nQH 188,231,30720\nQC 216,168,152\nQAW 40,40,241,50,1024\nQC 216,184,156\nQC 220,172,152\nQC 220,188,156\nQG 4\nQC 192,192,160\nQAM 30,0\ns_barrier\n"
"QC 192,208,164\nQDH 230\nQDH 231\nQC 196,196,160\nQH 128,230,0\nQC 196,212,164\nQAW 44,40,241,51,1024\nQC 200,200,160\nQC 200,216,164\nQH 132,231,0\nQC 204,204,160\nQFH 73,74\nQC 204,220,164\n"
"QAB 28,64,240,73,3072\nQC 208,192,168\nQH 136,230,2048\nQC 208,208,172\nQAW 48,40,241,50,2048\nQC 212,196,168\nQC 212,212,172\nQH 140,231,2048\nQC 216,200,168\nQC 216,216,172\nQC 220,204,168\n"
"QC 220,220,172\nQG 4\nQC 192,224,176\nQAW 52,40,241,51,2048\nQC 192,240,180\nQC 196,228,176\nQH 144,230,4096\nQC 196,244,180\nQC 200,232,176\nQC 200,248,180\nQH 148,231,4096\nQC 204,236,176\n"
"QAW 56,40,241,50,3072\nQC 204,252,180\nQC 208,224,184\nQH 152,230,6144\nQC 208,240,188\nQC 212,228,184\nQC 212,244,188\nQH 156,231,6144\nQC 216,232,184\nQAW 60,40,241,51,3072\nQC 216,248,188\nQGZ 49\n"
"QC 220,236,184\nQC 220,252,188\nQG 4\nQAN 64,0,128\nQL 36,249,48,0\nQC 64,16,132\nQAN 68,4,128\nQH 160,230,8192\nQC 68,20,132\nQK 192,78\nQK 193,78\nQAN 72,8,128\nQK 194,78\nQK 195,78\nQC 72,24,132\n"
"QH 164,231,8192\nQK 196,78\nQAN 76,12,128\nQL 36,249,48,1024\nQC 76,28,132\nQK 197,78\nQK 198,78\nQAN 80,0,136\nQH 168,230,10240\nQK 199,78\nQC 80,16,140\nQF 0,192,193\nQF 1,194,195\nQAN 84,4,136\n"
"QF 2,196,197\nQF 3,198,199\nQC 84,20,140\nQH 172,231,10240\nQK 200,78\nQAN 88,8,136\nQL 36,249,48,2048\nQC 88,24,140\nQK 201,78\nQK 202,78\nQAN 92,12,136\nQK 203,78\nQK 204,78\nQC 92,28,140\nQG 4\n"
"QK 205,78\nQK 206,78\nQC 64,32,144\nQK 207,78\nQF 4,200,201\nQC 64,48,148\nQF 5,202,203\nQF 6,204,205\nQC 68,36,144\nQH 176,230,12288\nQF 7,206,207\nQC 68,52,148\nQL 36,249,48,3072\nQC 72,40,144\n"
"QFI 49\nQK 208,78\nQK 209,78\nQC 72,56,148\nQH 180,231,12288\nQK 210,78\nQC 76,44,144\nQAA 48\nQK 211,78\nQK 212,78\nQC 76,60,148\nQK 213,78\nQK 214,78\nQC 80,32,152\nQH 184,230,14336\nQK 215,78\n"
"QC 80,48,156\nQL 36,249,48,0\nQC 84,36,152\nQBP 48\nQF 8,208,209\nQF 9,210,211\nQC 84,52,156\nQH 188,231,14336\nQF 10,212,213\nQC 88,40,152\nQBV 86\nQF 11,214,215\nQK 216,78\nQC 88,56,156\nQK 217,78\n"
"QK 218,78\nQC 92,44,152\nQK 219,78\nQK 220,78\nQC 92,60,156\nQG 4\nQK 221,78\nQK 222,78\nQC 64,64,160\nQBB 88,250,85\nQC 64,80,164\nQK 223,78\nQF 12,216,217\nQC 68,68,160\nQH 128,230,16384\n"
"QF 13,218,219\nQC 68,84,164\nQF 14,220,221\nQF 15,222,223\nQC 72,72,160\nQC 72,88,164\nQH 132,231,16384\nQC 76,76,160\nQS 88,251,85,1024\nQC 76,92,164\nQAO 85\nQC 80,64,168\nQH 136,230,18432\n"
"QC 80,80,172\nQCU 73,74\nQC 84,68,168\nQAB 0,64,240,73,0\nQC 84,84,172\nQH 140,231,18432\nQC 88,72,168\nQDV 86\nQC 88,88,172\nQBB 88,250,85\nQC 92,76,168\nQC 92,92,172\nQG 4\nQC 64,96,176\n"
"QC 64,112,180\nQC 68,100,176\nQH 144,230,20480\nQC 68,116,180\nQS 88,251,85,1024\nQC 72,104,176\nQAO 85\nQC 72,120,180\nQH 148,231,20480\nQC 76,108,176\nQDW 86\nQC 76,124,180\nQC 80,96,184\n"
"QH 152,230,22528\nQC 80,112,188\nQBB 88,250,85\nQC 84,100,184\nQC 84,116,188\nQH 156,231,22528\nQC 88,104,184\nQCU 73,74\nQC 88,120,188\nQAB 4,64,240,73,1024\nQC 92,108,184\nQC 92,124,188\nQG 4\n"
"QC 64,128,128\nQS 88,251,85,1024\nQC 64,144,132\nQAO 85\nQC 68,132,128\nQH 160,230,24576\nQC 68,148,132\nQFJ 49\nQC 72,136,128\nQC 72,152,132\nQH 164,231,24576\nQC 76,140,128\nQL 36,249,48,0\n"
"QC 76,156,132\nQC 80,128,136\nQH 168,230,26624\nQC 80,144,140\nQC 84,132,136\nQC 84,148,140\nQH 172,231,26624\nQC 88,136,136\nQL 36,249,48,1024\nQC 88,152,140\nQC 92,140,136\nQC 92,156,140\nQG 4\n"
"QC 64,160,144\nQC 64,176,148\nQC 68,164,144\nQH 176,230,28672\nQC 68,180,148\nQCU 73,74\nQC 72,168,144\nQAB 8,64,240,73,2048\nQC 72,184,148\nQH 180,231,28672\nQC 76,172,144\nQL 36,249,48,2048\n"
"QC 76,188,148\nQC 80,160,152\nQH 184,230,30720\nQC 80,176,156\nQC 84,164,152\nQC 84,180,156\nQH 188,231,30720\nQC 88,168,152\nQL 36,249,48,3072\nQC 88,184,156\nQGH 49\nQC 92,172,152\nQAA 48\n"
"QC 92,188,156\nQG 4\nQC 64,192,160\nQC 64,208,164\nQC 68,196,160\nQL 36,249,48,0\nQC 68,212,164\nQBP 48\nQC 72,200,160\nQC 72,216,164\nQC 76,204,160\nQCU 73,74\nQC 76,220,164\nQAB 12,64,240,73,3072\n"
"QC 80,192,168\nQC 80,208,172\nQC 84,196,168\nQC 84,212,172\nQC 88,200,168\nQC 88,216,172\nQC 92,204,168\nQC 92,220,172\nQG 0\nQC 64,224,176\nQC 64,240,180\nQC 68,228,176\nQC 68,244,180\n"
"QC 72,232,176\nQC 72,248,180\nQC 76,236,176\nQC 76,252,180\nQC 80,224,184\nQC 80,240,188\nQC 84,228,184\nQC 84,244,188\nQC 88,232,184\nQC 88,248,188\nQC 92,236,184\nQC 92,252,188\nQK 64,78\nQK 65,78\n"
"QK 66,78\nQK 67,78\nQK 68,78\nQK 69,78\nQK 70,78\nQK 71,78\nQF 16,64,65\nQF 17,66,67\nQF 18,68,69\nQF 19,70,71\nQK 72,78\nQK 73,78\nQK 74,78\nQK 75,78\nQK 76,78\nQK 77,78\nQK 78,78\nQK 79,78\n"
"QF 20,72,73\nQF 21,74,75\nQF 22,76,77\nQF 23,78,79\nQK 80,78\nQK 81,78\nQK 82,78\nQK 83,78\nQK 84,78\nQK 85,78\nQK 86,78\nQK 87,78\nQF 24,80,81\nQF 25,82,83\nQF 26,84,85\nQF 27,86,87\nQK 88,78\n"
"QK 89,78\nQK 90,78\nQK 91,78\nQK 92,78\nQK 93,78\nQK 94,78\nQK 95,78\nQF 28,88,89\nQF 29,90,91\nQF 30,92,93\nQF 31,94,95\nQFK 73,74\nQAB 16,64,240,73,0\nQFK 73,74\nQAB 20,64,240,73,1024\nQFK 73,74\n"
"QAB 24,64,240,73,2048\nQFK 73,74\nQAB 28,64,240,73,3072\ns_branch L_kv_end\nL_kv_v:\nQBD \nQAM 23,0\ns_barrier\nQGS 98,126\nQE 0,224,0\nQE 2,224,2048\nQE 4,225,0\nQE 6,225,2048\nQE 8,226,0\n"
"QE 10,226,2048\nQE 12,227,0\nQE 14,227,2048\nQH 128,228,0\nQH 132,228,2048\nQH 136,228,4096\nQH 140,228,6144\nQE 16,224,4096\nQE 18,224,6144\nQE 20,225,4096\nQE 22,225,6144\nQE 24,226,4096\n"
"QE 26,226,6144\nQE 28,227,4096\nQE 30,227,6144\nQH 144,229,0\nQH 148,229,2048\nQH 152,229,4096\nQH 156,229,6144\nQBV 84\nQBE \nQGT 80,116\ns_add_u32 m0, s84, 1024\nQBE \nQGT 82,117\nQAM 15,0\n"
"s_barrier\nQAS 0,128,0\nQE 32,224,8192\nQD 0,144,16\nQE 34,224,10240\nQBV 69\nQAS 8,128,4\nQS 56,232,68,0\nQD 8,144,20\nQE 36,225,8192\nQAS 16,128,8\nQE 38,225,10240\nQD 16,144,24\nQS 56,233,68,1024\n"
"QAS 24,128,12\nQE 40,226,8192\nQD 24,144,28\nQE 42,226,10240\nQAS 4,132,0\nQS 56,234,68,2048\nQD 4,148,16\nQE 44,227,8192\nQAS 12,132,4\nQE 46,227,10240\nQD 12,148,20\nQS 56,235,68,3072\n"
"QAS 20,132,8\nQH 160,228,8192\nQD 20,148,24\nQH 164,228,10240\nQAO 68\nQBR 68\nQDD 79\nQBT 75,72,70\nQAS 28,132,12\nQS 60,236,75,0\nQD 28,148,28\nQH 168,228,12288\nQAS 32,136,0\nQH 172,228,14336\n"
"QD 32,152,16\nQS 60,237,75,1024\nQAS 40,136,4\nQH 176,229,8192\nQD 40,152,20\nQH 180,229,10240\nQCX 70\nQCY 70\nQCT 69\nQAS 48,136,8\nQS 56,232,68,0\nQD 48,152,24\nQH 184,229,12288\nQAS 56,136,12\n"
"QH 188,229,14336\nQD 56,152,28\nQS 56,233,68,1024\nQAS 36,140,0\nQE 48,224,12288\nQD 36,156,16\nQE 50,224,14336\nQAS 44,140,4\nQS 56,234,68,2048\nQD 44,156,20\nQE 52,225,12288\nQAS 52,140,8\n"
"QE 54,225,14336\nQD 52,156,24\nQS 56,235,68,3072\nQAS 60,140,12\nQE 56,226,12288\nQD 60,156,28\nQE 58,226,14336\nQE 60,227,12288\nQE 62,227,14336\nQAO 68\nQBR 68\nQCE 98\nQBJ 127,98\nQDX 92,127,97\n"
"QAM 16,0\ns_barrier\nQD 0,160,32\nQE 64,224,16384\nQD 0,176,48\nQE 66,224,18432\nQDV 69\nQD 8,160,36\nQS 56,232,68,0\nQD 8,176,52\nQE 68,225,16384\nQD 16,160,40\nQE 70,225,18432\nQD 16,176,56\n"
"QS 56,233,68,1024\nQD 24,160,44\nQE 72,226,16384\nQD 24,176,60\nQE 74,226,18432\nQD 4,164,32\nQS 56,234,68,2048\nQD 4,180,48\nQE 76,227,16384\nQD 12,164,36\nQE 78,227,18432\nQD 12,180,52\n"
"QS 56,235,68,3072\nQD 20,164,40\nQH 128,228,16384\nQD 20,180,56\nQH 132,228,18432\nQAO 68\nQBR 68\ns_add_u32 m0, s79, 98304\nQBT 75,72,70\nQD 28,164,44\nQS 60,236,75,0\nQD 28,180,60\n"
"QH 136,228,20480\nQD 32,168,32\nQH 140,228,22528\nQD 32,184,48\nQS 60,237,75,1024\nQD 40,168,36\nQH 144,229,16384\nQD 40,184,52\nQH 148,229,18432\nQCX 70\nQCY 70\ns_add_u32 m0, s69, 12288\n"
"QD 48,168,40\nQS 56,232,68,0\nQD 48,184,56\nQH 152,229,20480\nQD 56,168,44\nQH 156,229,22528\nQD 56,184,60\nQS 56,233,68,1024\nQD 36,172,32\nQE 80,224,20480\nQD 36,188,48\nQE 82,224,22528\n"
"QD 44,172,36\nQS 56,234,68,2048\nQD 44,188,52\nQE 84,225,20480\nQD 52,172,40\nQE 86,225,22528\nQD 52,188,56\nQS 56,235,68,3072\nQD 60,172,44\nQE 88,226,20480\nQD 60,188,60\nQE 90,226,22528\n"
"QE 92,227,20480\nQE 94,227,22528\nQAO 68\nQBR 68\nQAM 11,0\ns_barrier\nQD 0,128,64\nQE 96,224,0\nQD 0,144,80\nQE 98,224,2048\nQDW 69\nQD 8,128,68\nQS 56,232,68,0\nQD 8,144,84\nQE 100,225,0\n"
"QD 16,128,72\nQE 102,225,2048\nQD 16,144,88\nQS 56,233,68,1024\nQD 24,128,76\nQE 104,226,0\nQD 24,144,92\nQE 106,226,2048\nQD 4,132,64\nQS 56,234,68,2048\nQD 4,148,80\nQE 108,227,0\nQD 12,132,68\n"
"QE 110,227,2048\nQD 12,148,84\nQS 56,235,68,3072\nQD 20,132,72\nQH 160,228,24576\nQD 20,148,88\nQH 164,228,26624\nQAO 68\nQBR 68\nQDN 79\nQBT 75,72,70\nQD 28,132,76\nQS 60,236,75,0\nQD 28,148,92\n"
"QH 168,228,28672\nQD 32,136,64\nQH 172,228,30720\nQD 32,152,80\nQS 60,237,75,1024\nQD 40,136,68\nQH 176,229,24576\nQD 40,152,84\nQH 180,229,26624\nQCX 70\nQCY 70\nQEX 69\nQD 48,136,72\n"
"QS 56,232,68,0\nQD 48,152,88\nQH 184,229,28672\nQD 56,136,76\nQH 188,229,30720\nQD 56,152,92\nQS 56,233,68,1024\nQD 36,140,64\nQE 112,224,4096\nQD 36,156,80\nQE 114,224,6144\nQD 44,140,68\n"
"QS 56,234,68,2048\nQD 44,156,84\nQE 116,225,4096\nQD 52,140,72\nQE 118,225,6144\nQD 52,156,88\nQS 56,235,68,3072\nQD 60,140,76\nQE 120,226,4096\nQD 60,156,92\nQE 122,226,6144\nQE 124,227,4096\n"
"QE 126,227,6144\nQAO 68\nQBR 68\nQCE 98\nQBJ 127,98\nQDX 92,127,97\nQAM 11,0\ns_barrier\nQD 0,160,96\nQE 128,224,8192\nQD 0,176,112\nQE 130,224,10240\nQBV 69\nQD 8,160,100\nQS 56,232,68,0\n"
"QD 8,176,116\nQE 132,225,8192\nQD 16,160,104\nQE 134,225,10240\nQD 16,176,120\nQS 56,233,68,1024\nQD 24,160,108\nQE 136,226,8192\nQD 24,176,124\nQE 138,226,10240\nQD 4,164,96\nQS 56,234,68,2048\n"
"QD 4,180,112\nQE 140,227,8192\nQD 12,164,100\nQE 142,227,10240\nQD 12,180,116\nQS 56,235,68,3072\nQD 20,164,104\nQH 128,228,0\nQD 20,180,120\nQH 132,228,2048\nQAO 68\nQBR 68\n"
"s_add_u32 m0, s79, 114688\nQBT 75,72,70\nQD 28,164,108\nQS 60,236,75,0\nQD 28,180,124\nQH 136,228,4096\nQD 32,168,96\nQH 140,228,6144\nQD 32,184,112\nQS 60,237,75,1024\nQD 40,168,100\nQH 144,229,0\n"
"QD 40,184,116\nQH 148,229,2048\nQCX 70\nQCY 70\nQCT 69\nQD 48,168,104\nQS 56,232,68,0\nQD 48,184,120\nQH 152,229,4096\nQD 56,168,108\nQH 156,229,6144\nQD 56,184,124\nQS 56,233,68,1024\nQD 36,172,96\n"
"QE 144,224,12288\nQD 36,188,112\nQE 146,224,14336\nQD 44,172,100\nQS 56,234,68,2048\nQD 44,188,116\nQE 148,225,12288\nQD 52,172,104\nQE 150,225,14336\nQD 52,188,120\nQS 56,235,68,3072\nQD 60,172,108\n"
"QE 152,226,12288\nQD 60,188,124\nQE 154,226,14336\nQE 156,227,12288\nQE 158,227,14336\nQEY 84,118,0\nQEY 86,118,512\nQEY 88,118,1024\nQEY 90,118,1536\nQH 96,248,0\nQH 100,248,1024\nQH 104,248,2048\n"
"QH 108,248,3072\nQH 112,248,4096\nQAO 68\nQBR 68\nQAM 11,0\ns_barrier\nQD 0,128,128\nQE 160,224,16384\nQD 0,144,144\nQE 162,224,18432\nQDV 69\nQD 8,128,132\nQS 56,232,68,0\nQD 8,144,148\n"
"QE 164,225,16384\nQD 16,128,136\nQE 166,225,18432\nQD 16,144,152\nQS 56,233,68,1024\nQD 24,128,140\nQE 168,226,16384\nQD 24,144,156\nQE 170,226,18432\nQD 4,132,128\nQS 56,234,68,2048\nQD 4,148,144\n"
"QE 172,227,16384\nQD 12,132,132\nQE 174,227,18432\nQD 12,148,148\nQS 56,235,68,3072\nQD 20,132,136\nQH 160,228,8192\nQD 20,148,152\nQH 164,228,10240\nQAO 68\nQBR 68\nQDD 79\nQBT 75,72,70\n"
"QD 28,132,140\nQS 60,236,75,0\nQD 28,148,156\nQH 168,228,12288\nQD 32,136,128\nQH 172,228,14336\nQD 32,152,144\nQS 60,237,75,1024\nQD 40,136,132\nQH 176,229,8192\nQD 40,152,148\nQH 180,229,10240\n"
"QCX 70\nQCY 70\ns_add_u32 m0, s69, 12288\nQD 48,136,136\nQS 56,232,68,0\nQD 48,152,152\nQH 184,229,12288\nQD 56,136,140\nQH 188,229,14336\nQD 56,152,156\nQS 56,233,68,1024\nQD 36,140,128\n"
"QE 176,224,20480\nQD 36,156,144\nQE 178,224,22528\nQD 44,140,132\nQS 56,234,68,2048\nQD 44,156,148\nQE 180,225,20480\nQD 52,140,136\nQE 182,225,22528\nQD 52,156,152\nQS 56,235,68,3072\nQD 60,140,140\n"
"QE 184,226,20480\nQD 60,156,156\nQE 186,226,22528\nQE 188,227,20480\nQE 190,227,22528\nQAO 68\nQBR 68\nQCE 98\nQBJ 127,98\nQDX 92,127,97\nQAM 11,0\ns_barrier\nQD 0,160,160\nQE 192,224,0\n"
"QD 0,176,176\nQE 194,224,2048\ns_add_u32 m0, s71, 131072\nQDE 75,76\nQD 8,160,164\nQS 60,236,75,0\nQD 8,176,180\nQE 196,225,0\nQD 16,160,168\nQE 198,225,2048\nQD 16,176,184\nQS 60,237,75,1024\n"
"QD 24,160,172\nQE 200,226,0\nQD 24,176,188\nQE 202,226,2048\nQD 4,164,160\nQS 60,238,75,2048\nQD 4,180,176\nQE 204,227,0\nQD 12,164,164\nQE 206,227,2048\nQD 12,180,180\nQS 60,239,75,3072\n"
"QD 20,164,168\nQH 128,228,16384\nQD 20,180,184\nQH 132,228,18432\ns_add_u32 m0, s71, 135168\nQDE 75,77\nQD 28,164,172\nQS 60,236,75,0\nQD 28,180,188\nQH 136,228,20480\nQD 32,168,160\n"
"QH 140,228,22528\nQD 32,184,176\nQS 60,237,75,1024\nQD 40,168,164\nQH 144,229,16384\nQD 40,184,180\nQH 148,229,18432\nQD 48,168,168\nQS 60,238,75,2048\nQD 48,184,184\nQH 152,229,20480\nQD 56,168,172\n"
"QH 156,229,22528\nQD 56,184,188\nQS 60,239,75,3072\nQD 36,172,160\nQE 208,224,4096\nQD 36,188,176\nQE 210,224,6144\nQD 44,172,164\nQE 212,225,4096\nQD 44,188,180\nQE 214,225,6144\nQD 52,172,168\n"
"QE 216,226,4096\nQD 52,188,184\nQE 218,226,6144\nQD 60,172,172\nQE 220,227,4096\nQD 60,188,188\nQE 222,227,6144\nQAM 9,0\ns_barrier\nQD 0,128,192\nQE 224,224,8192\nQD 0,144,208\nQE 226,224,10240\n"
"QD 8,128,196\nQAB 96,88,249,49,0\nQD 8,144,212\nQE 228,225,8192\nQD 16,128,200\nQE 230,225,10240\nQD 16,144,216\nQAB 100,88,249,49,2048\nQD 24,128,204\nQE 232,226,8192\nQD 24,144,220\n"
"QE 234,226,10240\nQAA 49\nQD 4,132,192\nQAB 104,88,249,49,0\nQD 4,148,208\nQE 236,227,8192\nQD 12,132,196\nQE 238,227,10240\nQD 12,148,212\nQAB 108,88,249,49,2048\nQD 20,132,200\nQH 160,228,24576\n"
"QD 20,148,216\nQH 164,228,26624\nQAA 49\nQD 28,132,204\nQAB 112,88,249,49,0\nQD 28,148,220\nQH 168,228,28672\nQD 32,136,192\nQH 172,228,30720\nQD 32,152,208\n"
"buffer_store_dwordx4 v[84:87], v241, s[88:91], s86 offen sc1\nQD 40,136,196\nQH 176,229,24576\nQD 40,152,212\nQH 180,229,26624\nQD 48,136,200\n"
"buffer_store_dwordx4 v[88:91], v241, s[88:91], s87 offen sc1\nQD 48,152,216\nQH 184,229,28672\nQD 56,136,204\nQH 188,229,30720\nQD 56,152,220\nQE 240,224,12288\nQD 36,140,192\nQE 242,224,14336\n"
"QD 36,156,208\nQE 244,225,12288\nQD 44,140,196\nQE 246,225,14336\nQD 44,156,212\nQE 248,226,12288\nQD 52,140,200\nQE 250,226,14336\nQD 52,156,216\nQE 252,227,12288\nQD 60,140,204\nQE 254,227,14336\n"
"QD 60,156,220\nQAM 7,0\ns_barrier\nQD 0,160,224\nQH 128,230,0\nQD 0,176,240\nQH 132,231,0\nQBV 71\nQCU 75,76\nQD 8,160,228\nQS 60,236,75,0\nQD 8,176,244\nQH 136,230,2048\nQD 16,160,232\n"
"QH 140,231,2048\nQD 16,176,248\nQS 60,237,75,1024\nQD 24,160,236\nQH 144,230,4096\nQD 24,176,252\nQH 148,231,4096\nQD 4,164,224\nQS 60,238,75,2048\nQD 4,180,240\nQH 152,230,6144\nQD 12,164,228\n"
"QH 156,231,6144\nQD 12,180,244\nQS 60,239,75,3072\nQCT 71\nQCU 75,77\nQD 20,164,232\nQS 60,236,75,0\nQD 20,180,248\nQS 60,237,75,1024\nQD 28,164,236\nQS 60,238,75,2048\nQD 28,180,252\n"
"QS 60,239,75,3072\nQGK 99\nQD 32,168,224\nQCZ 92,127,99\nQD 32,184,240\nQD 40,168,228\nQD 40,184,244\nQD 48,168,232\nQD 48,184,248\nQD 56,168,236\nQD 56,184,252\nQD 36,172,224\nQD 36,188,240\n"
"QD 44,172,228\nQD 44,188,244\nQD 52,172,232\nQD 52,188,248\nQD 60,172,236\nQD 60,188,252\nQG 4\nQAS 192,128,0\nQD 192,132,16\nQAS 200,128,4\nQH 160,230,8192\nQD 200,132,20\nQDF 71\nQGO 75,76\n"
"QK 0,78\nQAS 208,128,8\nQS 60,236,75,0\nQD 208,132,24\nQH 164,231,8192\nQK 1,78\nQAS 216,128,12\nQK 2,78\nQK 3,78\nQD 216,132,28\nQK 4,78\nQK 5,78\nQAS 196,136,0\nQH 168,230,10240\nQK 6,78\n"
"QD 196,140,16\nQK 7,78\nQAY 0,1\nQAS 204,136,4\nQS 60,237,75,1024\nQD 204,140,20\nQH 172,231,10240\nQF 1,2,3\nQAS 212,136,8\nQF 2,4,5\nQF 3,6,7\nQD 212,140,24\nQK 8,78\nQK 9,78\nQAS 220,136,12\n"
"QS 60,238,75,2048\nQD 220,140,28\nQG 4\nQK 10,78\nQK 11,78\nQD 192,144,32\nQK 12,78\nQK 13,78\nQD 192,148,48\nQK 14,78\nQK 15,78\nQD 200,144,36\nQH 176,230,12288\nQF 4,8,9\nQD 200,148,52\nQF 5,10,11\n"
"QF 6,12,13\nQD 208,144,40\nQS 60,239,75,3072\nQD 208,148,56\nQH 180,231,12288\nQF 7,14,15\nQD 216,144,44\nQK 16,78\nQK 17,78\nQD 216,148,60\nQK 18,78\nQK 19,78\nQD 196,152,32\nQH 184,230,14336\n"
"QK 20,78\nQD 196,156,48\ns_add_u32 m0, s71, 36864\nQGO 75,77\nQK 21,78\nQD 204,152,36\nQS 60,236,75,0\nQD 204,156,52\nQH 188,231,14336\nQK 22,78\nQD 212,152,40\nQK 23,78\nQF 8,16,17\nQD 212,156,56\n"
"QF 9,18,19\nQF 10,20,21\nQD 220,152,44\nQS 60,237,75,1024\nQD 220,156,60\nQG 4\nQF 11,22,23\nQK 24,78\nQD 192,160,64\nQK 25,78\nQK 26,78\nQD 192,164,80\nQK 27,78\nQK 28,78\nQD 200,160,68\n"
"QH 128,230,16384\nQK 29,78\nQD 200,164,84\nQK 30,78\nQK 31,78\nQD 208,160,72\nQS 60,238,75,2048\nQD 208,164,88\nQH 132,231,16384\nQF 12,24,25\nQD 216,160,76\nQF 13,26,27\nQF 14,28,29\nQD 216,164,92\n"
"QF 15,30,31\nQK 32,78\nQD 196,168,64\nQH 136,230,18432\nQK 33,78\nQD 196,172,80\nQK 34,78\nQK 35,78\nQD 204,168,68\nQS 60,239,75,3072\nQD 204,172,84\nQH 140,231,18432\nQK 36,78\nQD 212,168,72\n"
"QEZ 73,74\nQCG 100\nL_pc7:\ns_lshr_b32 s99, %1, 2\nQGB 99\ns_bitcmp1_b32 %0, 0\nQGC 99\nQER 99\ns_min_u32 s99, s99, L_done-L_pc7-512\nQZ 100,99\nQAX 101\nQES 252,241\nQGD 100,250,252\nQD 212,172,88\n"
"QAB 0,64,240,73,0\nQD 220,168,76\nQK 37,78\nQK 38,78\nQD 220,172,92\nQG 4\nQK 39,78\nQF 16,32,33\nQD 192,176,96\nQF 17,34,35\nQF 18,36,37\nQD 192,180,112\nQF 19,38,39\nQK 40,78\nQD 200,176,100\n"
"QH 144,230,20480\nQK 41,78\nQD 200,180,116\nQK 42,78\nQK 43,78\nQD 208,176,104\nQK 44,78\nQK 45,78\nQD 208,180,120\nQH 148,231,20480\nQK 46,78\nQD 216,176,108\nQK 47,78\nQF 20,40,41\nQD 216,180,124\n"
"QF 21,42,43\nQF 22,44,45\nQD 196,184,96\nQH 152,230,22528\nQF 23,46,47\nQD 196,188,112\nQEZ 73,74\nQK 48,78\nQK 49,78\nQD 204,184,100\nQAB 4,64,240,73,1024\nQD 204,188,116\nQH 156,231,22528\n"
"QK 50,78\nQD 212,184,104\nQK 51,78\nQK 52,78\nQD 212,188,120\nQK 53,78\nQK 54,78\nQD 220,184,108\nQK 55,78\nQF 24,48,49\nQD 220,188,124\nQG 4\nQF 25,50,51\nQF 26,52,53\nQD 192,128,128\nQF 27,54,55\n"
"QK 56,78\nQD 192,132,144\nQK 57,78\nQK 58,78\nQD 200,128,132\nQH 160,230,24576\nQK 59,78\nQD 200,132,148\nQK 60,78\nQK 61,78\nQD 208,128,136\nQK 62,78\nQK 63,78\nQD 208,132,152\nQH 164,231,24576\n"
"QF 28,56,57\nQD 216,128,140\nQF 29,58,59\nQF 30,60,61\nQD 216,132,156\nQF 31,62,63\nQEG 50,44\nQD 196,136,128\nQH 168,230,26624\ns_lshl_b32 s85, s44, 5\nQD 196,140,144\nQEZ 73,74\n"
"s_mov_b64 s[52:53], %5\nQGR 54\nQBA 55\ns_mul_i32 s50, s46, 80\nQD 204,136,132\nQAB 8,64,240,73,2048\nQD 204,140,148\nQH 172,231,26624\ns_mul_i32 s51, s44, 20\nQD 212,136,136\nQZ 50,51\n"
"s_lshl_b32 s50, s50, 13\ns_lshl_b32 s51, s45, 8\nQD 212,140,152\nQZ 50,51\nQDS 124,241\nv_min_u32 v124, 19, v124\nQD 220,136,140\nv_lshlrev_b32 v124, 13, v124\nQGU 122,241\nQD 220,140,156\nQG 4\n"
"QFX 124,122\nQD 192,144,160\nv_lshrrev_b32 v96, 6, v241\nv_cmp_le_u32 s[80:81], 1, v96\nQD 192,148,176\nv_cmp_le_u32 s[82:83], 2, v96\nv_cmp_le_u32 s[84:85], 5, v96\nQD 200,144,164\nQH 176,230,28672\n"
"v_cmp_le_u32 s[86:87], 10, v96\nQD 200,148,180\ns_mov_b64 s[88:89], %9\nQBJ 98,88\nQBJ 99,89\nQD 208,144,168\ns_mov_b64 s[88:89], %10\nQBJ 115,88\nQBJ 116,89\nQD 208,148,184\nQH 180,231,28672\n"
"QAL 80,98,115\nQD 216,144,172\nQAL 80,99,116\ns_mov_b64 s[88:89], %11\nQD 216,148,188\nQBJ 115,88\nQBJ 116,89\nQD 196,152,160\nQH 184,230,30720\nQAL 82,98,115\nQD 196,156,176\nQEZ 73,74\n"
"QAL 82,99,116\ns_mov_b64 s[88:89], %13\nQD 204,152,164\nQAB 12,64,240,73,3072\nQD 204,156,180\nQH 188,231,30720\nQBJ 115,88\nQD 212,152,168\nQBJ 116,89\nQAL 84,98,115\nQD 212,156,184\nQAL 84,99,116\n"
"s_mov_b64 s[88:89], %12\nQD 220,152,172\nQBJ 115,88\nQBJ 116,89\nQD 220,156,188\nQG 4\nQAL 86,98,115\nQAL 86,99,116\nQD 192,160,192\nQAM 13,0\ns_barrier\nv_xor_b32 v242, v127, v126\nQBE \n"
"QGS 100,242\nQBE \nQBZ 100\ns_cbranch_scc1 L_ps8\nL_pr8:\nQCR 244\ns_bfe_u32 s99, %1, 262144\nQBU 99\ns_add_u32 s99, s99, 0\nQZ 99,96\nQEI \nQEJ 92,244,99\nQEK \nQD 192,164,208\n"
"v_add_u32 v230, 4294836224, v230\nv_add_u32 v231, 4294836224, v231\nQD 200,160,196\nQH 128,230,0\nQAG 97\nQD 200,164,212\nQCR 115\nQAL 80,97,115\nQD 208,160,200\nv_mov_b32 v115, 2\nQAL 82,97,115\n"
"QD 208,164,216\nQH 132,231,0\nQFA 115\nQD 216,160,204\nQAL 84,97,115\nQFB 115\nQD 216,164,220\nQAL 86,97,115\nv_mov_b32 v109, 200\nQD 196,168,192\nQH 136,230,2048\nv_mov_b32 v115, 540\n"
"QD 196,172,208\nQFC 73,74\nQAL 82,109,115\nQGV 115\nQD 204,168,196\nQAB 16,64,240,73,0\nQD 204,172,212\nQH 140,231,2048\nQAL 84,109,115\nQD 212,168,200\nv_mov_b32 v115, 540\nQAL 86,109,115\n"
"QD 212,172,216\nQGV 100\nv_mov_b32 v115, 3456\nQD 220,168,204\nQAL 82,100,115\nQGV 115\nQD 220,172,220\nQG 4\nQAL 84,100,115\nQFB 101\nQD 192,176,224\nv_mov_b32 v115, 27\nQAL 82,101,115\n"
"QD 192,180,240\nQFB 115\nQAL 84,101,115\nQD 200,176,228\nQH 144,230,4096\nQGW 102\nQD 200,180,244\nv_mov_b32 v115, 2428\nQAL 82,102,115\nQD 208,176,232\nQGW 115\nQAL 84,102,115\nQD 208,180,248\n"
"QH 148,231,4096\nQCR 103\nQD 216,176,236\nQFA 115\nQAL 80,103,115\nQD 216,180,252\nQCR 115\nQAL 84,103,115\nQD 196,184,224\nQH 152,230,6144\nv_mov_b32 v115, 7\nQD 196,188,240\nQAL 86,103,115\n"
"v_mov_b32 v104, 65536\nQD 204,184,228\nQGX 115\nQAL 80,104,115\nQD 204,188,244\nQH 156,231,6144\nv_mov_b32 v115, 65536\nQD 212,184,232\nQAL 84,104,115\nv_mov_b32 v115, 9363\nQD 212,188,248\n"
"QAL 86,104,115\nQFA 105\nQD 220,184,236\nv_mov_b32 v115, 7\nQAL 82,105,115\nQD 220,188,252\nQG 4\nQFB 115\nQAL 84,105,115\nQAS 64,128,0\nQD 64,132,16\nQAS 72,128,4\nQH 160,230,8192\nQD 72,132,20\n"
"QDY 71\ns_add_u32 s75, s76, 163840\nQK 192,78\nQAS 80,128,8\nQS 60,236,75,0\nQD 80,132,24\nQH 164,231,8192\nQK 193,78\nQAS 88,128,12\nQK 194,78\nQK 195,78\nQD 88,132,28\nQK 196,78\nQK 197,78\n"
"QAS 68,136,0\nQH 168,230,10240\nQK 198,78\nQD 68,140,16\nQK 199,78\nQF 0,192,193\nQAS 76,136,4\nQS 60,237,75,1024\nQD 76,140,20\nQH 172,231,10240\nQF 1,194,195\nQAS 84,136,8\nQF 2,196,197\n"
"QF 3,198,199\nQD 84,140,24\nQK 200,78\nQK 201,78\nQAS 92,136,12\nQS 60,238,75,2048\nQD 92,140,28\nQG 4\nQK 202,78\nQK 203,78\nQD 64,144,32\nQK 204,78\nQK 205,78\nQD 64,148,48\nQK 206,78\nQK 207,78\n"
"QD 72,144,36\nQH 176,230,12288\nQF 4,200,201\nQD 72,148,52\nQF 5,202,203\nQF 6,204,205\nQD 80,144,40\nQS 60,239,75,3072\nQD 80,148,56\nQH 180,231,12288\nQF 7,206,207\nQD 88,144,44\nQK 208,78\n"
"QK 209,78\nQD 88,148,60\nQK 210,78\nQK 211,78\nQD 68,152,32\nQH 184,230,14336\nQK 212,78\nQD 68,156,48\ns_add_u32 m0, s71, 69632\ns_add_u32 s75, s77, 163840\nQK 213,78\nQD 76,152,36\nQS 60,236,75,0\n"
"QD 76,156,52\nQH 188,231,14336\nQK 214,78\nQD 84,152,40\nQK 215,78\nQF 8,208,209\nQD 84,156,56\nQF 9,210,211\nQF 10,212,213\nQD 92,152,44\nQS 60,237,75,1024\nQD 92,156,60\nQG 4\nQF 11,214,215\n"
"QK 216,78\nQD 64,160,64\nQK 217,78\nQK 218,78\nQD 64,164,80\nQK 219,78\nQK 220,78\nQD 72,160,68\nQH 128,230,16384\nQK 221,78\nQD 72,164,84\nQK 222,78\nQK 223,78\nQD 80,160,72\nQS 60,238,75,2048\n"
"QD 80,164,88\nQH 132,231,16384\nQF 12,216,217\nQD 88,160,76\nQF 13,218,219\nQF 14,220,221\nQD 88,164,92\nQF 15,222,223\nQFA 115\nQD 68,168,64\nQH 136,230,18432\nQAL 86,105,115\nQD 68,172,80\nQGX 106\n"
"v_mov_b32 v115, 9363\nQD 76,168,68\nQS 60,239,75,3072\nQD 76,172,84\nQH 140,231,18432\nQAL 82,106,115\nQD 84,168,72\nQFC 73,74\nQGW 115\nQAL 84,106,115\nQD 84,172,88\nQAB 20,64,240,73,1024\n"
"QD 92,168,76\nQGX 115\nQAL 86,106,115\nQD 92,172,92\nQG 4\nv_mov_b32 v107, 20\nv_mov_b32 v115, 128\nQD 64,176,96\nQAL 84,107,115\nv_mov_b32 v115, 56\nQD 64,180,112\nQAL 86,107,115\nQAG 108\n"
"QD 72,176,100\nQH 144,230,20480\nv_mov_b32 v115, 819200\nQD 72,180,116\nQAL 80,108,115\nv_mov_b32 v115, 1638400\nQD 80,176,104\nQAL 82,108,115\nv_mov_b32 v115, 6225920\nQD 80,180,120\n"
"QH 148,231,20480\nQAL 84,108,115\nQD 88,176,108\nv_mov_b32 v115, 3932160\nQAL 86,108,115\nQD 88,180,124\nv_sub_u32 v97, v96, v97\nv_lshlrev_b32 v97, 8, v97\nQD 68,184,96\nQH 152,230,22528\n"
"v_add_u32 v97, %1, v97\nQD 68,188,112\nQFC 73,74\nv_cmp_lt_u32 s[88:89], v97, v109\nv_mov_b32 v115, 0x40000000\nQD 76,184,100\nQAB 24,64,240,73,2048\nQD 76,188,116\nQH 156,231,22528\n"
"v_cndmask_b32 v108, v115, v108, s[88:89]\nQD 84,184,104\nv_cndmask_b32 v97, 0, v97, s[88:89]\nQDZ 110,97,102\nQD 84,188,120\nQDA 110\nQDZ 115,110,101\nQD 92,184,108\nQFD 111,97,115\nQDZ 115,110,104\n"
"QD 92,188,124\nQG 4\nQDA 115\nv_mul_u32_u24 v115, v115, v103\nQD 64,128,128\nQFD 112,110,115\nv_cmp_gt_u32 vcc, 42, v110\nQD 64,132,144\nQBE \nv_cndmask_b32 v112, 0, v112, vcc\n"
"v_max_u32 v103, 3, v103\nQD 72,128,132\nQH 160,230,24576\nv_add_u32 v103, -1, v103\nQD 72,132,148\nv_lshlrev_b32 v113, 1, v110\nQCL 115,103\nQD 80,128,136\nv_cmp_ge_u32 vcc, v112, v115\n"
"QFD 116,113,103\nQD 80,132,152\nQH 164,231,24576\nv_cndmask_b32 v115, 0, 1, vcc\nQD 88,128,140\nQDM 113,115\nv_cmp_eq_u32 vcc, v112, v103\nQD 88,132,156\nv_lshlrev_b32 v117, 2, v111\n"
"v_add_u32 v117, %0, v117\nQD 68,136,128\nQH 168,230,26624\nQDG 113,116\nQD 68,140,144\nQFC 73,74\nv_cndmask_b32 v114, 0, v103, vcc\nv_mul_u32_u24 v113, v113, v100\nQD 76,136,132\n"
"QAB 28,64,240,73,3072\nQD 76,140,148\nQH 172,231,26624\nQDT 113\nQD 84,136,136\nv_lshl_add_u32 v113, v117, 5, v113\nv_add_co_u32 v98, vcc, v98, v113\nQD 84,140,152\nQBE \n"
"v_addc_co_u32 v99, vcc, 0, v99, vcc\nv_lshl_add_u32 v100, v114, 16, v100\nQD 92,136,140\nQDZ 118,117,106\nQDA 118\nQD 92,140,156\nQG 4\nQDZ 115,118,105\nQFD 111,117,115\nQD 64,144,160\n"
"v_subrev_u32 v115, 84, v117\nv_mul_u32_u24 v116, 10923, v115\nQD 64,148,176\nQDA 116\nv_mul_u32_u24 v112, 6, v116\nQD 72,144,164\nQH 176,230,28672\nv_sub_u32 v115, v115, v112\nQD 72,148,180\n"
"v_add_u32 v116, 12, v116\nv_cmp_lt_u32 vcc, 83, v117\nQD 80,144,168\nQBE \nQDG 118,116\nQDG 111,115\nQD 80,148,184\nQH 180,231,28672\nv_mad_u32_u24 v118, v118, v107, v110\nQD 88,144,172\n"
"v_mad_u32_u24 v118, v118, v105, v111\nv_lshl_add_u32 v108, v118, 10, v108\nQD 88,148,188\nv_bfe_u32 v115, v241, 4, 2\nv_cmp_eq_u32 vcc, 1, v115\nQD 68,152,160\nQH 184,230,30720\n"
"v_cndmask_b32 v254, v98, v99, vcc\nQD 68,156,176\nQEA 73,74\nv_cmp_eq_u32 vcc, 2, v115\nQBD \nQDG 254,100\nQD 76,152,164\nQAB 0,64,240,73,0\nQD 76,156,180\nQH 188,231,30720\n"
"v_cmp_eq_u32 vcc, 3, v115\nQD 84,152,168\nQBE \nQDG 254,108\nQD 84,156,184\nQD 92,152,172\nQD 92,156,188\nQG 4\nQD 64,160,192\nQAM 19,0\ns_barrier\nQD 64,164,208\nQDH 230\nQDH 231\nQD 72,160,196\n"
"QH 128,230,0\nQD 72,164,212\nQD 80,160,200\nQD 80,164,216\nQH 132,231,0\nQD 88,160,204\nQD 88,164,220\nQD 68,168,192\nQH 136,230,2048\nQD 68,172,208\nQEA 73,74\nQD 76,168,196\nQAB 4,64,240,73,1024\n"
"QD 76,172,212\nQH 140,231,2048\nQD 84,168,200\nQD 84,172,216\nQD 92,168,204\nQD 92,172,220\nQG 4\nQD 64,176,224\nQD 64,180,240\nQD 72,176,228\nQH 144,230,4096\nQD 72,180,244\nQD 80,176,232\n"
"QD 80,180,248\nQH 148,231,4096\nQD 88,176,236\nQD 88,180,252\nQD 68,184,224\nQH 152,230,6144\nQD 68,188,240\nQD 76,184,228\nQD 76,188,244\nQH 156,231,6144\nQD 84,184,232\nQD 84,188,248\n"
"QD 92,184,236\nQD 92,188,252\nQG 4\nQAS 192,128,0\nQD 192,132,16\nQAS 200,128,4\nQH 160,230,8192\nQD 200,132,20\nQBV 71\ns_add_u32 s75, s76, 196608\nQK 64,78\nQAS 208,128,8\nQS 60,236,75,0\n"
"QD 208,132,24\nQH 164,231,8192\nQK 65,78\nQAS 216,128,12\nQK 66,78\nQK 67,78\nQD 216,132,28\nQK 68,78\nQK 69,78\nQAS 196,136,0\nQH 168,230,10240\nQK 70,78\nQD 196,140,16\nQK 71,78\nQF 16,64,65\n"
"QAS 204,136,4\nQS 60,237,75,1024\nQD 204,140,20\nQH 172,231,10240\nQF 17,66,67\nQAS 212,136,8\nQF 18,68,69\nQF 19,70,71\nQD 212,140,24\nQK 72,78\nQK 73,78\nQAS 220,136,12\nQS 60,238,75,2048\n"
"QD 220,140,28\nQG 4\nQK 74,78\nQK 75,78\nQD 192,144,32\nQK 76,78\nQK 77,78\nQD 192,148,48\nQK 78,78\nQK 79,78\nQD 200,144,36\nQH 176,230,12288\nQF 20,72,73\nQD 200,148,52\nQF 21,74,75\nQF 22,76,77\n"
"QD 208,144,40\nQS 60,239,75,3072\nQD 208,148,56\nQH 180,231,12288\nQF 23,78,79\nQD 216,144,44\nQK 80,78\nQK 81,78\nQD 216,148,60\nQK 82,78\nQK 83,78\nQD 196,152,32\nQH 184,230,14336\nQK 84,78\n"
"QD 196,156,48\nQCT 71\ns_add_u32 s75, s77, 196608\nQK 85,78\nQD 204,152,36\nQS 60,236,75,0\nQD 204,156,52\nQH 188,231,14336\nQK 86,78\nQD 212,152,40\nQK 87,78\nQF 24,80,81\nQD 212,156,56\n"
"QF 25,82,83\nQF 26,84,85\nQD 220,152,44\nQS 60,237,75,1024\nQD 220,156,60\nQG 4\nQF 27,86,87\nQK 88,78\nQD 192,160,64\nQK 89,78\nQK 90,78\nQD 192,164,80\nQK 91,78\nQK 92,78\nQD 200,160,68\n"
"QH 128,230,16384\nQK 93,78\nQD 200,164,84\nQK 94,78\nQK 95,78\nQD 208,160,72\nQS 60,238,75,2048\nQD 208,164,88\nQH 132,231,16384\nQF 28,88,89\nQD 216,160,76\nQF 29,90,91\nQF 30,92,93\nQD 216,164,92\n"
"QF 31,94,95\nQD 196,168,64\nQH 136,230,18432\nQD 196,172,80\nQD 204,168,68\nQS 60,239,75,3072\nQD 204,172,84\nQH 140,231,18432\nQD 212,168,72\nQD 212,172,88\n"
"buffer_load_dword v123, v124, s[52:55], s50 offen offset:0\nQD 220,168,76\nQD 220,172,92\nQG 4\nQD 192,176,96\nQEA 73,74\nQD 192,180,112\nQAB 8,64,240,73,2048\nQD 200,176,100\nQH 144,230,20480\n"
"QD 200,180,116\nQD 208,176,104\nQD 208,180,120\nQH 148,231,20480\nQD 216,176,108\nQD 216,180,124\nQD 196,184,96\nQH 152,230,22528\nQD 196,188,112\nQEA 73,74\nQD 204,184,100\nQAB 12,64,240,73,3072\n"
"QD 204,188,116\nQH 156,231,22528\nQD 212,184,104\nQD 212,188,120\nQD 220,184,108\nQD 220,188,124\nQG 4\nQD 192,128,128\nQD 192,132,144\nQD 200,128,132\nQH 160,230,24576\nQD 200,132,148\n"
"QD 208,128,136\nQD 208,132,152\nQH 164,231,24576\nQD 216,128,140\nQD 216,132,156\nQD 196,136,128\nQH 168,230,26624\nQD 196,140,144\nQEL 73,74\nQD 204,136,132\nQAB 16,64,240,73,0\nQD 204,140,148\n"
"QH 172,231,26624\nQD 212,136,136\nQD 212,140,152\nQD 220,136,140\nQD 220,140,156\nQG 4\nQD 192,144,160\nQD 192,148,176\nQD 200,144,164\nQH 176,230,28672\nQD 200,148,180\nQD 208,144,168\n"
"QD 208,148,184\nQH 180,231,28672\nQD 216,144,172\nQD 216,148,188\nQD 196,152,160\nQH 184,230,30720\nQD 196,156,176\nQEL 73,74\nQD 204,152,164\nQAB 20,64,240,73,1024\nQD 204,156,180\nQH 188,231,30720\n"
"QD 212,152,168\nQD 212,156,184\nQD 220,152,172\nQD 220,156,188\nQG 4\nQD 192,160,192\nQAM 18,0\ns_barrier\nQD 192,164,208\nQDH 230\nQDH 231\nQD 200,160,196\nQH 128,230,0\nQD 200,164,212\n"
"QD 208,160,200\nQD 208,164,216\nQH 132,231,0\nQD 216,160,204\nQD 216,164,220\nQD 196,168,192\nQH 136,230,2048\nQD 196,172,208\nQEL 73,74\nQD 204,168,196\nQAB 24,64,240,73,2048\nQD 204,172,212\n"
"QH 140,231,2048\nQD 212,168,200\nQCG 36\nL_pc9:\ns_lshr_b32 s38, %1, 3\nQBH 38\ns_add_u32 s38, s38, %0\ns_mul_i32 s38, s38, ((L_done-L_roleS-1024)/127+63)&-64\n"
"s_min_u32 s38, s38, L_done-L_roleS-1024\ns_add_u32 s38, s38, L_roleS-L_pc9\nQZ 36,38\nQAX 37\nglobal_load_dwordx4 v[120:123], v241, s[36:37]\nQD 212,172,216\nQD 220,168,204\nQD 220,172,220\nQG 4\n"
"QD 192,176,224\nQD 192,180,240\nQD 200,176,228\nQH 144,230,4096\nQD 200,180,244\nQD 208,176,232\nQD 208,180,248\nQH 148,231,4096\nQD 216,176,236\nQD 216,180,252\nQD 196,184,224\nQH 152,230,6144\n"
"QD 196,188,240\nQD 204,184,228\nQD 204,188,244\nQH 156,231,6144\nQD 212,184,232\nQD 212,188,248\nQD 220,184,236\nQD 220,188,252\nQG 4\nQAS 64,128,0\nQD 64,132,16\nQAS 72,128,4\nQH 160,230,8192\n"
"QD 72,132,20\nQDF 71\ns_add_u32 s75, s76, 229376\nQK 192,78\nQAS 80,128,8\nQS 60,236,75,0\nQD 80,132,24\nQH 164,231,8192\nQK 193,78\nQAS 88,128,12\nQCV 243,241\nQCW 243\nQDC 99\nQD 88,132,28\n"
"QEB 92,242,243,99\nQAS 68,136,0\nQH 168,230,10240\nQK 194,78\nQD 68,140,16\nQK 195,78\nQK 196,78\nQAS 76,136,4\nQS 60,237,75,1024\nQD 76,140,20\nQH 172,231,10240\nQK 197,78\nQAS 84,136,8\nQK 198,78\n"
"QK 199,78\nQD 84,140,24\nQF 0,192,193\nQF 1,194,195\nQAS 92,136,12\nQS 60,238,75,2048\nQD 92,140,28\nQG 4\nQF 2,196,197\nQF 3,198,199\nQD 64,144,32\nQK 200,78\nQK 201,78\nQD 64,148,48\nQK 202,78\n"
"QK 203,78\nQD 72,144,36\nQH 176,230,12288\nQK 204,78\nQD 72,148,52\nQK 205,78\nQK 206,78\nQD 80,144,40\nQS 60,239,75,3072\nQD 80,148,56\nQH 180,231,12288\nQK 207,78\nQD 88,144,44\nQF 4,200,201\n"
"QF 5,202,203\nQD 88,148,60\nQF 6,204,205\nQF 7,206,207\nQD 68,152,32\nQH 184,230,14336\nQK 208,78\nQD 68,156,48\ns_add_u32 m0, s71, 36864\ns_add_u32 s75, s77, 229376\nQK 209,78\nQD 76,152,36\n"
"QS 60,236,75,0\nQD 76,156,52\nQH 188,231,14336\nQK 210,78\nQD 84,152,40\nQK 211,78\nQK 212,78\nQD 84,156,56\nQK 213,78\nQK 214,78\nQD 92,152,44\nQS 60,237,75,1024\nQD 92,156,60\nQG 4\nQK 215,78\n"
"QF 8,208,209\nQD 64,160,64\nQF 9,210,211\nQF 10,212,213\nQD 64,164,80\nQF 11,214,215\nQK 216,78\nQD 72,160,68\nQH 128,230,16384\nQK 217,78\nQD 72,164,84\nQK 218,78\nQK 219,78\nQD 80,160,72\n"
"QS 60,238,75,2048\nQD 80,164,88\nQH 132,231,16384\nQK 220,78\nQD 88,160,76\nQK 221,78\nQK 222,78\nQD 88,164,92\nQK 223,78\nQF 12,216,217\nQD 68,168,64\nQH 136,230,18432\nQF 13,218,219\nQD 68,172,80\n"
"QF 14,220,221\nQF 15,222,223\nQD 76,168,68\nQS 60,239,75,3072\nQD 76,172,84\nQH 140,231,18432\ns_mov_b32 s80, %0\nQD 84,168,72\nQEL 73,74\ns_and_b32 s83, %1, 7\ns_lshr_b32 s84, %1, 3\nQD 84,172,88\n"
"QAB 28,64,240,73,3072\nQD 92,168,76\ns_and_b32 s81, s84, 7\ns_lshr_b32 s84, s84, 3\nQEG 82,83\nQD 92,172,92\nQG 4\nQZ 82,84\ns_mov_b64 s[88:89], %5\nQD 64,176,96\ns_lshl_b32 s83, s82, 8\nQZ 88,83\n"
"QAX 89\ns_sub_u32 s88, s88, 1024\ns_subb_u32 s89, s89, 0\nQD 64,180,112\ns_sub_u32 s90, 5243904, s83\nQBA 91\nQGY 83,81\nQD 72,176,100\nQH 144,230,20480\nQFY 80\ns_cselect_b32 s84, s83, s81\n"
"QD 72,180,116\nQCK 84\nQBX 85,80\nQBH 85\nQD 80,176,104\nQZ 84,85\nQGE 87,84\nv_lshrrev_b32 v252, 7, v241\nQD 80,180,120\nQH 148,231,20480\nQFE 83,80\nQD 88,176,108\nQBM 253,83,252\n"
"v_lshlrev_b32 v253, 13, v253\nQD 88,180,124\nQEV 252\nv_lshlrev_b32 v252, 5, v252\nQD 68,184,96\nQH 152,230,22528\nv_and_b32 v250, 0x70, v241\nQD 68,188,112\nQDE 73,74\nQET 252,250\nQDM 253,252\n"
"QD 76,184,100\nQAB 0,64,240,73,0\nQD 76,188,116\nQH 156,231,22528\nv_add_u32 v250, 1024, v253\nQD 84,184,104\nv_add_u32 v251, 128, v253\nQFZ 86,80\nQD 84,188,120\nQDC 85\ns_bfe_u32 s55, %1, 196611\n"
"s_bfe_u32 s54, %1, 131072\nQD 92,184,108\nQBH 54\ns_bfe_u32 s83, %1, 131078\nQZ 54,83\nQD 92,188,124\nQG 4\nQBH 54\ns_add_u32 s54, s54, %0\nQD 64,128,128\ns_lshl_b32 s54, s54, 1\nQCJ 83,55\n"
"s_mul_i32 s83, s83, 5120\nQD 64,132,144\nQGE 50,54\nQBT 52,50,83\nQBX 84,55\nQD 72,128,132\nQH 160,230,24576\nQFE 83,84\nQD 72,132,148\nQBT 53,52,83\nQAA 53\nQFZ 83,84\nQD 80,128,136\nQZ 52,83\n"
"s_mov_b64 s[36:37], %2\ns_add_u32 s36, s36, 68157440\nQAX 37\nQD 80,132,152\nQH 164,231,24576\ns_mov_b32 s38, 1638400\nQD 88,128,140\nQBA 39\ns_mov_b64 s[40:41], %2\nQGN 40\nQAX 41\nQD 88,132,156\n"
"QGR 42\nQBA 43\ns_add_u32 s50, s50, 2621440\nQD 68,136,128\nQH 168,230,26624\ns_mov_b32 s44, 4096\nQD 68,140,144\nQDE 73,74\ns_mul_i32 s49, %0, 5120\ns_mul_i32 s48, s55, 102400\nQD 76,136,132\n"
"QAB 4,64,240,73,1024\nQD 76,140,148\nQH 172,231,26624\ns_bitcmp1_b32 %1, 2\ns_cselect_b32 s83, 819200, 0\nQBI 50,87\ns_cselect_b32 s44, 40960, s44\nQD 84,136,136\nQZ 48,83\ns_add_u32 s51, s50, 20480\n"
"QBM 249,49,241\nQD 84,140,152\nQD 92,136,140\nQD 92,140,156\nQG 4\nQD 64,144,160\nQAH 10\nQFF 242\nQBD \ns_cbranch_vccnz L_ps10\nL_pr10:\nQD 64,148,176\nQGF 49\nQD 72,144,164\nQH 176,230,28672\n"
"QD 72,148,180\nQL 36,249,48,0\nQD 80,144,168\nQD 80,148,184\nQH 180,231,28672\nQD 88,144,172\nQD 88,148,188\nQD 68,152,160\nQH 184,230,30720\nQD 68,156,176\nQDE 73,74\nQD 76,152,164\n"
"QAB 8,64,240,73,2048\nQD 76,156,180\nQH 188,231,30720\nQD 84,152,168\nQL 36,249,48,1024\nQD 84,156,184\nQD 92,152,172\nQD 92,156,188\nQG 4\nQD 64,160,192\nQAM 13,0\ns_barrier\nQD 64,164,208\n"
"v_add_u32 v230, 4294901760, v230\nv_add_u32 v231, 4294901760, v231\nQD 72,160,196\nQH 128,230,0\nQD 72,164,212\nQL 36,249,48,2048\nQD 80,160,200\nQD 80,164,216\nQH 132,231,0\nQD 88,160,204\n"
"QD 88,164,220\nQD 68,168,192\nQH 136,230,2048\nQD 68,172,208\nQDE 73,74\nQD 76,168,196\nQAB 12,64,240,73,3072\nQD 76,172,212\nQH 140,231,2048\nQD 84,168,200\nQL 36,249,48,3072\nQD 84,172,216\nQDN 49\n"
"QD 92,168,204\nQAA 48\nQD 92,172,220\nQG 4\nQD 64,176,224\nQD 64,180,240\nQD 72,176,228\nQH 144,230,4096\nQD 72,180,244\nQL 36,249,48,0\nQD 80,176,232\nQBP 48\nQD 80,180,248\nQH 148,231,4096\n"
"QD 88,176,236\nQD 88,180,252\nQD 68,184,224\nQH 152,230,6144\nQD 68,188,240\nQAW 96,40,241,50,0\nQD 76,184,228\nQD 76,188,244\nQH 156,231,6144\nQD 84,184,232\nQD 84,188,248\nQD 92,184,236\n"
"QD 92,188,252\nQG 4\nQAS 192,128,0\nQAW 100,40,241,51,0\nQD 192,132,16\nQAS 200,128,4\nQH 160,230,8192\nQD 200,132,20\nQK 64,78\nQK 65,78\nQAS 208,128,8\nQK 66,78\nQK 67,78\nQD 208,132,24\n"
"QH 164,231,8192\nQK 68,78\nQAS 216,128,12\nQAW 104,40,241,50,1024\nQD 216,132,28\nQK 69,78\nQK 70,78\nQAS 196,136,0\nQH 168,230,10240\nQK 71,78\nQD 196,140,16\nQF 16,64,65\nQF 17,66,67\n"
"QAS 204,136,4\nQF 18,68,69\nQF 19,70,71\nQD 204,140,20\nQH 172,231,10240\nQK 72,78\nQAS 212,136,8\nQCG 68\nL_pc11:\ns_lshr_b32 s70, %1, 3\nQBH 70\ns_add_u32 s70, s70, %0\n"
"s_mul_i32 s70, s70, ((L_att-L_kv_end-1024)/127+63)&-64\ns_min_u32 s70, s70, L_att-L_kv_end-1024\ns_add_u32 s70, s70, L_kv_end-L_pc11\nQZ 68,70\nQAX 69\nQCL 246,241\nQFG 244,68,246\nQD 212,140,24\n"
"QAW 108,40,241,51,1024\nQAS 220,136,12\nQK 73,78\nQK 74,78\nQD 220,140,28\nQG 4\nQK 75,78\nQK 76,78\nQD 192,144,32\nQK 77,78\nQK 78,78\nQD 192,148,48\nQK 79,78\nQF 20,72,73\nQD 200,144,36\n"
"QH 176,230,12288\nQF 21,74,75\nQD 200,148,52\nQAW 112,40,241,50,2048\nQD 208,144,40\nQF 22,76,77\nQF 23,78,79\nQD 208,148,56\nQH 180,231,12288\nQK 80,78\nQD 216,144,44\nQK 81,78\nQK 82,78\n"
"QD 216,148,60\nQK 83,78\nQK 84,78\nQD 196,152,32\nQH 184,230,14336\nQK 85,78\nQD 196,156,48\nQAW 116,40,241,51,2048\nQD 204,152,36\nQK 86,78\nQK 87,78\nQD 204,156,52\nQH 188,231,14336\nQF 24,80,81\n"
"QD 212,152,40\nQF 25,82,83\nQF 26,84,85\nQD 212,156,56\nQF 27,86,87\nQK 88,78\nQD 220,152,44\nQK 89,78\nQK 90,78\nQD 220,156,60\nQG 4\nQK 91,78\nQK 92,78\nQD 192,160,64\nQAW 120,40,241,50,3072\n"
"QD 192,164,80\nQK 93,78\nQK 94,78\nQD 200,160,68\nQH 128,230,16384\nQK 95,78\nQD 200,164,84\nQF 28,88,89\nQF 29,90,91\nQD 208,160,72\nQF 30,92,93\nQF 31,94,95\nQD 208,164,88\nQH 132,231,16384\n"
"QD 216,160,76\nQAW 124,40,241,51,3072\nQD 216,164,92\nQDD 49\nQD 196,168,64\nQH 136,230,18432\nQD 196,172,80\nQFH 73,74\nQD 204,168,68\nQAB 16,64,240,73,0\nQD 204,172,84\nQH 140,231,18432\n"
"QD 212,168,72\nQL 36,249,48,0\nQD 212,172,88\nQD 220,168,76\nQD 220,172,92\nQG 4\nQD 192,176,96\nQD 192,180,112\nQD 200,176,100\nQH 144,230,20480\nQD 200,180,116\nQL 36,249,48,1024\nQD 208,176,104\n"
"QD 208,180,120\nQH 148,231,20480\nQD 216,176,108\nQD 216,180,124\nQD 196,184,96\nQH 152,230,22528\nQD 196,188,112\nQL 36,249,48,2048\nQD 204,184,100\nQD 204,188,116\nQH 156,231,22528\nQD 212,184,104\n"
"QFH 73,74\nQD 212,188,120\nQAB 20,64,240,73,1024\nQD 220,184,108\nQD 220,188,124\nQG 4\nQD 192,128,128\nQL 36,249,48,3072\nQD 192,132,144\nQGG 49\nQD 200,128,132\nQH 160,230,24576\nQD 200,132,148\n"
"QAA 48\nQD 208,128,136\nQD 208,132,152\nQH 164,231,24576\nQD 216,128,140\nQL 36,249,48,0\nQD 216,132,156\nQBP 48\nQD 196,136,128\nQH 168,230,26624\nQD 196,140,144\nQZ 50,44\nQD 204,136,132\n"
"QD 204,140,148\nQH 172,231,26624\nQD 212,136,136\nQAW 32,40,241,50,0\nQD 212,140,152\nQZ 51,44\nQD 220,136,140\nQD 220,140,156\nQG 4\nQD 192,144,160\nQD 192,148,176\nQD 200,144,164\nQH 176,230,28672\n"
"QD 200,148,180\nQFH 73,74\nQD 208,144,168\nQAB 24,64,240,73,2048\nQD 208,148,184\nQH 180,231,28672\nQD 216,144,172\nQAW 36,40,241,51,0\nQD 216,148,188\nQD 196,152,160\nQH 184,230,30720\n"
"QD 196,156,176\nQD 204,152,164\nQD 204,156,180\nQH 188,231,30720\nQD 212,152,168\nQAW 40,40,241,50,1024\nQD 212,156,184\nQD 220,152,172\nQD 220,156,188\nQG 4\nQD 192,160,192\nQAM 30,0\ns_barrier\n"
"QD 192,164,208\nQDH 230\nQDH 231\nQD 200,160,196\nQH 128,230,0\nQD 200,164,212\nQAW 44,40,241,51,1024\nQD 208,160,200\nQD 208,164,216\nQH 132,231,0\nQD 216,160,204\nQFH 73,74\nQD 216,164,220\n"
"QAB 28,64,240,73,3072\nQD 196,168,192\nQH 136,230,2048\nQD 196,172,208\nQAW 48,40,241,50,2048\nQD 204,168,196\nQD 204,172,212\nQH 140,231,2048\nQD 212,168,200\nQD 212,172,216\nQD 220,168,204\n"
"QD 220,172,220\nQG 4\nQD 192,176,224\nQAW 52,40,241,51,2048\nQD 192,180,240\nQD 200,176,228\nQH 144,230,4096\nQD 200,180,244\nQD 208,176,232\nQD 208,180,248\nQH 148,231,4096\nQD 216,176,236\n"
"QAW 56,40,241,50,3072\nQD 216,180,252\nQD 196,184,224\nQH 152,230,6144\nQD 196,188,240\nQD 204,184,228\nQD 204,188,244\nQH 156,231,6144\nQD 212,184,232\nQAW 60,40,241,51,3072\nQD 212,188,248\nQGZ 49\n"
"QD 220,184,236\nQD 220,188,252\nQG 4\nQAS 64,128,0\nQL 36,249,48,0\nQD 64,132,16\nQAS 72,128,4\nQH 160,230,8192\nQD 72,132,20\nQK 192,78\nQK 193,78\nQAS 80,128,8\nQK 194,78\nQK 195,78\nQD 80,132,24\n"
"QH 164,231,8192\nQK 196,78\nQAS 88,128,12\nQL 36,249,48,1024\nQD 88,132,28\nQK 197,78\nQK 198,78\nQAS 68,136,0\nQH 168,230,10240\nQK 199,78\nQD 68,140,16\nQF 0,192,193\nQF 1,194,195\nQAS 76,136,4\n"
"QF 2,196,197\nQF 3,198,199\nQD 76,140,20\nQH 172,231,10240\nQK 200,78\nQAS 84,136,8\nQL 36,249,48,2048\nQD 84,140,24\nQK 201,78\nQK 202,78\nQAS 92,136,12\nQK 203,78\nQK 204,78\nQD 92,140,28\nQG 4\n"
"QK 205,78\nQK 206,78\nQD 64,144,32\nQK 207,78\nQF 4,200,201\nQD 64,148,48\nQF 5,202,203\nQF 6,204,205\nQD 72,144,36\nQH 176,230,12288\nQF 7,206,207\nQD 72,148,52\nQL 36,249,48,3072\nQD 80,144,40\n"
"QFI 49\nQK 208,78\nQK 209,78\nQD 80,148,56\nQH 180,231,12288\nQK 210,78\nQD 88,144,44\nQAA 48\nQK 211,78\nQK 212,78\nQD 88,148,60\nQK 213,78\nQK 214,78\nQD 68,152,32\nQH 184,230,14336\nQK 215,78\n"
"QD 68,156,48\nQL 36,249,48,0\nQD 76,152,36\nQBP 48\nQF 8,208,209\nQF 9,210,211\nQD 76,156,52\nQH 188,231,14336\nQF 10,212,213\nQD 84,152,40\nQBV 86\nQF 11,214,215\nQK 216,78\nQD 84,156,56\nQK 217,78\n"
"QK 218,78\nQD 92,152,44\nQK 219,78\nQK 220,78\nQD 92,156,60\nQG 4\nQK 221,78\nQK 222,78\nQD 64,160,64\nQBB 88,250,85\nQD 64,164,80\nQK 223,78\nQF 12,216,217\nQD 72,160,68\nQH 128,230,16384\n"
"QF 13,218,219\nQD 72,164,84\nQF 14,220,221\nQF 15,222,223\nQD 80,160,72\nQD 80,164,88\nQH 132,231,16384\nQD 88,160,76\nQS 88,251,85,1024\nQD 88,164,92\nQAO 85\nQD 68,168,64\nQH 136,230,18432\n"
"QD 68,172,80\nQCU 73,74\nQD 76,168,68\nQAB 0,64,240,73,0\nQD 76,172,84\nQH 140,231,18432\nQD 84,168,72\nQDV 86\nQD 84,172,88\nQBB 88,250,85\nQD 92,168,76\nQD 92,172,92\nQG 4\nQD 64,176,96\n"
"QD 64,180,112\nQD 72,176,100\nQH 144,230,20480\nQD 72,180,116\nQS 88,251,85,1024\nQD 80,176,104\nQAO 85\nQD 80,180,120\nQH 148,231,20480\nQD 88,176,108\nQDW 86\nQD 88,180,124\nQD 68,184,96\n"
"QH 152,230,22528\nQD 68,188,112\nQBB 88,250,85\nQD 76,184,100\nQD 76,188,116\nQH 156,231,22528\nQD 84,184,104\nQCU 73,74\nQD 84,188,120\nQAB 4,64,240,73,1024\nQD 92,184,108\nQD 92,188,124\nQG 4\n"
"QD 64,128,128\nQS 88,251,85,1024\nQD 64,132,144\nQAO 85\nQD 72,128,132\nQH 160,230,24576\nQD 72,132,148\nQFJ 49\nQD 80,128,136\nQD 80,132,152\nQH 164,231,24576\nQD 88,128,140\nQL 36,249,48,0\n"
"QD 88,132,156\nQD 68,136,128\nQH 168,230,26624\nQD 68,140,144\nQD 76,136,132\nQD 76,140,148\nQH 172,231,26624\nQD 84,136,136\nQL 36,249,48,1024\nQD 84,140,152\nQD 92,136,140\nQD 92,140,156\nQG 4\n"
"QD 64,144,160\nQD 64,148,176\nQD 72,144,164\nQH 176,230,28672\nQD 72,148,180\nQCU 73,74\nQD 80,144,168\nQAB 8,64,240,73,2048\nQD 80,148,184\nQH 180,231,28672\nQD 88,144,172\nQL 36,249,48,2048\n"
"QD 88,148,188\nQD 68,152,160\nQH 184,230,30720\nQD 68,156,176\nQD 76,152,164\nQD 76,156,180\nQH 188,231,30720\nQD 84,152,168\nQL 36,249,48,3072\nQD 84,156,184\nQGH 49\nQD 92,152,172\nQAA 48\n"
"QD 92,156,188\nQG 4\nQD 64,160,192\nQD 64,164,208\nQD 72,160,196\nQL 36,249,48,0\nQD 72,164,212\nQBP 48\nQD 80,160,200\nQD 80,164,216\nQD 88,160,204\nQCU 73,74\nQD 88,164,220\nQAB 12,64,240,73,3072\n"
"QD 68,168,192\nQD 68,172,208\nQD 76,168,196\nQD 76,172,212\nQD 84,168,200\nQD 84,172,216\nQD 92,168,204\nQD 92,172,220\nQG 0\nQD 64,176,224\nQD 64,180,240\nQD 72,176,228\nQD 72,180,244\n"
"QD 80,176,232\nQD 80,180,248\nQD 88,176,236\nQD 88,180,252\nQD 68,184,224\nQD 68,188,240\nQD 76,184,228\nQD 76,188,244\nQD 84,184,232\nQD 84,188,248\nQD 92,184,236\nQD 92,188,252\nQK 64,78\nQK 65,78\n"
"QK 66,78\nQK 67,78\nQK 68,78\nQK 69,78\nQK 70,78\nQK 71,78\nQF 16,64,65\nQF 17,66,67\nQF 18,68,69\nQF 19,70,71\nQK 72,78\nQK 73,78\nQK 74,78\nQK 75,78\nQK 76,78\nQK 77,78\nQK 78,78\nQK 79,78\n"
"QF 20,72,73\nQF 21,74,75\nQF 22,76,77\nQF 23,78,79\nQK 80,78\nQK 81,78\nQK 82,78\nQK 83,78\nQK 84,78\nQK 85,78\nQK 86,78\nQK 87,78\nQF 24,80,81\nQF 25,82,83\nQF 26,84,85\nQF 27,86,87\nQK 88,78\n"
"QK 89,78\nQK 90,78\nQK 91,78\nQK 92,78\nQK 93,78\nQK 94,78\nQK 95,78\nQF 28,88,89\nQF 29,90,91\nQF 30,92,93\nQF 31,94,95\nQFK 73,74\nQAB 16,64,240,73,0\nQFK 73,74\nQAB 20,64,240,73,1024\nQFK 73,74\n"
"QAB 24,64,240,73,2048\nQFK 73,74\nQAB 28,64,240,73,3072\nL_kv_end:\nQDO \nQN 185,241\nQN 187,254\nQEU 144,185\nv_add_u32 v145, 65536, v185\nQGL 146,185\nv_add_u32 v147, 20480, v185\n"
"s_bitcmp1_b32 %1, 2\ns_cbranch_scc1 L_setX\nQDO \nQAH 41\ns_barrier\nQB 228,145,36864\nQB 232,145,37888\nQB 236,145,38912\nQB 240,145,39936\nQB 244,145,40960\nQB 248,145,41984\nQG 4\nQAN 192,228,96\n"
"QB 252,145,43008\nQAA 50\nQAN 212,228,100\nQJ 144,40,241,50,0\nQAN 196,232,96\nQB 228,145,44032\nQG 4\nQAA 51\nQAN 216,232,100\ns_mov_b64 s[76:77], %2\nQGN 76\nQAX 77\nQDL 78\nQAN 200,236,96\n"
"QB 232,145,45056\nQBA 79\nQAN 220,236,100\nQJ 148,40,241,51,0\nQAN 204,240,96\nQB 236,145,46080\nQG 4\nQAN 224,240,100\nQDU 143,185\nQEU 132,185\nQAN 208,244,96\nQB 240,145,47104\n"
"v_add_u32 v133, 20480, v185\nQAN 228,244,100\nQJ 152,40,241,50,1024\nQC 192,248,104\nQB 244,145,48128\nQG 4\nQC 212,248,108\nv_add_u32 v134, 40960, v185\nv_add_u32 v135, 61440, v185\nQC 196,252,104\n"
"QB 248,145,49152\nQDS 140,143\nQC 216,252,108\nQJ 156,40,241,51,1024\nQC 200,228,104\nQB 252,145,50176\nQG 4\nQC 220,228,108\nv_lshlrev_b32 v140, 11, v140\nv_bfe_u32 v141, v143, 2, 3\nQC 204,232,104\n"
"QB 228,145,51200\nv_lshl_add_u32 v140, v141, 7, v140\nQC 224,232,108\nQJ 160,40,241,50,2048\nQC 208,236,104\nQB 232,145,52224\nQG 4\nQC 228,236,108\nv_and_b32 v142, 3, v143\nQFX 140,142\n"
"QC 192,240,112\nQB 236,145,53248\nQEV 141\nQC 212,240,116\nQJ 164,40,241,51,2048\nQC 196,244,112\nQB 240,145,54272\nQG 4\nQC 216,244,116\nv_lshl_add_u32 v140, v141, 5, v140\nv_xor_b32 v128, 0, v140\n"
"QC 200,248,112\nQB 244,145,55296\nv_xor_b32 v129, 32, v140\nQC 220,248,116\nQJ 168,40,241,50,3072\nQC 204,252,112\nQB 248,145,56320\nQG 4\nQC 224,252,116\nQGJ 130,140\nv_xor_b32 v131, 96, v140\n"
"QC 208,228,112\nQAH 31\ns_barrier\nQB 252,145,57344\ns_mov_b64 s[44:45], %2\nQC 228,228,116\nQJ 172,40,241,51,3072\nQC 192,232,120\nQB 228,145,58368\nQG 4\nQAA 50\nQC 212,232,124\n"
"s_add_u32 s44, s44, 55574528\nQAX 45\nQDL 46\nQBA 47\nQC 196,236,120\nQB 232,145,59392\nv_mov_b32 v148, 0xbdd2d3e8\nQC 216,236,124\nQJ 176,40,241,50,0\nQC 200,240,120\nQB 236,145,60416\nQG 4\nQAA 51\n"
"QC 220,240,124\nv_mov_b32 v149, 0xc0135761\nQC 204,244,120\nQB 240,145,61440\nQC 224,244,124\nQJ 180,40,241,51,0\nQC 208,248,120\nQB 244,145,62464\nQG 4\nQC 228,248,124\nQC 192,252,32\n"
"QB 248,145,63488\nQC 212,252,36\nQJ 184,40,241,50,1024\nQC 196,228,32\nQB 252,145,64512\nQG 4\nQC 216,228,36\nQC 200,232,32\nQB 228,146,0\nQC 220,232,36\nQJ 188,40,241,51,1024\nQC 204,236,32\n"
"QB 232,146,1024\nQG 4\nQC 224,236,36\nQC 208,240,32\nQB 236,146,2048\nQC 228,240,36\nQJ 192,40,241,50,2048\nQC 192,244,40\nQB 240,146,3072\nQG 4\nQC 212,244,44\nQC 196,248,40\nQB 244,146,4096\n"
"QC 216,248,44\nQJ 196,40,241,51,2048\nQC 200,252,40\nQB 248,146,5120\nQG 4\nQC 220,252,44\nQC 204,228,40\nQB 252,146,6144\nQC 224,228,44\nQJ 200,40,241,50,3072\nQC 208,232,40\nQB 228,146,7168\nQG 4\n"
"QC 228,232,44\nQC 192,236,48\nQB 232,146,8192\nQC 212,236,52\nQJ 204,40,241,51,3072\nQC 196,240,48\nQB 236,146,9216\nQG 4\nQAA 50\nQC 216,240,52\nQC 200,244,48\nQB 240,146,10240\nQC 220,244,52\n"
"QJ 0,40,241,50,0\nQC 204,248,48\nQB 244,146,11264\nQG 4\nQAA 51\nQC 224,248,52\nQC 208,252,48\nQAH 9\ns_barrier\nQB 248,146,12288\nQC 228,252,52\nQJ 4,40,241,51,0\nQC 192,228,56\nQB 252,146,13312\n"
"QG 4\nQC 212,228,60\nQC 196,232,56\nQB 228,146,14336\nQC 216,232,60\nQJ 8,40,241,50,1024\nQC 200,236,56\nQB 232,146,15360\nQG 4\nQC 220,236,60\nQC 204,240,56\nQB 236,146,16384\nQC 224,240,60\n"
"QJ 12,40,241,51,1024\nQC 208,244,56\nQB 240,146,17408\nQG 4\nQC 228,244,60\nQA 192,248,144\nQB 244,146,18432\nQA 212,248,148\nQJ 16,40,241,50,2048\nQA 196,252,144\nQB 248,146,19456\nQG 4\n"
"QA 216,252,148\nQA 200,228,144\nQB 252,146,20480\nQA 220,228,148\nQJ 20,40,241,51,2048\nQA 204,232,144\nQB 228,146,21504\nQG 4\nQA 224,232,148\nQA 208,236,144\nQB 232,146,22528\nQA 228,236,148\n"
"QJ 24,40,241,50,3072\nQA 192,240,152\nQB 236,146,23552\nQG 4\nQA 212,240,156\nQA 196,244,152\nQB 240,146,24576\nQA 216,244,156\nQJ 28,40,241,51,3072\nQA 200,248,152\nQB 244,146,25600\nQG 4\nQFL 49\n"
"QA 220,248,156\nQA 204,252,152\nQB 248,146,26624\nQA 224,252,156\nQL 36,249,48,0\nQA 208,228,152\nQB 252,146,27648\nQG 4\nQA 228,228,156\nQA 192,232,160\nQB 228,146,28672\nQA 212,232,164\n"
"QL 36,249,48,1024\nQA 196,236,160\nQB 232,146,29696\nQG 4\nQA 216,236,164\nQA 200,240,160\nQB 236,146,30720\nQA 220,240,164\nQL 36,249,48,2048\nQA 204,244,160\nQB 240,146,31744\nQG 4\nQA 224,244,164\n"
"QA 208,248,160\nQAH 11\ns_barrier\nQB 244,145,16384\nQA 228,248,164\nQL 36,249,48,3072\nQA 192,252,168\nQB 248,145,17408\nQDY 49\nQA 212,252,172\nQG 4\nQAA 48\nQA 196,228,168\nQB 252,145,18432\n"
"QA 216,228,172\nQL 36,249,48,0\nQA 200,232,168\nQB 228,145,19456\nQBP 48\nQA 220,232,172\nQG 4\nQA 204,236,168\nQB 232,145,20480\nQA 224,236,172\nQA 208,240,168\nQB 236,145,21504\nQA 228,240,172\n"
"QG 4\nQA 192,244,176\nQB 240,145,22528\nQA 212,244,180\nQA 196,248,176\nQB 244,145,23552\nQA 216,248,180\nQG 4\nQA 200,252,176\nQB 248,145,24576\nQA 220,252,180\nQA 204,228,176\nQB 252,145,25600\n"
"QA 224,228,180\nQG 4\nQA 208,232,176\nQB 228,145,26624\nQA 228,232,180\nQA 192,236,184\nQB 232,145,27648\nQA 212,236,188\nQG 4\nQA 196,240,184\nQB 236,145,28672\nQA 216,240,188\nQA 200,244,184\n"
"QB 240,145,29696\nQA 220,244,188\nQG 4\nQA 204,248,184\nQB 244,145,30720\nQA 224,248,188\nQA 208,252,184\nQB 248,145,31744\nQA 228,252,188\nQG 4\nQA 192,228,192\nQB 252,145,32768\nQA 212,228,196\n"
"QA 196,232,192\nQB 228,145,33792\nQA 216,232,196\nQG 4\nQA 200,236,192\nQB 232,145,34816\nQA 220,236,196\nQA 204,240,192\nQB 236,145,35840\nQA 224,240,196\nQG 4\nQA 208,244,192\nQAH 0\ns_barrier\n"
"QB 240,144,61440\nQA 228,244,196\nQJ 64,76,132,87,0\nQA 192,248,200\nQB 244,144,62464\nQA 212,248,204\nQG 4\nQA 196,252,200\nQB 248,144,63488\nQA 216,252,204\nQJ 68,76,133,87,0\nQA 200,228,200\n"
"QB 252,144,64512\nQA 220,228,204\nQG 4\nQA 204,232,200\nQB 228,145,0\nQA 224,232,204\nQJ 72,76,134,87,0\nQA 208,236,200\nQB 232,145,1024\nQA 228,236,204\nQG 4\nQA 192,240,0\nQB 236,145,2048\n"
"QA 212,240,4\nQJ 76,76,135,87,0\nQA 196,244,0\nQB 240,145,3072\nQA 216,244,4\nQG 4\nQA 200,248,0\nQB 244,145,4096\nQA 220,248,4\nQA 204,252,0\nQB 248,145,5120\nQA 224,252,4\nQG 4\nQA 208,228,0\n"
"QB 252,145,6144\nQA 228,228,4\nQA 192,232,8\nQB 228,145,7168\nQA 212,232,12\nQG 4\nQA 196,236,8\nQB 232,145,8192\nQA 216,236,12\nQA 200,240,8\nQB 236,145,9216\nQA 220,240,12\nQG 4\nQA 204,244,8\n"
"QB 240,145,10240\nQA 224,244,12\nQA 208,248,8\nQB 244,145,11264\nQA 228,248,12\nQG 4\nQA 192,252,16\nQB 248,145,12288\nQA 212,252,20\nQA 196,228,16\nQB 252,145,13312\nQA 216,228,20\nQG 4\n"
"QA 200,232,16\nQB 228,145,14336\nQA 220,232,20\nQA 204,236,16\nQB 232,145,15360\nQA 224,236,20\nQG 4\nQA 208,240,16\nQA 228,240,20\nQA 192,244,24\nQA 212,244,28\nQG 3\nQA 196,248,24\nQA 216,248,28\n"
"QG 2\nQA 200,252,24\nQA 220,252,28\nQG 1\nQA 204,228,24\nQA 224,228,28\nQG 0\nQA 208,232,24\nQA 228,232,28\nQJ 80,76,132,87,1024\nQAP 64,192\nQAP 66,194\nQAP 68,196\nQAP 70,198\nQAP 72,200\n"
"QAP 74,202\nQAP 76,204\nQAP 78,206\nQAP 80,208\nQAP 82,210\nQAV 64,148\nQJ 84,76,133,87,1024\nQAV 66,148\nQAV 68,148\nQAV 70,148\nQAV 72,148\nQAV 74,148\nQAV 76,148\nQAV 78,148\nQAV 80,148\n"
"QAV 82,148\nQX 64,192\nQX 66,194\nQJ 88,76,134,87,1024\nQX 68,196\nQX 70,198\nQX 72,200\nQX 74,202\nQX 76,204\nQX 78,206\nQX 80,208\nQX 82,210\nQW 64\nQW 65\nQW 66\nQJ 92,76,135,87,1024\nQW 67\n"
"QW 68\nQW 69\nQW 70\nQW 71\nQW 72\nQW 73\nQW 74\nQW 75\nQW 76\nQW 77\nQJ 96,76,132,87,2048\nQW 78\nQW 79\nQW 80\nQW 81\nQW 82\nQW 83\nQAK 64\nQAK 66\nQAK 68\nQAK 70\nQAK 72\nQJ 100,76,133,87,2048\n"
"QAK 74\nQAK 76\nQAK 78\nQAK 80\nQAK 82\nQV 64\nQV 65\nQV 66\nQV 67\nQV 68\nQV 69\nQJ 104,76,134,87,2048\nQV 70\nQV 71\nQV 72\nQV 73\nQV 74\nQV 75\nQV 76\nQV 77\nQV 78\nQV 79\nQV 80\n"
"QJ 108,76,135,87,2048\nQV 81\nQV 82\nQV 83\nQX 192,64\nQX 194,66\nQX 196,68\nQX 198,70\nQX 200,72\nQX 202,74\nQX 204,76\nQX 206,78\nQX 208,80\nQX 210,82\nQF 84,192,193\nQF 85,194,195\nQF 86,196,197\n"
"QF 87,198,199\nQAB 84,44,185,52,0\nQF 88,200,201\nQF 89,202,203\nQF 90,204,205\nQF 91,206,207\nQAB 88,44,185,52,1024\nQF 92,208,209\nQF 93,210,211\nQFM 92,44,185,53\nQAP 64,212\nQAP 66,214\n"
"QAP 68,216\nQAP 70,218\nQAP 72,220\nQAP 74,222\nQAP 76,224\nQAP 78,226\nQAP 80,228\nQAP 82,230\nQAV 64,148\nQAV 66,148\nQAV 68,148\nQAV 70,148\nQAV 72,148\nQAV 74,148\nQAV 76,148\nQAV 78,148\n"
"QAV 80,148\nQAV 82,148\nQX 64,212\nQX 66,214\nQX 68,216\nQX 70,218\nQX 72,220\nQX 74,222\nQX 76,224\nQX 78,226\nQX 80,228\nQX 82,230\nQW 64\nQW 65\nQW 66\nQW 67\nQW 68\nQW 69\nQW 70\nQW 71\nQW 72\n"
"QW 73\nQW 74\nQW 75\nQW 76\nQW 77\nQW 78\nQW 79\nQW 80\nQW 81\nQW 82\nQW 83\nQAK 64\nQAK 66\nQAK 68\nQAK 70\nQAK 72\nQAK 74\nQAK 76\nQAK 78\nQAK 80\nQAK 82\nQV 64\nQV 65\nQV 66\nQV 67\nQV 68\nQV 69\n"
"QV 70\nQV 71\nQV 72\nQV 73\nQV 74\nQV 75\nQV 76\nQV 77\nQV 78\nQV 79\nQV 80\nQV 81\nQV 82\nQV 83\nQX 212,64\nQX 214,66\nQX 216,68\nQX 218,70\nQX 220,72\nQX 222,74\nQX 224,76\nQX 226,78\nQX 228,80\n"
"QX 230,82\nQF 94,212,213\nQF 95,214,215\nQF 96,216,217\nQF 97,218,219\nQAB 94,44,147,52,0\nQF 98,220,221\nQF 99,222,223\nQF 100,224,225\nQF 101,226,227\nQAB 98,44,147,52,1024\nQF 102,228,229\n"
"QF 103,230,231\nQFM 102,44,147,53\nQAM 18,0\ns_barrier\nQE 0,128,0\nQE 2,128,4096\nQE 4,129,0\nQE 6,129,4096\nQE 8,130,0\nQE 10,130,4096\nQE 12,131,0\nQE 14,131,4096\nQE 16,128,1024\nQE 18,128,5120\n"
"QE 20,129,1024\nQE 22,129,5120\nQE 24,130,1024\nQE 26,130,5120\nQE 28,131,1024\nQE 30,131,5120\nQAM 14,0\ns_barrier\nQAE 0,0,64\nQEM 86\ns_mov_b64 s[36:37], %2\ns_mov_b32 s38, 72089600\nQAE 4,0,68\n"
"QE 32,128,8192\nQBA 39\nQAE 8,0,72\nQE 34,128,12288\nQGI 40,80\nQAE 12,0,76\nQBB 88,250,85\nQAE 16,4,64\nQE 36,129,8192\nQBM 182,40,185\nQAE 20,4,68\nQE 38,129,12288\ns_lshl_b32 s41, s82, 20\n"
"QAE 24,4,72\nQS 88,251,85,1024\nQAE 28,4,76\nQE 40,130,8192\nQAO 85\nQAE 32,8,64\nQE 42,130,12288\ns_lshl_b32 s67, s81, 1\nQAE 36,8,68\nQJ 112,76,132,87,3072\nQAE 40,8,72\nQE 44,131,8192\n"
"QBT 83,67,80\nQAE 44,8,76\nQE 46,131,12288\nQCS 83\nQAE 48,12,64\nQJ 116,76,133,87,3072\nQAE 52,12,68\nQE 48,128,9216\nQFY 80\ns_cselect_b32 s65, 28, s83\nQAE 56,12,72\nQE 50,128,13312\n"
"s_sub_u32 s66, s65, 1\nQAE 60,12,76\nQJ 120,76,134,87,3072\nQAE 64,16,64\nQE 52,129,9216\nQFE 83,80\nQAE 68,16,68\nQE 54,129,13312\nQZ 83,81\nQAE 72,16,72\nQJ 124,76,135,87,3072\nQAE 76,16,76\n"
"QE 56,130,9216\ns_cmp_eq_u32 s83, 7\ns_cselect_b32 s66, 16, s66\nQAE 80,20,64\nQE 58,130,13312\nQHA 68,66\nQAE 84,20,68\nQHA 83,65\nQHA 84,67\ns_or_b32 s69, s68, s83\nQAE 88,20,72\nQE 60,131,9216\n"
"s_or_b32 s69, s69, s84\nQAE 92,20,76\nQE 62,131,13312\ns_or_b32 s69, s69, 131072\nQAE 96,24,64\nQDC 64\ns_mov_b32 s71, 0x53800000\ns_lshl_b32 s83, s82, 20\nQAE 100,24,68\nQEA 56,83\n"
"s_add_u32 s57, s83, 16384\ns_add_u32 s58, s83, 1048576\nQAE 104,24,72\ns_add_u32 s59, s83, 1097728\nQGI 83,80\ns_add_u32 s60, s83, 0x4000\nQAE 108,24,76\ns_add_u32 s61, s83, 0x8000\n"
"s_add_u32 s62, s83, 81920\nQCU 63,83\nQAE 112,28,64\nQBX 83,80\nQCJ 84,80\ns_xor_b32 s83, s83, s84\nQAE 116,28,68\nQGA 83\nQEC 83\nQBM 186,83,185\nQAE 120,28,72\nQAE 124,28,76\nQAM 16,0\ns_barrier\n"
"QA 0,32,80\nQDF 86\nQA 4,32,84\nQE 0,128,16384\nQA 8,32,88\nQE 2,128,20480\nQA 12,32,92\nQBB 88,250,85\nQA 16,36,80\nQE 4,129,16384\nQA 20,36,84\nQE 6,129,20480\nQA 24,36,88\nQS 88,251,85,1024\n"
"QA 28,36,92\nQE 8,130,16384\nQAO 85\nQA 32,40,80\nQE 10,130,20480\nQAA 87\nQA 36,40,84\nQJ 128,76,132,87,0\nQA 40,40,88\nQE 12,131,16384\nQA 44,40,92\nQE 14,131,20480\nQA 48,44,80\n"
"QJ 132,76,133,87,0\nQA 52,44,84\nQE 16,128,17408\nQA 56,44,88\nQE 18,128,21504\nQA 60,44,92\nQJ 136,76,134,87,0\nQA 64,48,80\nQE 20,129,17408\nQA 68,48,84\nQE 22,129,21504\nQA 72,48,88\n"
"QJ 140,76,135,87,0\nQA 76,48,92\nQE 24,130,17408\nQA 80,52,80\nQE 26,130,21504\nQA 84,52,84\nQA 88,52,88\nQE 28,131,17408\nQA 92,52,92\nQE 30,131,21504\nQA 96,56,80\nQA 100,56,84\nQA 104,56,88\n"
"QA 108,56,92\nQA 112,60,80\nQA 116,60,84\nQA 120,60,88\nQA 124,60,92\nQAM 10,0\ns_barrier\nQCR 158\ns_bfe_u32 s99, %1, 196608\nQBH 99\ns_bfe_u32 s100, %1, 131078\nQZ 99,100\nQBU 99\n"
"s_add_u32 s99, s99, 2048\nQZ 99,96\nQEI \nQEJ 92,158,99\nQEK \nQA 0,0,96\nQEN 86\nQA 4,0,100\nQE 32,128,24576\nQA 8,0,104\nQE 34,128,28672\nQA 12,0,108\nQBB 88,250,85\nQA 16,4,96\nQE 36,129,24576\n"
"QA 20,4,100\nQE 38,129,28672\nQA 24,4,104\nQS 88,251,85,1024\nQA 28,4,108\nQE 40,130,24576\nQAO 85\nQA 32,8,96\nQE 42,130,28672\nQA 36,8,100\nQJ 64,76,132,87,1024\nQA 40,8,104\nQE 44,131,24576\n"
"QA 44,8,108\nQE 46,131,28672\nQA 48,12,96\nQJ 68,76,133,87,1024\nQA 52,12,100\nQE 48,128,25600\nQA 56,12,104\nQE 50,128,29696\nQA 60,12,108\nQJ 72,76,134,87,1024\nQA 64,16,96\nQE 52,129,25600\n"
"QA 68,16,100\nQE 54,129,29696\nQA 72,16,104\nQJ 76,76,135,87,1024\nQA 76,16,108\nQE 56,130,25600\nQA 80,20,96\nQE 58,130,29696\nQA 84,20,100\nQA 88,20,104\nQE 60,131,25600\nQA 92,20,108\n"
"QE 62,131,29696\nQA 96,24,96\nQA 100,24,100\nQA 104,24,104\nQA 108,24,108\nQA 112,28,96\nQA 116,28,100\nQA 120,28,104\nQA 124,28,108\nQAM 11,0\ns_barrier\nQA 0,32,112\ns_add_u32 m0, s86, 49152\n"
"QA 4,32,116\nQE 0,128,32768\nQA 8,32,120\nQE 2,128,36864\nQA 12,32,124\nQBB 88,250,85\nQA 16,36,112\nQE 4,129,32768\nQA 20,36,116\nQE 6,129,36864\nQA 24,36,120\nQS 88,251,85,1024\nQA 28,36,124\n"
"QE 8,130,32768\nQAO 85\nQA 32,40,112\nQE 10,130,36864\nQA 36,40,116\nQJ 80,76,132,87,2048\nQA 40,40,120\nQE 12,131,32768\nQA 44,40,124\nQE 14,131,36864\nQA 48,44,112\nQJ 84,76,133,87,2048\n"
"QA 52,44,116\nQE 16,128,33792\nQA 56,44,120\nQE 18,128,37888\nQA 60,44,124\nQJ 88,76,134,87,2048\nQA 64,48,112\nQE 20,129,33792\nQA 68,48,116\nQE 22,129,37888\nQA 72,48,120\nQJ 92,76,135,87,2048\n"
"QA 76,48,124\nQE 24,130,33792\nQA 80,52,112\nQE 26,130,37888\nQA 84,52,116\nQA 88,52,120\nQE 28,131,33792\nQA 92,52,124\nQE 30,131,37888\nQA 96,56,112\nQA 100,56,116\nQA 104,56,120\nQA 108,56,124\n"
"QA 112,60,112\nQA 116,60,116\nQA 120,60,120\nQA 124,60,124\nQAM 10,0\ns_barrier\nQCG 100\nL_pc12:\ns_lshr_b32 s99, %1, 2\nQGB 99\ns_bitcmp1_b32 %0, 0\nQGC 99\nQER 99\n"
"s_min_u32 s99, s99, L_done-L_pc12-512\nQZ 100,99\nQAX 101\nQES 138,185\nQGD 100,136,138\nQA 0,0,128\ns_add_u32 m0, s86, 57344\nQA 4,0,132\nQE 32,128,40960\nQA 8,0,136\nQE 34,128,45056\nQA 12,0,140\n"
"QBB 88,250,85\nQA 16,4,128\nQE 36,129,40960\nQA 20,4,132\nQE 38,129,45056\nQA 24,4,136\nQS 88,251,85,1024\nQA 28,4,140\nQE 40,130,40960\nQAO 85\nQA 32,8,128\nQE 42,130,45056\nQA 36,8,132\n"
"QJ 96,76,132,87,3072\nQA 40,8,136\nQE 44,131,40960\nQA 44,8,140\nQE 46,131,45056\nQA 48,12,128\nQJ 100,76,133,87,3072\nQA 52,12,132\nQE 48,128,41984\nQA 56,12,136\nQE 50,128,46080\nQA 60,12,140\n"
"QJ 104,76,134,87,3072\nQA 64,16,128\nQE 52,129,41984\nQA 68,16,132\nQE 54,129,46080\nQA 72,16,136\nQJ 108,76,135,87,3072\nQA 76,16,140\nQE 56,130,41984\nQA 80,20,128\nQE 58,130,46080\nQA 84,20,132\n"
"QA 88,20,136\nQE 60,131,41984\nQA 92,20,140\nQE 62,131,46080\nQA 96,24,128\nQA 100,24,132\nQA 104,24,136\nQA 108,24,140\nQA 112,28,128\nQA 116,28,132\nQA 120,28,136\nQA 124,28,140\nQAM 11,0\n"
"s_barrier\nQA 0,32,64\nQDY 86\nQA 4,32,68\nQE 0,128,49152\nQA 8,32,72\nQE 2,128,53248\nQA 12,32,76\nQBB 88,250,85\nQA 16,36,64\nQE 4,129,49152\nQA 20,36,68\nQE 6,129,53248\nQA 24,36,72\n"
"QS 88,251,85,1024\nQA 28,36,76\nQE 8,130,49152\nQAO 85\nQA 32,40,64\nQE 10,130,53248\nQAA 87\nQA 36,40,68\nQJ 112,76,132,87,0\nQA 40,40,72\nQE 12,131,49152\nQA 44,40,76\nQE 14,131,53248\nQA 48,44,64\n"
"QJ 116,76,133,87,0\nQA 52,44,68\nQE 16,128,50176\nQA 56,44,72\nQE 18,128,54272\nQA 60,44,76\nQJ 120,76,134,87,0\nQA 64,48,64\nQE 20,129,50176\nQA 68,48,68\nQE 22,129,54272\nQA 72,48,72\n"
"QJ 124,76,135,87,0\nQA 76,48,76\nQE 24,130,50176\nQA 80,52,64\nQE 26,130,54272\nQA 84,52,68\nQA 88,52,72\nQE 28,131,50176\nQA 92,52,76\nQE 30,131,54272\nQA 96,56,64\nQA 100,56,68\nQA 104,56,72\n"
"QA 108,56,76\nQA 112,60,64\nQA 116,60,68\nQA 120,60,72\nQA 124,60,76\nQAM 10,0\ns_barrier\nQA 0,0,80\ns_add_u32 m0, s86, 73728\nQA 4,0,84\nQE 32,128,57344\nQA 8,0,88\nQE 34,128,61440\nQA 12,0,92\n"
"QBB 88,250,85\nQA 16,4,80\nQE 36,129,57344\nQA 20,4,84\nQE 38,129,61440\nQA 24,4,88\nQS 88,251,85,1024\nQA 28,4,92\nQE 40,130,57344\nQAO 85\nQA 32,8,80\nQE 42,130,61440\nQA 36,8,84\n"
"QJ 128,76,132,87,1024\nQA 40,8,88\nQE 44,131,57344\nQA 44,8,92\nQE 46,131,61440\nQA 48,12,80\nQJ 132,76,133,87,1024\nQA 52,12,84\nQE 48,128,58368\nQA 56,12,88\nQE 50,128,62464\nQA 60,12,92\n"
"QJ 136,76,134,87,1024\nQA 64,16,80\nQE 52,129,58368\nQA 68,16,84\nQE 54,129,62464\nQA 72,16,88\nQJ 140,76,135,87,1024\nQA 76,16,92\nQE 56,130,58368\nQA 80,20,80\nQE 58,130,62464\nQA 84,20,84\n"
"QA 88,20,88\nQE 60,131,58368\nQA 92,20,92\nQE 62,131,62464\nQA 96,24,80\nQA 100,24,84\nQA 104,24,88\nQA 108,24,92\nQA 112,28,80\nQA 116,28,84\nQA 120,28,88\nQA 124,28,92\nQAM 10,0\ns_barrier\n"
"QA 0,32,96\nQFN 128\nQFN 129\nQFN 130\nQFN 131\nQA 4,32,100\nQE 0,128,0\nQFJ 86\nQA 8,32,104\nQE 2,128,4096\nQA 12,32,108\nQBB 88,250,85\nQA 16,36,96\nQE 4,129,0\nQA 20,36,100\nQE 6,129,4096\n"
"QA 24,36,104\nQS 88,251,85,1024\nQA 28,36,108\nQE 8,130,0\nQAO 85\nQA 32,40,96\nQE 10,130,4096\nQA 36,40,100\nQJ 64,76,132,87,2048\nQA 40,40,104\nQE 12,131,0\nQA 44,40,108\nQE 14,131,4096\n"
"QA 48,44,96\nQJ 68,76,133,87,2048\nQA 52,44,100\nQE 16,128,1024\nQA 56,44,104\nQE 18,128,5120\nQA 60,44,108\nQJ 72,76,134,87,2048\nQA 64,48,96\nQE 20,129,1024\nQA 68,48,100\nQE 22,129,5120\n"
"QA 72,48,104\nQJ 76,76,135,87,2048\nQA 76,48,108\nQE 24,130,1024\nQA 80,52,96\nQE 26,130,5120\nQA 84,52,100\nQA 88,52,104\nQE 28,131,1024\nQA 92,52,108\nQE 30,131,5120\nQA 96,56,96\nQA 100,56,100\n"
"QA 104,56,104\nQA 108,56,108\nQA 112,60,96\nQA 116,60,100\nQA 120,60,104\nQA 124,60,108\nQAM 10,0\ns_barrier\nQA 0,0,112\ns_add_u32 m0, s86, 90112\nQA 4,0,116\nQE 32,128,8192\nQA 8,0,120\n"
"QE 34,128,12288\nQA 12,0,124\nQBB 88,250,85\nQA 16,4,112\nQE 36,129,8192\nQA 20,4,116\nQE 38,129,12288\nQA 24,4,120\nQS 88,251,85,1024\nQA 28,4,124\nQE 40,130,8192\nQAO 85\nQA 32,8,112\n"
"QE 42,130,12288\nQA 36,8,116\nQJ 80,76,132,87,3072\nQA 40,8,120\nQE 44,131,8192\nQA 44,8,124\nQE 46,131,12288\nQA 48,12,112\nQJ 84,76,133,87,3072\nQA 52,12,116\nQE 48,128,9216\nQA 56,12,120\n"
"QE 50,128,13312\nQA 60,12,124\nQJ 88,76,134,87,3072\nQA 64,16,112\nQE 52,129,9216\nQA 68,16,116\nQE 54,129,13312\nQA 72,16,120\nQJ 92,76,135,87,3072\nQA 76,16,124\nQE 56,130,9216\nQA 80,20,112\n"
"QE 58,130,13312\nQA 84,20,116\nQA 88,20,120\nQE 60,131,9216\nQA 92,20,124\nQE 62,131,13312\nQA 96,24,112\nQA 100,24,116\nQA 104,24,120\nQA 108,24,124\nQA 112,28,112\nQA 116,28,116\nQA 120,28,120\n"
"QA 124,28,124\nQAM 10,0\ns_barrier\ns_bfe_u32 s99, %1, 196608\nQBH 99\ns_bfe_u32 s100, %1, 131078\nQZ 99,100\nQBU 99\nbuffer_load_dword v156, off, s[92:95], s99 offset:2048 sc1\nQA 0,32,128\n"
"s_add_u32 m0, s86, 98304\nQA 4,32,132\nQE 0,128,16384\nQA 8,32,136\nQE 2,128,20480\nQA 12,32,140\nQBB 88,250,85\nQA 16,36,128\nQE 4,129,16384\nQA 20,36,132\nQE 6,129,20480\nQA 24,36,136\n"
"QS 88,251,85,1024\nQA 28,36,140\nQE 8,130,16384\nQAO 85\nQA 32,40,128\nQE 10,130,20480\nQAA 87\nQA 36,40,132\nQJ 96,76,132,87,0\nQA 40,40,136\nQE 12,131,16384\nQA 44,40,140\nQE 14,131,20480\n"
"QA 48,44,128\nQJ 100,76,133,87,0\nQA 52,44,132\nQE 16,128,17408\nQA 56,44,136\nQE 18,128,21504\nQA 60,44,140\nQJ 104,76,134,87,0\nQA 64,48,128\nQE 20,129,17408\nQA 68,48,132\nQE 22,129,21504\n"
"QA 72,48,136\nQJ 108,76,135,87,0\nQA 76,48,140\nQE 24,130,17408\nQA 80,52,128\nQE 26,130,21504\nQA 84,52,132\nQA 88,52,136\nQE 28,131,17408\nQA 92,52,140\nQE 30,131,21504\nQA 96,56,128\n"
"QA 100,56,132\nQA 104,56,136\nQA 108,56,140\nQA 112,60,128\nQA 116,60,132\nQA 120,60,136\nQA 124,60,140\nQAM 11,0\ns_barrier\nQA 0,0,64\nQDN 86\nQA 4,0,68\nQE 32,128,24576\nQA 8,0,72\n"
"QE 34,128,28672\nQA 12,0,76\nQBB 88,250,85\nQA 16,4,64\nQE 36,129,24576\nQA 20,4,68\nQE 38,129,28672\nQA 24,4,72\nQS 88,251,85,1024\nQA 28,4,76\nQE 40,130,24576\nQAO 85\nQA 32,8,64\nQE 42,130,28672\n"
"QA 36,8,68\nQJ 112,76,132,87,1024\nQA 40,8,72\nQE 44,131,24576\nQA 44,8,76\nQE 46,131,28672\nQA 48,12,64\nQJ 116,76,133,87,1024\nQA 52,12,68\nQE 48,128,25600\nQA 56,12,72\nQE 50,128,29696\n"
"QA 60,12,76\nQJ 120,76,134,87,1024\nQA 64,16,64\nQE 52,129,25600\nQA 68,16,68\nQE 54,129,29696\nQA 72,16,72\nQJ 124,76,135,87,1024\nQA 76,16,76\nQE 56,130,25600\nQA 80,20,64\nQE 58,130,29696\n"
"QA 84,20,68\nQA 88,20,72\nQE 60,131,25600\nQA 92,20,76\nQE 62,131,29696\nQA 96,24,64\nQA 100,24,68\nQA 104,24,72\nQA 108,24,76\nQA 112,28,64\nQA 116,28,68\nQA 120,28,72\nQA 124,28,76\nQAM 10,0\n"
"s_barrier\nv_cmp_ne_u32 vcc, 8, v156\nQBD \ns_cbranch_vccnz L_ps13\nL_pr13:\nQA 0,32,80\ns_add_u32 m0, s86, 114688\nQA 4,32,84\nQE 0,128,32768\nQA 8,32,88\nQE 2,128,36864\nQA 12,32,92\nQBB 88,250,85\n"
"QA 16,36,80\nQE 4,129,32768\nQA 20,36,84\nQE 6,129,36864\nQA 24,36,88\nQS 88,251,85,1024\nQA 28,36,92\nQE 8,130,32768\nQAO 85\nQA 32,40,80\nQE 10,130,36864\nQA 36,40,84\nQJ 128,76,132,87,2048\n"
"QA 40,40,88\nQE 12,131,32768\nQA 44,40,92\nQE 14,131,36864\nQA 48,44,80\nQJ 132,76,133,87,2048\nQA 52,44,84\nQE 16,128,33792\nQA 56,44,88\nQE 18,128,37888\nQA 60,44,92\nQJ 136,76,134,87,2048\n"
"QA 64,48,80\nQE 20,129,33792\nQA 68,48,84\nQE 22,129,37888\nQA 72,48,88\nQJ 140,76,135,87,2048\nQA 76,48,92\nQE 24,130,33792\nQBV 40\nQA 80,52,80\nQE 26,130,37888\nQA 84,52,84\nQL 36,182,41,0\n"
"QA 88,52,88\nQE 28,131,33792\nQA 92,52,92\nQE 30,131,37888\nQA 96,56,80\nQA 100,56,84\nQL 36,182,41,1024\nQA 104,56,88\nQA 108,56,92\nQA 112,60,80\nQA 116,60,84\nQA 120,60,88\nQA 124,60,92\nQAM 12,0\n"
"s_barrier\nQA 0,0,96\nQDD 86\nQA 4,0,100\nQE 32,128,40960\nQA 8,0,104\nQE 34,128,45056\nQA 12,0,108\nQBB 88,250,85\nQA 16,4,96\nQE 36,129,40960\nQA 20,4,100\nQE 38,129,45056\nQA 24,4,104\n"
"QS 88,251,85,1024\nQA 28,4,108\nQE 40,130,40960\nQAO 85\nQA 32,8,96\nQE 42,130,45056\nQA 36,8,100\nQJ 64,76,132,87,3072\nQA 40,8,104\nQE 44,131,40960\nQA 44,8,108\nQE 46,131,45056\nQA 48,12,96\n"
"QJ 68,76,133,87,3072\nQA 52,12,100\nQE 48,128,41984\nQA 56,12,104\nQE 50,128,46080\nQA 60,12,108\nQJ 72,76,134,87,3072\nQA 64,16,96\nQE 52,129,41984\nQA 68,16,100\nQE 54,129,46080\nQA 72,16,104\n"
"QJ 76,76,135,87,3072\nQA 76,16,108\nQE 56,130,41984\nQBV 40\nQA 80,20,96\nQE 58,130,46080\nQA 84,20,100\nQL 36,182,41,2048\nQA 88,20,104\nQE 60,131,41984\nQA 92,20,108\nQE 62,131,46080\nQA 96,24,96\n"
"QA 100,24,100\nQL 36,182,41,3072\nQA 104,24,104\nQA 108,24,108\nQA 112,28,96\nQA 116,28,100\nQA 120,28,104\nQA 124,28,108\nQAM 14,0\ns_barrier\nQA 0,32,112\ns_add_u32 m0, s86, 131072\nQA 4,32,116\n"
"QE 0,128,49152\nQA 8,32,120\nQE 2,128,53248\nQA 12,32,124\nQBB 88,250,85\nQA 16,36,112\nQE 4,129,49152\nQA 20,36,116\nQE 6,129,53248\nQA 24,36,120\nQS 88,251,85,1024\nQA 28,36,124\nQE 8,130,49152\n"
"QAO 85\nQA 32,40,112\nQE 10,130,53248\nQAA 87\nQA 36,40,116\nQJ 80,76,132,87,0\nQA 40,40,120\nQE 12,131,49152\nQA 44,40,124\nQE 14,131,53248\nQA 48,44,112\nQJ 84,76,133,87,0\nQA 52,44,116\n"
"QE 16,128,50176\nQA 56,44,120\nQE 18,128,54272\nQA 60,44,124\nQJ 88,76,134,87,0\nQA 64,48,112\nQE 20,129,50176\nQA 68,48,116\nQE 22,129,54272\nQA 72,48,120\nQJ 92,76,135,87,0\nQA 76,48,124\n"
"QE 24,130,50176\nQDF 40\nQA 80,52,112\nQE 26,130,54272\ns_add_u32 s41, s41, 0x4000\nQA 84,52,116\nQL 36,182,41,0\nQA 88,52,120\nQE 28,131,50176\nQA 92,52,124\nQE 30,131,54272\nQA 96,56,112\n"
"QA 100,56,116\nQL 36,182,41,1024\nQA 104,56,120\nQA 108,56,124\nQA 112,60,112\nQA 116,60,116\nQA 120,60,120\nQA 124,60,124\nQAM 14,0\ns_barrier\nQA 0,0,128\ns_add_u32 m0, s86, 139264\nQA 4,0,132\n"
"QE 32,128,57344\nQA 8,0,136\nQE 34,128,61440\nQA 12,0,140\nQBB 88,250,85\nQA 16,4,128\nQE 36,129,57344\nQA 20,4,132\nQE 38,129,61440\nQA 24,4,136\nQS 88,251,85,1024\nQA 28,4,140\nQE 40,130,57344\n"
"QAO 85\nQA 32,8,128\nQE 42,130,61440\nQA 36,8,132\nQJ 96,76,132,87,1024\nQA 40,8,136\nQE 44,131,57344\nQA 44,8,140\nQE 46,131,61440\nQA 48,12,128\nQJ 100,76,133,87,1024\nQA 52,12,132\n"
"QE 48,128,58368\nQA 56,12,136\nQE 50,128,62464\nQA 60,12,140\nQJ 104,76,134,87,1024\nQA 64,16,128\nQE 52,129,58368\nQA 68,16,132\nQE 54,129,62464\nQA 72,16,136\nQJ 108,76,135,87,1024\nQA 76,16,140\n"
"QE 56,130,58368\nQDF 40\nQA 80,20,128\nQE 58,130,62464\nQA 84,20,132\nQL 36,182,41,2048\nQA 88,20,136\nQE 60,131,58368\nQA 92,20,140\nQE 62,131,62464\nQA 96,24,128\nQA 100,24,132\nQL 36,182,41,3072\n"
"QA 104,24,136\nQA 108,24,140\nQA 112,28,128\nQA 116,28,132\nQA 120,28,136\nQA 124,28,140\nQAM 14,0\ns_barrier\nQA 0,32,64\nQFN 128\nQFN 129\nQFN 130\nQFN 131\nQA 4,32,68\nQE 0,128,0\nQFI 86\n"
"QA 8,32,72\nQE 2,128,4096\nQA 12,32,76\nQBB 88,250,85\nQA 16,36,64\nQE 4,129,0\nQA 20,36,68\nQE 6,129,4096\nQA 24,36,72\nQS 88,251,85,1024\nQA 28,36,76\nQE 8,130,0\nQAO 85\nQA 32,40,64\n"
"QE 10,130,4096\nQA 36,40,68\nQJ 112,76,132,87,2048\nQA 40,40,72\nQE 12,131,0\nQA 44,40,76\nQE 14,131,4096\nQA 48,44,64\nQJ 116,76,133,87,2048\nQA 52,44,68\nQE 16,128,1024\nQA 56,44,72\n"
"QE 18,128,5120\nQA 60,44,76\nQJ 120,76,134,87,2048\nQA 64,48,64\nQE 20,129,1024\nQA 68,48,68\nQE 22,129,5120\nQA 72,48,72\nQJ 124,76,135,87,2048\nQA 76,48,76\nQE 24,130,1024\nQDW 40\nQA 80,52,64\n"
"QE 26,130,5120\ns_add_u32 s41, s41, 0x4000\nQA 84,52,68\nQL 36,182,41,0\nQA 88,52,72\nQE 28,131,1024\nQA 92,52,76\nQE 30,131,5120\nQA 96,56,64\nQA 100,56,68\nQL 36,182,41,1024\nQA 104,56,72\n"
"QA 108,56,76\nQA 112,60,64\nQA 116,60,68\nQA 120,60,72\nQA 124,60,76\nQAM 14,0\ns_barrier\nQA 0,0,80\ns_add_u32 m0, s86, 155648\nQA 4,0,84\nQE 32,128,8192\nQA 8,0,88\nQE 34,128,12288\nQA 12,0,92\n"
"QBB 88,250,85\nQA 16,4,80\nQE 36,129,8192\nQA 20,4,84\nQE 38,129,12288\nQA 24,4,88\nQS 88,251,85,1024\nQA 28,4,92\nQE 40,130,8192\nQAO 85\nQA 32,8,80\nQE 42,130,12288\nQA 36,8,84\n"
"QJ 128,76,132,87,3072\nQA 40,8,88\nQE 44,131,8192\nQA 44,8,92\nQE 46,131,12288\nQA 48,12,80\nQJ 132,76,133,87,3072\nQA 52,12,84\nQE 48,128,9216\nQA 56,12,88\nQE 50,128,13312\nQA 60,12,92\n"
"QJ 136,76,134,87,3072\nQA 64,16,80\nQE 52,129,9216\nQA 68,16,84\nQE 54,129,13312\nQA 72,16,88\nQJ 140,76,135,87,3072\nQA 76,16,92\nQE 56,130,9216\nQDW 40\nQA 80,20,80\nQE 58,130,13312\nQA 84,20,84\n"
"QL 36,182,41,2048\nQA 88,20,88\nQE 60,131,9216\nQA 92,20,92\nQE 62,131,13312\nQA 96,24,80\nQA 100,24,84\nQL 36,182,41,3072\nQA 104,24,88\nQA 108,24,92\nQA 112,28,80\nQA 116,28,84\nQA 120,28,88\n"
"QA 124,28,92\nQAM 14,0\ns_barrier\nQCG 72\nL_pc14:\ns_lshr_b32 s74, %1, 3\nQBH 74\ns_add_u32 s74, s74, %0\ns_mul_i32 s74, s74, ((L_attn_end-L_att-1024)/127+63)&-64\n"
"s_min_u32 s74, s74, L_attn_end-L_att-1024\ns_add_u32 s74, s74, L_att-L_pc14\nQZ 72,74\nQAX 73\nQCL 138,185\nQFG 136,72,138\nQA 0,32,96\nQA 4,32,100\nQE 0,128,16384\nQA 8,32,104\nQE 2,128,20480\n"
"QA 12,32,108\nQA 16,36,96\nQE 4,129,16384\nQA 20,36,100\nQE 6,129,20480\nQA 24,36,104\nQA 28,36,108\nQE 8,130,16384\nQA 32,40,96\nQE 10,130,20480\nQA 36,40,100\nQA 40,40,104\nQE 12,131,16384\n"
"QA 44,40,108\nQE 14,131,20480\nQA 48,44,96\nQA 52,44,100\nQE 16,128,17408\nQA 56,44,104\nQE 18,128,21504\nQA 60,44,108\nQA 64,48,96\nQE 20,129,17408\nQA 68,48,100\nQE 22,129,21504\nQA 72,48,104\n"
"QA 76,48,108\nQE 24,130,17408\nQA 80,52,96\nQE 26,130,21504\nQA 84,52,100\nQA 88,52,104\nQE 28,131,17408\nQA 92,52,108\nQE 30,131,21504\nQA 96,56,96\nQA 100,56,100\nQA 104,56,104\nQA 108,56,108\n"
"QA 112,60,96\nQA 116,60,100\nQA 120,60,104\nQA 124,60,108\nQAM 7,0\ns_barrier\nQA 0,0,112\nQA 4,0,116\nQE 32,128,24576\nQA 8,0,120\nQE 34,128,28672\nQA 12,0,124\nQA 16,4,112\nQE 36,129,24576\n"
"QA 20,4,116\nQE 38,129,28672\nQA 24,4,120\nQA 28,4,124\nQE 40,130,24576\nQA 32,8,112\nQE 42,130,28672\nQA 36,8,116\nQA 40,8,120\nQE 44,131,24576\nQA 44,8,124\nQE 46,131,28672\nQA 48,12,112\n"
"QA 52,12,116\nQE 48,128,25600\nQA 56,12,120\nQE 50,128,29696\nQA 60,12,124\nQA 64,16,112\nQE 52,129,25600\nQA 68,16,116\nQE 54,129,29696\nQA 72,16,120\nQA 76,16,124\nQE 56,130,25600\nQA 80,20,112\n"
"QE 58,130,29696\nQA 84,20,116\nQA 88,20,120\nQE 60,131,25600\nQA 92,20,124\nQE 62,131,29696\nQA 96,24,112\nQA 100,24,116\nQA 104,24,120\nQA 108,24,124\nQA 112,28,112\nQA 116,28,116\nQA 120,28,120\n"
"QA 124,28,124\nQAM 3,0\ns_barrier\nQA 0,32,128\nQA 4,32,132\nQA 8,32,136\nQA 12,32,140\nQA 16,36,128\nQA 20,36,132\nQA 24,36,136\nQA 28,36,140\nQF 192,0,1\nQF 193,2,3\nQA 32,40,128\nQF 194,16,17\n"
"QF 195,18,19\nQA 36,40,132\nQF 208,4,5\nQF 209,6,7\nQA 40,40,136\nQF 210,20,21\nQF 211,22,23\nQA 44,40,140\nQF 224,8,9\nQF 225,10,11\nQA 48,44,128\nQF 226,24,25\nQF 227,26,27\nQA 52,44,132\n"
"QF 240,12,13\nQF 241,14,15\nQA 56,44,136\nQF 242,28,29\nQF 243,30,31\nQA 60,44,140\nQF 196,32,33\nQF 197,34,35\nQA 64,48,128\nQF 198,48,49\nQF 199,50,51\nQA 68,48,132\nQF 212,36,37\nQF 213,38,39\n"
"QA 72,48,136\nQF 214,52,53\nQF 215,54,55\nQA 76,48,140\nQF 228,40,41\nQF 229,42,43\nQA 80,52,128\nQF 230,56,57\nQF 231,58,59\nQA 84,52,132\nQF 244,44,45\nQF 245,46,47\nQA 88,52,136\nQF 246,60,61\n"
"QF 247,62,63\nQA 92,52,140\nQF 200,64,65\nQF 201,66,67\nQA 96,56,128\nQF 202,80,81\nQF 203,82,83\nQA 100,56,132\nQF 216,68,69\nQF 217,70,71\nQA 104,56,136\nQF 218,84,85\nQF 219,86,87\nQA 108,56,140\n"
"QF 232,72,73\nQF 233,74,75\nQA 112,60,128\nQF 234,88,89\nQF 235,90,91\nQA 116,60,132\nQF 248,76,77\nQF 249,78,79\nQA 120,60,136\nQF 250,92,93\nQF 251,94,95\nQA 124,60,140\nQF 204,96,97\nQF 205,98,99\n"
"QF 206,112,113\nQF 207,114,115\nQF 220,100,101\nQF 221,102,103\nQF 222,116,117\nQF 223,118,119\nQF 236,104,105\nQF 237,106,107\nQF 238,120,121\nQF 239,122,123\nQF 252,108,109\nQF 253,110,111\n"
"QF 254,124,125\nQF 255,126,127\nQAG 176\nQAG 177\nQAG 178\nQAG 179\nQAG 96\nQAG 97\nQAG 98\nQAG 99\nQAG 100\nQAG 101\nQAG 102\nQAG 103\nQAG 104\nQAG 105\nQAG 106\nQAG 107\nQAG 108\nQAG 109\nQAG 110\n"
"QAG 111\nQAG 112\nQAG 113\nQAG 114\nQAG 115\nQAG 116\nQAG 117\nQAG 118\nQAG 119\nQAG 120\nQAG 121\nQAG 122\nQAG 123\nQAG 124\nQAG 125\nQAG 126\nQAG 127\nQN 180,185\nv_add_u32 v181, 0xc000, v185\n"
"v_mov_b32 v184, 0xff800000\nQCN 160\nQCN 161\nQCN 162\nQCN 163\ns_mov_b64 s[76:77], s[36:37]\ns_mov_b64 s[78:79], s[38:39]\nQAH 0\ns_branch L_att\nL_setX:\nQDO \nQAH 24\nQO 64,96\nQO 65,97\nQO 66,98\n"
"QO 67,99\nQO 68,100\nQO 69,101\nQO 70,102\nQO 71,103\nQO 72,32\nQO 73,33\nQO 74,34\nQO 75,35\nQO 76,36\nQO 77,37\nQO 78,38\nQO 79,39\nQO 80,104\nQO 81,105\nQO 82,106\nQO 83,107\nQO 84,108\nQO 85,109\n"
"QO 86,110\nQO 87,111\nQO 88,40\nQO 89,41\nQO 90,42\nQO 91,43\nQO 92,44\nQO 93,45\nQO 94,46\nQO 95,47\nQO 96,112\nQO 97,113\nQO 98,114\nQO 99,115\nQO 100,116\nQO 101,117\nQO 102,118\nQO 103,119\n"
"QO 104,48\nQO 105,49\nQO 106,50\nQO 107,51\nQO 108,52\nQO 109,53\nQO 110,54\nQO 111,55\nQO 112,120\nQO 113,121\nQO 114,122\nQO 115,123\nQO 116,124\nQO 117,125\nQO 118,126\nQO 119,127\nQO 120,56\n"
"QO 121,57\nQO 122,58\nQO 123,59\nQO 124,60\nQO 125,61\nQO 126,62\nQO 127,63\ns_mov_b64 s[76:77], %2\nQGN 76\nQAX 77\nQDL 78\nQBA 79\nQDU 143,185\nQEU 132,185\nv_add_u32 v133, 20480, v185\n"
"v_add_u32 v134, 40960, v185\nv_add_u32 v135, 61440, v185\nQDS 140,143\nv_lshlrev_b32 v140, 11, v140\nv_bfe_u32 v141, v143, 2, 3\nv_lshl_add_u32 v140, v141, 7, v140\nv_and_b32 v142, 3, v143\n"
"QFX 140,142\nQEV 141\nv_lshl_add_u32 v140, v141, 5, v140\nv_xor_b32 v128, 0, v140\nv_xor_b32 v129, 32, v140\nQGJ 130,140\nv_xor_b32 v131, 96, v140\nQAM 17,0\ns_barrier\nQE 0,128,0\nQE 2,128,4096\n"
"QE 4,129,0\nQE 6,129,4096\nQE 8,130,0\nQE 10,130,4096\nQE 12,131,0\nQE 14,131,4096\nQE 16,128,1024\nQE 18,128,5120\nQE 20,129,1024\nQE 22,129,5120\nQE 24,130,1024\nQE 26,130,5120\nQE 28,131,1024\n"
"QE 30,131,5120\nQAM 14,0\ns_barrier\nQAE 0,0,64\nQEM 86\nQN 159,249\ns_mov_b64 s[40:41], %2\nQAE 4,0,68\nQE 32,128,8192\ns_add_u32 s40, s40, 55574528\nQAX 41\nQAE 8,0,72\nQE 34,128,12288\nQDL 42\n"
"QAE 12,0,76\nQBB 88,250,85\nQAE 16,4,64\nQE 36,129,8192\nQBA 43\nQAE 20,4,68\nQE 38,129,12288\nQDC 50\nQAE 24,4,72\nQS 88,251,85,1024\nQAE 28,4,76\nQE 40,130,8192\nQAO 85\nQAE 32,8,64\n"
"QE 42,130,12288\nQDC 51\nQAE 36,8,68\nv_and_b32 v182, 0x300, v185\nv_bfe_u32 v180, v185, 4, 4\nQAE 40,8,72\nQE 44,131,8192\ns_lshl_b32 s83, s54, 4\nQAE 44,8,76\nQE 46,131,12288\n"
"v_add_u32 v180, s83, v180\nQAE 48,12,64\ns_mov_b32 s84, 20480\nv_add_u32 v181, -1, v180\nQEH 148,181\nQAE 52,12,68\nQE 48,128,9216\nQFO 148,182\nQAE 56,12,72\nQE 50,128,13312\nQFP 181\nQAE 60,12,76\n"
"QED 148,181,84\nv_add_u32 v181, 15, v180\nQAE 64,16,64\nQE 52,129,9216\nQEH 149,181\nQAE 68,16,68\nQE 54,129,13312\nQFO 149,182\nQAE 72,16,72\nQFP 181\nQED 149,181,84\nQAE 76,16,76\nQE 56,130,9216\n"
"v_add_u32 v181, 1, v180\nQAE 80,20,64\nQE 58,130,13312\nQEH 150,181\nQAE 84,20,68\nQFO 150,182\nQFP 181\nQAE 88,20,72\nQE 60,131,9216\nQED 150,181,84\nQAE 92,20,76\nQE 62,131,13312\n"
"v_add_u32 v181, 17, v180\nQAE 96,24,64\nQEH 151,181\nQFO 151,182\nQAE 100,24,68\nQFP 181\nQED 151,181,84\nQAE 104,24,72\nv_add_u32 v181, -2, v180\nQEH 152,181\nQAE 108,24,76\nQFO 152,182\nQFP 181\n"
"QAE 112,28,64\nQED 152,181,84\nv_add_u32 v181, 14, v180\nQAE 116,28,68\nQEH 153,181\nQFO 153,182\nQAE 120,28,72\nQFP 181\nQED 153,181,84\nQAE 124,28,76\nv_add_u32 v181, 2, v180\nQEH 154,181\n"
"QAM 13,0\ns_barrier\nQA 0,32,80\nQDF 86\nQFO 154,182\nQFP 181\nQA 4,32,84\nQE 0,128,16384\nQED 154,181,84\nQA 8,32,88\nQE 2,128,20480\nv_add_u32 v181, 18, v180\nQA 12,32,92\nQBB 88,250,85\n"
"QA 16,36,80\nQE 4,129,16384\nQEH 155,181\nQA 20,36,84\nQE 6,129,20480\nQFO 155,182\nQA 24,36,88\nQS 88,251,85,1024\nQA 28,36,92\nQE 8,130,16384\nQAO 85\nQA 32,40,80\nQE 10,130,20480\nQAA 87\n"
"QA 36,40,84\nQJ 128,76,132,87,0\nQA 40,40,88\nQE 12,131,16384\nQFP 181\nQA 44,40,92\nQE 14,131,20480\nQED 155,181,84\nQA 48,44,80\nQJ 132,76,133,87,0\nQA 52,44,84\nQE 16,128,17408\n"
"s_mov_b64 s[44:45], %2\nQA 56,44,88\nQE 18,128,21504\nQGN 44\nQAX 45\nQA 60,44,92\nQJ 136,76,134,87,0\nQA 64,48,80\nQE 20,129,17408\nQDL 46\nQA 68,48,84\nQE 22,129,21504\nQBA 47\nQA 72,48,88\n"
"QJ 140,76,135,87,0\nQA 76,48,92\nQE 24,130,17408\ns_mul_i32 s60, s55, 5\nQA 80,52,80\nQE 26,130,21504\nQGE 61,54\nQA 84,52,84\nQA 88,52,88\nQE 28,131,17408\nQA 92,52,92\nQE 30,131,21504\nQA 96,56,80\n"
"QA 100,56,84\nQA 104,56,88\nQA 108,56,92\nQA 112,60,80\nQA 116,60,84\nQA 120,60,88\nQA 124,60,92\nQAM 6,0\ns_barrier\nQCR 158\ns_bfe_u32 s99, %1, 196608\nQBH 99\ns_bfe_u32 s100, %1, 131078\n"
"QZ 99,100\nQBU 99\ns_add_u32 s99, s99, 2048\nQZ 99,96\nQEI \nQEJ 92,158,99\nQEK \nQA 0,0,96\nQEN 86\nQA 4,0,100\nQE 32,128,24576\nQA 8,0,104\nQE 34,128,28672\nQA 12,0,108\nQBB 88,250,85\nQA 16,4,96\n"
"QE 36,129,24576\nQA 20,4,100\nQE 38,129,28672\nQA 24,4,104\nQS 88,251,85,1024\nQA 28,4,108\nQE 40,130,24576\nQAO 85\nQA 32,8,96\nQE 42,130,28672\nQA 36,8,100\nQJ 64,76,132,87,1024\nQA 40,8,104\n"
"QE 44,131,24576\nQA 44,8,108\nQE 46,131,28672\nQA 48,12,96\nQJ 68,76,133,87,1024\nQA 52,12,100\nQE 48,128,25600\nQA 56,12,104\nQE 50,128,29696\nQA 60,12,108\nQJ 72,76,134,87,1024\nQA 64,16,96\n"
"QE 52,129,25600\nQA 68,16,100\nQE 54,129,29696\nQA 72,16,104\nQJ 76,76,135,87,1024\nQA 76,16,108\nQE 56,130,25600\nQA 80,20,96\nQE 58,130,29696\nQA 84,20,100\nQA 88,20,104\nQE 60,131,25600\n"
"QA 92,20,108\nQE 62,131,29696\nQA 96,24,96\nQA 100,24,100\nQA 104,24,104\nQA 108,24,108\nQA 112,28,96\nQA 116,28,100\nQA 120,28,104\nQA 124,28,108\nQAM 11,0\ns_barrier\nQA 0,32,112\nQBV 86\n"
"QA 4,32,116\nQE 0,128,32768\nQA 8,32,120\nQE 2,128,36864\nQA 12,32,124\nQBB 88,250,85\nQA 16,36,112\nQE 4,129,32768\nQA 20,36,116\nQE 6,129,36864\nQA 24,36,120\nQS 88,251,85,1024\nQA 28,36,124\n"
"QE 8,130,32768\nQAO 85\nQA 32,40,112\nQE 10,130,36864\nQA 36,40,116\nQJ 80,76,132,87,2048\nQA 40,40,120\nQE 12,131,32768\nQA 44,40,124\nQE 14,131,36864\nQA 48,44,112\nQJ 84,76,133,87,2048\n"
"QA 52,44,116\nQE 16,128,33792\nQA 56,44,120\nQE 18,128,37888\nQA 60,44,124\nQJ 88,76,134,87,2048\nQA 64,48,112\nQE 20,129,33792\nQA 68,48,116\nQE 22,129,37888\nQA 72,48,120\nQJ 92,76,135,87,2048\n"
"QA 76,48,124\nQE 24,130,33792\nQA 80,52,112\nQE 26,130,37888\nQA 84,52,116\nQA 88,52,120\nQE 28,131,33792\nQA 92,52,124\nQE 30,131,37888\nQA 96,56,112\nQA 100,56,116\nQA 104,56,120\nQA 108,56,124\n"
"QA 112,60,112\nQA 116,60,116\nQA 120,60,120\nQA 124,60,124\nQAM 10,0\ns_barrier\nQA 0,0,128\nQDV 86\nQA 4,0,132\nQE 32,128,40960\nQA 8,0,136\nQE 34,128,45056\nQA 12,0,140\nQBB 88,250,85\nQA 16,4,128\n"
"QE 36,129,40960\nQA 20,4,132\nQE 38,129,45056\nQA 24,4,136\nQS 88,251,85,1024\nQA 28,4,140\nQE 40,130,40960\nQAO 85\nQA 32,8,128\nQE 42,130,45056\nQA 36,8,132\nQJ 96,76,132,87,3072\nQA 40,8,136\n"
"QE 44,131,40960\nQA 44,8,140\nQE 46,131,45056\nQA 48,12,128\nQJ 100,76,133,87,3072\nQA 52,12,132\nQE 48,128,41984\nQA 56,12,136\nQE 50,128,46080\nQA 60,12,140\nQJ 104,76,134,87,3072\nQA 64,16,128\n"
"QE 52,129,41984\nQA 68,16,132\nQE 54,129,46080\nQA 72,16,136\nQJ 108,76,135,87,3072\nQA 76,16,140\nQE 56,130,41984\nQFL 49\nQA 80,20,128\nQE 58,130,46080\nQA 84,20,132\nQL 36,159,48,0\nQA 88,20,136\n"
"QE 60,131,41984\nQA 92,20,140\nQE 62,131,46080\nQA 96,24,128\nQA 100,24,132\nQL 36,159,48,1024\nQA 104,24,136\nQA 108,24,140\nQA 112,28,128\nQA 116,28,132\nQA 120,28,136\nQA 124,28,140\nQAM 12,0\n"
"s_barrier\nQA 0,32,64\nQDW 86\nQA 4,32,68\nQE 0,128,0\nQA 8,32,72\nQE 2,128,4096\nQA 12,32,76\nQBB 88,250,85\nQA 16,36,64\nQE 4,129,0\nQA 20,36,68\nQE 6,129,4096\nQA 24,36,72\nQS 88,251,85,1024\n"
"QA 28,36,76\nQE 8,130,0\nQAO 85\nQA 32,40,64\nQE 10,130,4096\nQAA 87\nQA 36,40,68\nQJ 112,76,132,87,0\nQA 40,40,72\nQE 12,131,0\nQA 44,40,76\nQE 14,131,4096\nQA 48,44,64\nQJ 116,76,133,87,0\n"
"QA 52,44,68\nQE 16,128,1024\nQA 56,44,72\nQE 18,128,5120\nQA 60,44,76\nQJ 120,76,134,87,0\nQA 64,48,64\nQE 20,129,1024\nQA 68,48,68\nQE 22,129,5120\nQA 72,48,72\nQJ 124,76,135,87,0\nQA 76,48,76\n"
"QE 24,130,1024\nQFL 49\nQA 80,52,64\nQE 26,130,5120\nQA 84,52,68\nQL 36,159,48,2048\nQA 88,52,72\nQE 28,131,1024\nQA 92,52,76\nQE 30,131,5120\nQA 96,56,64\nQA 100,56,68\nQL 36,159,48,3072\n"
"QA 104,56,72\nQA 108,56,76\nQA 112,60,64\nQA 116,60,68\nQA 120,60,72\nQA 124,60,76\nQAM 14,0\ns_barrier\nQA 0,0,80\nQEM 86\nQA 4,0,84\nQE 32,128,8192\nQA 8,0,88\nQE 34,128,12288\nQA 12,0,92\n"
"QBB 88,250,85\nQA 16,4,80\nQE 36,129,8192\nQA 20,4,84\nQE 38,129,12288\nQA 24,4,88\nQS 88,251,85,1024\nQA 28,4,92\nQE 40,130,8192\nQAO 85\nQA 32,8,80\nQE 42,130,12288\nQA 36,8,84\n"
"QJ 128,76,132,87,1024\nQA 40,8,88\nQE 44,131,8192\nQA 44,8,92\nQE 46,131,12288\nQA 48,12,80\nQJ 132,76,133,87,1024\nQA 52,12,84\nQE 48,128,9216\nQA 56,12,88\nQE 50,128,13312\nQA 60,12,92\n"
"QJ 136,76,134,87,1024\nQA 64,16,80\nQE 52,129,9216\nQA 68,16,84\nQE 54,129,13312\nQA 72,16,88\nQJ 140,76,135,87,1024\nQA 76,16,92\nQE 56,130,9216\nQDY 49\nQA 80,20,80\nQE 58,130,13312\nQAA 48\n"
"QA 84,20,84\nQL 36,159,48,0\nQA 88,20,88\nQE 60,131,9216\nQBP 48\nQA 92,20,92\nQE 62,131,13312\nQA 96,24,80\nQA 100,24,84\nQA 104,24,88\nQA 108,24,92\nQA 112,28,80\nQA 116,28,84\nQA 120,28,88\n"
"QA 124,28,92\nQAM 13,0\ns_barrier\nQA 0,32,96\nQDF 86\nQA 4,32,100\nQE 0,128,16384\nQA 8,32,104\nQE 2,128,20480\nQA 12,32,108\nQBB 88,250,85\nQA 16,36,96\nQE 4,129,16384\nQA 20,36,100\n"
"QE 6,129,20480\nQA 24,36,104\nQS 88,251,85,1024\nQA 28,36,108\nQE 8,130,16384\nQAO 85\nQA 32,40,96\nQE 10,130,20480\nQA 36,40,100\nQJ 64,76,132,87,2048\nQA 40,40,104\nQE 12,131,16384\nQA 44,40,108\n"
"QE 14,131,20480\nQA 48,44,96\nQJ 68,76,133,87,2048\nQA 52,44,100\nQE 16,128,17408\nQA 56,44,104\nQE 18,128,21504\nQA 60,44,108\nQJ 72,76,134,87,2048\nQA 64,48,96\nQE 20,129,17408\nQA 68,48,100\n"
"QE 22,129,21504\nQA 72,48,104\nQJ 76,76,135,87,2048\nQA 76,48,108\nQE 24,130,17408\nQCJ 62,60\nQA 80,52,96\nQE 26,130,21504\nQGM 62\nQA 84,52,100\nQBX 63,60\nQCK 63\nQZ 62,63\nQA 88,52,104\n"
"QE 28,131,17408\nQZ 62,61\nQA 92,52,108\nQE 30,131,21504\nQCE 60\nQA 96,56,96\nQA 100,56,100\nQDI 160,44,185,62\nQA 104,56,104\nQA 108,56,108\nQA 112,60,96\nQDI 170,44,147,62\nQA 116,60,100\n"
"QA 120,60,104\nQA 124,60,108\nQAM 13,0\ns_barrier\nQA 0,0,112\nQEN 86\nQA 4,0,116\nQE 32,128,24576\nQA 8,0,120\nQE 34,128,28672\nQA 12,0,124\nQBB 88,250,85\nQA 16,4,112\nQE 36,129,24576\nQA 20,4,116\n"
"QE 38,129,28672\nQA 24,4,120\nQS 88,251,85,1024\nQA 28,4,124\nQE 40,130,24576\nQAO 85\nQA 32,8,112\nQE 42,130,28672\nQA 36,8,116\nQJ 80,76,132,87,3072\nQA 40,8,120\nQE 44,131,24576\nQA 44,8,124\n"
"QE 46,131,28672\nQA 48,12,112\nQJ 84,76,133,87,3072\nQA 52,12,116\nQE 48,128,25600\nQA 56,12,120\nQE 50,128,29696\nQA 60,12,124\nQJ 88,76,134,87,3072\nQA 64,16,112\nQE 52,129,25600\nQA 68,16,116\n"
"QE 54,129,29696\nQA 72,16,120\nQJ 92,76,135,87,3072\nQA 76,16,124\nQE 56,130,25600\nQCJ 62,60\nQA 80,20,112\nQE 58,130,29696\nQGM 62\nQA 84,20,116\nQBX 63,60\nQCK 63\nQZ 62,63\nQA 88,20,120\n"
"QE 60,131,25600\nQZ 62,61\nQA 92,20,124\nQE 62,131,29696\nQCE 60\nQA 96,24,112\nQA 100,24,116\nQDI 162,44,185,62\nQA 104,24,120\nQA 108,24,124\nQA 112,28,112\nQDI 172,44,147,62\nQA 116,28,116\n"
"QA 120,28,120\nQA 124,28,124\nQAM 14,0\ns_barrier\nQCG 100\nL_pc15:\ns_lshr_b32 s99, %1, 2\nQGB 99\ns_bitcmp1_b32 %0, 0\nQGC 99\nQER 99\ns_min_u32 s99, s99, L_done-L_pc15-512\nQZ 100,99\nQAX 101\n"
"QES 138,185\nQGD 100,136,138\nQA 0,32,128\nQBV 86\nQA 4,32,132\nQE 0,128,32768\nQA 8,32,136\nQE 2,128,36864\nQA 12,32,140\nQBB 88,250,85\nQA 16,36,128\nQE 4,129,32768\nQA 20,36,132\nQE 6,129,36864\n"
"QA 24,36,136\nQS 88,251,85,1024\nQA 28,36,140\nQE 8,130,32768\nQAO 85\nQA 32,40,128\nQE 10,130,36864\nQAA 87\nQA 36,40,132\nQJ 96,76,132,87,0\nQA 40,40,136\nQE 12,131,32768\nQA 44,40,140\n"
"QE 14,131,36864\nQA 48,44,128\nQJ 100,76,133,87,0\nQA 52,44,132\nQE 16,128,33792\nQA 56,44,136\nQE 18,128,37888\nQA 60,44,140\nQJ 104,76,134,87,0\nQA 64,48,128\nQE 20,129,33792\nQA 68,48,132\n"
"QE 22,129,37888\nQA 72,48,136\nQJ 108,76,135,87,0\nQA 76,48,140\nQE 24,130,33792\nQCJ 62,60\nQA 80,52,128\nQE 26,130,37888\nQGM 62\nQA 84,52,132\nQBX 63,60\nQCK 63\nQZ 62,63\nQA 88,52,136\n"
"QE 28,131,33792\nQZ 62,61\nQA 92,52,140\nQE 30,131,37888\nQCE 60\nQA 96,56,128\nQA 100,56,132\nQDI 164,44,185,62\nQA 104,56,136\nQA 108,56,140\nQA 112,60,128\nQDI 174,44,147,62\nQA 116,60,132\n"
"QA 120,60,136\nQA 124,60,140\nQAM 15,0\ns_barrier\nQA 0,0,64\nQDV 86\nQA 4,0,68\nQE 32,128,40960\nQA 8,0,72\nQE 34,128,45056\nQA 12,0,76\nQBB 88,250,85\nQA 16,4,64\nQE 36,129,40960\nQA 20,4,68\n"
"QE 38,129,45056\nQA 24,4,72\nQS 88,251,85,1024\nQA 28,4,76\nQE 40,130,40960\nQAO 85\nQA 32,8,64\nQE 42,130,45056\nQA 36,8,68\nQJ 112,76,132,87,1024\nQA 40,8,72\nQE 44,131,40960\nQA 44,8,76\n"
"QE 46,131,45056\nQA 48,12,64\nQJ 116,76,133,87,1024\nQA 52,12,68\nQE 48,128,41984\nQA 56,12,72\nQE 50,128,46080\nQA 60,12,76\nQJ 120,76,134,87,1024\nQA 64,16,64\nQE 52,129,41984\nQA 68,16,68\n"
"QE 54,129,46080\nQA 72,16,72\nQJ 124,76,135,87,1024\nQA 76,16,76\nQE 56,130,41984\nQCJ 62,60\nQA 80,20,64\nQE 58,130,46080\nQGM 62\nQA 84,20,68\nQBX 63,60\nQCK 63\nQZ 62,63\nQA 88,20,72\n"
"QE 60,131,41984\nQZ 62,61\nQA 92,20,76\nQE 62,131,46080\nQCE 60\nQA 96,24,64\nQA 100,24,68\nQDI 166,44,185,62\nQA 104,24,72\nQA 108,24,76\nQA 112,28,64\nQDI 176,44,147,62\nQA 116,28,68\nQA 120,28,72\n"
"QA 124,28,76\nQAM 14,0\ns_barrier\nQA 0,32,80\nQDW 86\nQA 4,32,84\nQE 0,128,0\nQA 8,32,88\nQE 2,128,4096\nQA 12,32,92\nQBB 88,250,85\nQA 16,36,80\nQE 4,129,0\nQA 20,36,84\nQE 6,129,4096\nQA 24,36,88\n"
"QS 88,251,85,1024\nQA 28,36,92\nQE 8,130,0\nQAO 85\nQA 32,40,80\nQE 10,130,4096\nQA 36,40,84\nQJ 128,76,132,87,2048\nQA 40,40,88\nQE 12,131,0\nQA 44,40,92\nQE 14,131,4096\nQA 48,44,80\n"
"QJ 132,76,133,87,2048\nQA 52,44,84\nQE 16,128,1024\nQA 56,44,88\nQE 18,128,5120\nQA 60,44,92\nQJ 136,76,134,87,2048\nQA 64,48,80\nQE 20,129,1024\nQA 68,48,84\nQE 22,129,5120\nQA 72,48,88\n"
"QJ 140,76,135,87,2048\nQA 76,48,92\nQE 24,130,1024\nQCJ 62,60\nQA 80,52,80\nQE 26,130,5120\nQGM 62\nQA 84,52,84\nQBX 63,60\nQCK 63\nQZ 62,63\nQA 88,52,88\nQE 28,131,1024\nQZ 62,61\nQA 92,52,92\n"
"QE 30,131,5120\nQCE 60\nQA 96,56,80\nQA 100,56,84\nQDI 168,44,185,62\nQA 104,56,88\nQA 108,56,92\nQA 112,60,80\nQDI 178,44,147,62\nQA 116,60,84\nQA 120,60,88\nQA 124,60,92\nQAM 14,0\ns_barrier\n"
"QA 0,0,96\nQEM 86\ns_lshl_b32 s67, s81, 1\nQBT 83,67,80\nQA 4,0,100\nQE 32,128,8192\nQCS 83\nQA 8,0,104\nQE 34,128,12288\nQFY 80\ns_cselect_b32 s65, 28, s83\nQA 12,0,108\nQBB 88,250,85\nQA 16,4,96\n"
"QE 36,129,8192\ns_sub_u32 s66, s65, 1\nQA 20,4,100\nQE 38,129,12288\nQFE 83,80\nQA 24,4,104\nQS 88,251,85,1024\nQA 28,4,108\nQE 40,130,8192\nQAO 85\nQA 32,8,96\nQE 42,130,12288\nQZ 83,81\n"
"QA 36,8,100\nQJ 64,76,132,87,3072\nQA 40,8,104\nQE 44,131,8192\ns_cmp_eq_u32 s83, 7\ns_cselect_b32 s66, 16, s66\nQA 44,8,108\nQE 46,131,12288\nQHA 68,66\nQA 48,12,96\nQJ 68,76,133,87,3072\n"
"QA 52,12,100\nQE 48,128,9216\nQHA 83,65\nQA 56,12,104\nQE 50,128,13312\nQHA 84,67\nQA 60,12,108\nQJ 72,76,134,87,3072\nQA 64,16,96\nQE 52,129,9216\ns_or_b32 s69, s68, s83\nQA 68,16,100\n"
"QE 54,129,13312\ns_or_b32 s69, s69, s84\nQA 72,16,104\nQJ 76,76,135,87,3072\nQA 76,16,108\nQE 56,130,9216\ns_or_b32 s69, s69, 131072\nQA 80,20,96\nQE 58,130,13312\nQDC 64\nQA 84,20,100\n"
"s_mov_b32 s71, 0x53800000\ns_lshl_b32 s83, s82, 20\nQEA 56,83\nQA 88,20,104\nQE 60,131,9216\ns_add_u32 s57, s83, 16384\nQA 92,20,108\nQE 62,131,13312\ns_add_u32 s58, s83, 1048576\nQA 96,24,96\n"
"s_add_u32 s59, s83, 1097728\nQGI 83,80\ns_add_u32 s60, s83, 0x4000\nQA 100,24,100\ns_add_u32 s61, s83, 0x8000\ns_add_u32 s62, s83, 81920\nQCU 63,83\nQA 104,24,104\nQBX 83,80\nQCJ 84,80\n"
"s_xor_b32 s83, s83, s84\nQA 108,24,108\nQGA 83\nQEC 83\nQBM 186,83,185\nQA 112,28,96\nQA 116,28,100\nQA 120,28,104\nQA 124,28,108\nQAM 12,0\ns_barrier\nQA 0,32,112\nQDF 86\nQA 4,32,116\n"
"QE 0,128,16384\nQA 8,32,120\nQE 2,128,20480\nQA 12,32,124\nQBB 88,250,85\nQA 16,36,112\nQE 4,129,16384\nQA 20,36,116\nQE 6,129,20480\nQA 24,36,120\nQS 88,251,85,1024\nQA 28,36,124\nQE 8,130,16384\n"
"QAO 85\nQA 32,40,112\nQE 10,130,20480\nQAA 87\nQA 36,40,116\nQJ 80,76,132,87,0\nQA 40,40,120\nQE 12,131,16384\nQA 44,40,124\nQE 14,131,20480\nQA 48,44,112\nQJ 84,76,133,87,0\nQA 52,44,116\n"
"QE 16,128,17408\nQA 56,44,120\nQE 18,128,21504\nQA 60,44,124\nQJ 88,76,134,87,0\nQA 64,48,112\nQE 20,129,17408\nQA 68,48,116\nQE 22,129,21504\nQA 72,48,120\nQJ 92,76,135,87,0\nQA 76,48,124\n"
"QE 24,130,17408\nQA 80,52,112\nQE 26,130,21504\nQA 84,52,116\nQA 88,52,120\nQE 28,131,17408\nQA 92,52,124\nQE 30,131,21504\nQA 96,56,112\nQA 100,56,116\nQA 104,56,120\nQA 108,56,124\nQA 112,60,112\n"
"QA 116,60,116\nQA 120,60,120\nQA 124,60,124\nQAM 10,0\ns_barrier\nQA 0,0,128\nQEN 86\nQA 4,0,132\nQE 32,128,24576\nQA 8,0,136\nQE 34,128,28672\nQA 12,0,140\nQBB 88,250,85\nQA 16,4,128\n"
"QE 36,129,24576\nQA 20,4,132\nQE 38,129,28672\nQA 24,4,136\nQS 88,251,85,1024\nQA 28,4,140\nQE 40,130,24576\nQAO 85\nQA 32,8,128\nQE 42,130,28672\nQA 36,8,132\nQJ 96,76,132,87,1024\nQA 40,8,136\n"
"QE 44,131,24576\nQA 44,8,140\nQE 46,131,28672\nQA 48,12,128\nQJ 100,76,133,87,1024\nQA 52,12,132\nQE 48,128,25600\nQA 56,12,136\nQE 50,128,29696\nQA 60,12,140\nQJ 104,76,134,87,1024\nQA 64,16,128\n"
"QE 52,129,25600\nQA 68,16,132\nQE 54,129,29696\nQA 72,16,136\nQJ 108,76,135,87,1024\nQA 76,16,140\nQE 56,130,25600\nQA 80,20,128\nQE 58,130,29696\nQA 84,20,132\nQA 88,20,136\nQE 60,131,25600\n"
"QA 92,20,140\nQE 62,131,29696\nQA 96,24,128\nQA 100,24,132\nQA 104,24,136\nQA 108,24,140\nQA 112,28,128\nQA 116,28,132\nQA 120,28,136\nQA 124,28,140\nQAM 10,0\ns_barrier\ns_bfe_u32 s99, %1, 196608\n"
"QBH 99\ns_bfe_u32 s100, %1, 131078\nQZ 99,100\ns_sub_u32 s100, s99, 17\nQDU 156,185\nQBM 157,100,156\nv_med3_i32 v157, v157, 0, 15\nv_cmp_lt_u32 vcc, 2, v156\nQBJ 156,99\nQBE \nQDG 157,156\n"
"v_lshlrev_b32 v157, 7, v157\nbuffer_load_dword v156, v157, s[92:95], 0 offen offset:2048 sc1\nQA 0,32,64\nQBV 86\nQA 4,32,68\nQE 0,128,32768\nQA 8,32,72\nQE 2,128,36864\nQA 12,32,76\nQBB 88,250,85\n"
"QA 16,36,64\nQE 4,129,32768\nQA 20,36,68\nQE 6,129,36864\nQA 24,36,72\nQS 88,251,85,1024\nQA 28,36,76\nQE 8,130,32768\nQAO 85\nQA 32,40,64\nQE 10,130,36864\nQA 36,40,68\nQJ 112,76,132,87,2048\n"
"QA 40,40,72\nQE 12,131,32768\nQA 44,40,76\nQE 14,131,36864\nQA 48,44,64\nQJ 116,76,133,87,2048\nQA 52,44,68\nQE 16,128,33792\nQA 56,44,72\nQE 18,128,37888\nQA 60,44,76\nQJ 120,76,134,87,2048\n"
"QA 64,48,64\nQE 20,129,33792\nQA 68,48,68\nQE 22,129,37888\nQA 72,48,72\nQJ 124,76,135,87,2048\nQA 76,48,76\nQE 24,130,33792\nQA 80,52,64\nQE 26,130,37888\nQA 84,52,68\nQA 88,52,72\nQE 28,131,33792\n"
"QA 92,52,76\nQE 30,131,37888\nQA 96,56,64\nQA 100,56,68\nQA 104,56,72\nQA 108,56,76\nQA 112,60,64\nQA 116,60,68\nQA 120,60,72\nQA 124,60,76\nQAM 11,0\ns_barrier\nQA 0,0,80\nQDV 86\nQA 4,0,84\n"
"QE 32,128,40960\nQA 8,0,88\nQE 34,128,45056\nQA 12,0,92\nQBB 88,250,85\nQA 16,4,80\nQE 36,129,40960\nQA 20,4,84\nQE 38,129,45056\nQA 24,4,88\nQS 88,251,85,1024\nQA 28,4,92\nQE 40,130,40960\nQAO 85\n"
"QA 32,8,80\nQE 42,130,45056\nQA 36,8,84\nQJ 128,76,132,87,3072\nQA 40,8,88\nQE 44,131,40960\nQA 44,8,92\nQE 46,131,45056\nQA 48,12,80\nQJ 132,76,133,87,3072\nQA 52,12,84\nQE 48,128,41984\n"
"QA 56,12,88\nQE 50,128,46080\nQA 60,12,92\nQJ 136,76,134,87,3072\nQA 64,16,80\nQE 52,129,41984\nQA 68,16,84\nQE 54,129,46080\nQA 72,16,88\nQJ 140,76,135,87,3072\nQA 76,16,92\nQE 56,130,41984\n"
"QA 80,20,80\nQE 58,130,46080\nQA 84,20,84\nQA 88,20,88\nQE 60,131,41984\nQA 92,20,92\nQE 62,131,46080\nQA 96,24,80\nQA 100,24,84\nQA 104,24,88\nQA 108,24,92\nQA 112,28,80\nQA 116,28,84\nQA 120,28,88\n"
"QA 124,28,92\nQAM 10,0\ns_barrier\nv_cmp_ne_u32 vcc, 8, v156\nQBD \ns_cbranch_vccnz L_ps16\nL_pr16:\nQA 0,32,96\nQA 4,32,100\nQE 0,128,0\nQA 8,32,104\nQE 2,128,4096\nQA 12,32,108\nQJ 144,40,148,50,0\n"
"QA 16,36,96\nQE 4,129,0\nQA 20,36,100\nQE 6,129,4096\nQA 24,36,104\nQJ 148,40,149,51,0\nQA 28,36,108\nQE 8,130,0\nQA 32,40,96\nQE 10,130,4096\nQA 36,40,100\nQJ 152,40,148,50,1024\nQA 40,40,104\n"
"QE 12,131,0\nQA 44,40,108\nQE 14,131,4096\nQA 48,44,96\nQJ 156,40,149,51,1024\nQA 52,44,100\nQE 16,128,1024\nQA 56,44,104\nQE 18,128,5120\nQA 60,44,108\nQJ 160,40,148,50,2048\nQA 64,48,96\n"
"QE 20,129,1024\nQA 68,48,100\nQE 22,129,5120\nQA 72,48,104\nQJ 164,40,149,51,2048\nQA 76,48,108\nQE 24,130,1024\nQA 80,52,96\nQE 26,130,5120\nQA 84,52,100\nQJ 168,40,148,50,3072\nQA 88,52,104\n"
"QE 28,131,1024\nQA 92,52,108\nQE 30,131,5120\nQA 96,56,96\nQA 100,56,100\nQJ 172,40,149,51,3072\nQA 104,56,104\nQAA 50\nQA 108,56,108\nQA 112,60,96\nQJ 176,40,148,50,0\nQA 116,60,100\nQAA 51\n"
"QA 120,60,104\nQA 124,60,108\nQJ 180,40,149,51,0\nQAM 14,0\ns_barrier\nQA 0,0,112\nQA 4,0,116\nQE 32,128,8192\nQA 8,0,120\nQE 34,128,12288\nQA 12,0,124\nQJ 184,40,150,50,1024\nQA 16,4,112\n"
"QE 36,129,8192\nQA 20,4,116\nQE 38,129,12288\nQA 24,4,120\nQJ 188,40,151,51,1024\nQA 28,4,124\nQE 40,130,8192\nQA 32,8,112\nQE 42,130,12288\nQA 36,8,116\nQJ 192,40,150,50,2048\nQA 40,8,120\n"
"QE 44,131,8192\nQA 44,8,124\nQE 46,131,12288\nQA 48,12,112\nQJ 196,40,151,51,2048\nQA 52,12,116\nQE 48,128,9216\nQA 56,12,120\nQE 50,128,13312\nQA 60,12,124\nQJ 200,40,150,50,3072\nQA 64,16,112\n"
"QE 52,129,9216\nQA 68,16,116\nQE 54,129,13312\nQA 72,16,120\nQJ 204,40,151,51,3072\nQA 76,16,124\nQE 56,130,9216\nQA 80,20,112\nQE 58,130,13312\nQA 84,20,116\nQA 88,20,120\nQE 60,131,9216\n"
"QA 92,20,124\nQE 62,131,13312\nQA 96,24,112\nQA 100,24,116\nQA 104,24,120\nQA 108,24,124\nQA 112,28,112\nQA 116,28,116\nQA 120,28,120\nQA 124,28,124\nQAM 16,0\ns_barrier\nQCG 72\nL_pc17:\n"
"s_lshr_b32 s74, %1, 3\nQBH 74\ns_add_u32 s74, s74, %0\ns_mul_i32 s74, s74, ((L_attn_end-L_att-1024)/127+63)&-64\ns_min_u32 s74, s74, L_attn_end-L_att-1024\ns_add_u32 s74, s74, L_att-L_pc17\nQZ 72,74\n"
"QAX 73\nQCL 138,185\nQFG 136,72,138\nQA 0,32,128\nQA 4,32,132\nQA 8,32,136\nQA 12,32,140\nQA 16,36,128\nQA 20,36,132\nQA 24,36,136\nQA 28,36,140\nQF 192,0,1\nQF 193,2,3\nQA 32,40,128\nQF 194,16,17\n"
"QF 195,18,19\nQA 36,40,132\nQF 208,4,5\nQF 209,6,7\nQA 40,40,136\nQF 210,20,21\nQF 211,22,23\nQA 44,40,140\nQF 224,8,9\nQF 225,10,11\nQA 48,44,128\nQF 226,24,25\nQF 227,26,27\nQA 52,44,132\n"
"QF 240,12,13\nQF 241,14,15\nQA 56,44,136\nQF 242,28,29\nQF 243,30,31\nQA 60,44,140\nQF 196,32,33\nQF 197,34,35\nQA 64,48,128\nQF 198,48,49\nQF 199,50,51\nQA 68,48,132\nQF 212,36,37\nQF 213,38,39\n"
"QA 72,48,136\nQF 214,52,53\nQF 215,54,55\nQA 76,48,140\nQF 228,40,41\nQF 229,42,43\nQA 80,52,128\nQF 230,56,57\nQF 231,58,59\nQA 84,52,132\nQF 244,44,45\nQF 245,46,47\nQA 88,52,136\nQF 246,60,61\n"
"QF 247,62,63\nQA 92,52,140\nQF 200,64,65\nQF 201,66,67\nQA 96,56,128\nQF 202,80,81\nQF 203,82,83\nQA 100,56,132\nQF 216,68,69\nQF 217,70,71\nQA 104,56,136\nQF 218,84,85\nQF 219,86,87\nQA 108,56,140\n"
"QF 232,72,73\nQF 233,74,75\nQA 112,60,128\nQF 234,88,89\nQF 235,90,91\nQA 116,60,132\nQF 248,76,77\nQF 249,78,79\nQA 120,60,136\nQF 250,92,93\nQF 251,94,95\nQA 124,60,140\nQF 204,96,97\nQF 205,98,99\n"
"QF 206,112,113\nQF 207,114,115\nQF 220,100,101\nQF 221,102,103\nQF 222,116,117\nQF 223,118,119\nQF 236,104,105\nQF 237,106,107\nQF 238,120,121\nQF 239,122,123\nQF 252,108,109\nQF 253,110,111\n"
"QF 254,124,125\nQF 255,126,127\ns_mov_b64 s[76:77], %2\ns_mov_b32 s78, 72089600\nQBA 79\nQGI 88,80\nQBM 182,88,185\ns_lshl_b32 s89, s82, 20\nQAH 17\nQU 0,160\nQY 1,160\nQU 2,161\nQY 3,161\nQU 4,162\n"
"QY 5,162\nQU 6,163\nQY 7,163\nQU 8,164\nQY 9,164\nQU 10,165\nQY 11,165\nQU 12,166\nQY 13,166\nQU 14,167\nQY 15,167\nQU 16,168\nQY 17,168\nQU 18,169\nQY 19,169\nQU 20,170\nQY 21,170\nQU 22,171\n"
"QY 23,171\nQU 24,172\nQY 25,172\nQU 26,173\nQY 27,173\nQU 28,174\nQY 29,174\nQU 30,175\nQY 31,175\nQU 32,176\nQY 33,176\nQU 34,177\nQY 35,177\nQU 36,178\nQY 37,178\nQU 38,179\nQY 39,179\nQAH 9\n"
"s_barrier\nQB 228,145,36864\nQB 232,145,37888\nQB 236,145,38912\nQB 240,145,39936\nQB 244,145,40960\nQB 248,145,41984\nQG 4\nQA 0,228,144\nQB 252,145,43008\nQAA 50\nQA 20,228,148\nQJ 0,40,150,50,0\n"
"QA 4,232,144\nQB 228,145,44032\nQG 4\nQAA 51\nQA 24,232,148\nQAG 176\nQAG 177\nQA 8,236,144\nQB 232,145,45056\nQAG 178\nQA 28,236,148\nQJ 4,40,151,51,0\nQA 12,240,144\nQB 236,145,46080\nQG 4\n"
"QA 32,240,148\nQAG 179\nQAG 96\nQA 16,244,144\nQB 240,145,47104\nQAG 97\nQA 36,244,148\nQJ 8,40,150,50,1024\nQA 0,248,152\nQB 244,145,48128\nQG 4\nQA 20,248,156\nQAG 98\nQAG 99\nQA 4,252,152\n"
"QB 248,145,49152\nQAG 100\nQA 24,252,156\nQJ 12,40,151,51,1024\nQA 8,228,152\nQB 252,145,50176\nQG 4\nQA 28,228,156\nQAG 101\nQAG 102\nQA 12,232,152\nQB 228,145,51200\nQAG 103\nQA 32,232,156\n"
"QJ 16,40,152,50,2048\nQA 16,236,152\nQB 232,145,52224\nQG 4\nQA 36,236,156\nQAG 104\nQAG 105\nQA 0,240,160\nQB 236,145,53248\nQAG 106\nQA 20,240,164\nQJ 20,40,153,51,2048\nQA 4,244,160\n"
"QB 240,145,54272\nQG 4\nQA 24,244,164\nQAG 107\nQAG 108\nQA 8,248,160\nQB 244,145,55296\nQAG 109\nQA 28,248,164\nQJ 24,40,152,50,3072\nQA 12,252,160\nQB 248,145,56320\nQG 4\nQA 32,252,164\nQAG 110\n"
"QAG 111\nQA 16,228,160\nQAH 8\ns_barrier\nQB 252,145,57344\nQAG 112\nQA 36,228,164\nQJ 28,40,153,51,3072\nQA 0,232,168\nQB 228,145,58368\nQG 4\nQAA 50\nQA 20,232,172\nQAG 113\nQAG 114\nQA 4,236,168\n"
"QB 232,145,59392\nQAG 115\nQA 24,236,172\nQJ 32,40,152,50,0\nQA 8,240,168\nQB 236,145,60416\nQG 4\nQAA 51\nQA 28,240,172\nQAG 116\nQAG 117\nQA 12,244,168\nQB 240,145,61440\nQAG 118\nQA 32,244,172\n"
"QJ 36,40,153,51,0\nQA 16,248,168\nQB 244,145,62464\nQG 4\nQA 36,248,172\nQAG 119\nQAG 120\nQA 0,252,176\nQB 248,145,63488\nQAG 121\nQA 20,252,180\nQJ 40,40,152,50,1024\nQA 4,228,176\n"
"QB 252,145,64512\nQG 4\nQA 24,228,180\nQAG 122\nQAG 123\nQA 8,232,176\nQB 228,146,0\nQAG 124\nQA 28,232,180\nQJ 44,40,153,51,1024\nQA 12,236,176\nQB 232,146,1024\nQG 4\nQA 32,236,180\nQAG 125\n"
"QAG 126\nQA 16,240,176\nQB 236,146,2048\nQAG 127\nQA 36,240,180\nQJ 48,40,152,50,2048\nQA 0,244,184\nQB 240,146,3072\nQG 4\nQA 20,244,188\nQN 180,185\nv_add_u32 v181, 0xc000, v185\nQA 4,248,184\n"
"QB 244,146,4096\nv_mov_b32 v184, 0xff800000\nQA 24,248,188\nQJ 52,40,153,51,2048\nQA 8,252,184\nQB 248,146,5120\nQG 4\nQA 28,252,188\nQCN 160\nQCN 161\nQA 12,228,184\nQB 252,146,6144\nQCN 162\n"
"QA 32,228,188\nQJ 56,40,154,50,3072\nQA 16,232,184\nQB 228,146,7168\nQG 4\nQA 36,232,188\nQCN 163\ns_mov_b64 s[44:45], %2\nQA 0,236,192\nQB 232,146,8192\ns_add_u32 s44, s44, 58195968\nQAX 45\n"
"QA 20,236,196\nQJ 60,40,155,51,3072\nQA 4,240,192\nQB 236,146,9216\nQG 4\nQAA 50\nQA 24,240,196\ns_mov_b32 s46, 21561344\nQBA 47\nQA 8,244,192\nQB 240,146,10240\nQA 28,244,196\nQJ 64,40,154,50,0\n"
"QA 12,248,192\nQB 244,146,11264\nQG 4\nQAA 51\nQA 32,248,196\nQA 16,252,192\nQAH 9\ns_barrier\nQB 248,146,12288\nQA 36,252,196\nQJ 68,40,155,51,0\nQA 0,228,200\nQB 252,146,13312\nQG 4\nQA 20,228,204\n"
"QA 4,232,200\nQB 228,146,14336\nQA 24,232,204\nQJ 72,40,154,50,1024\nQA 8,236,200\nQB 232,146,15360\nQG 4\nQA 28,236,204\nQA 12,240,200\nQB 236,146,16384\nQA 32,240,204\nQJ 76,40,155,51,1024\n"
"QA 16,244,200\nQB 240,146,17408\nQG 4\nQA 36,244,204\nQA 0,248,0\nQB 244,146,18432\nQA 20,248,4\nQJ 80,40,154,50,2048\nQA 4,252,0\nQB 248,146,19456\nQG 4\nQA 24,252,4\nQA 8,228,0\nQB 252,146,20480\n"
"QA 28,228,4\nQJ 84,40,155,51,2048\nQA 12,232,0\nQB 228,146,21504\nQG 4\nQA 32,232,4\nQA 16,236,0\nQB 232,146,22528\nQA 36,236,4\nQJ 88,40,154,50,3072\nQA 0,240,8\nQB 236,146,23552\nQG 4\n"
"QA 20,240,12\nQA 4,244,8\nQB 240,146,24576\nQA 24,244,12\nQJ 92,40,155,51,3072\nQA 8,248,8\nQB 244,146,25600\nQG 4\nQBV 88\nQA 28,248,12\nQA 12,252,8\nQB 248,146,26624\nQA 32,252,12\nQL 76,182,89,0\n"
"QA 16,228,8\nQB 252,146,27648\nQG 4\nQA 36,228,12\nQA 0,232,16\nQB 228,146,28672\nQA 20,232,20\nQL 76,182,89,1024\nQA 4,236,16\nQB 232,146,29696\nQG 4\nQA 24,236,20\nQA 8,240,16\nQB 236,146,30720\n"
"QA 28,240,20\nQL 76,182,89,2048\nQA 12,244,16\nQB 240,146,31744\nQG 4\nQA 32,244,20\nQA 16,248,16\nQAH 11\ns_barrier\nQB 244,145,16384\nQA 36,248,20\nQL 76,182,89,3072\nQA 0,252,24\nQB 248,145,17408\n"
"QDF 88\nQA 20,252,28\nQG 4\ns_add_u32 s89, s89, 0x4000\nQA 4,228,24\nQB 252,145,18432\nQA 24,228,28\nQL 76,182,89,0\nQA 8,232,24\nQB 228,145,19456\nQA 28,232,28\nQG 4\nQA 12,236,24\nQB 232,145,20480\n"
"QA 32,236,28\nQL 76,182,89,1024\nQA 16,240,24\nQB 236,145,21504\nQA 36,240,28\nQG 4\nQA 0,244,32\nQB 240,145,22528\nQA 20,244,36\nQL 76,182,89,2048\nQA 4,248,32\nQB 244,145,23552\nQA 24,248,36\nQG 4\n"
"QA 8,252,32\nQB 248,145,24576\nQA 28,252,36\nQL 76,182,89,3072\nQA 12,228,32\nQB 252,145,25600\nQDW 88\nQA 32,228,36\nQG 4\ns_add_u32 s89, s89, 0x4000\nQA 16,232,32\nQB 228,145,26624\nQA 36,232,36\n"
"QL 76,182,89,0\nQA 0,236,40\nQB 232,145,27648\nQA 20,236,44\nQG 4\nQA 4,240,40\nQB 236,145,28672\nQA 24,240,44\nQL 76,182,89,1024\nQA 8,244,40\nQB 240,145,29696\nQA 28,244,44\nQG 4\nQA 12,248,40\n"
"QB 244,145,30720\nQA 32,248,44\nQL 76,182,89,2048\nQA 16,252,40\nQB 248,145,31744\nQA 36,252,44\nQG 4\nQA 0,228,48\nQB 252,145,32768\nQA 20,228,52\nQL 76,182,89,3072\nQA 4,232,48\nQB 228,145,33792\n"
"QA 24,232,52\nQG 4\nQA 8,236,48\nQB 232,145,34816\nQA 28,236,52\nQA 12,240,48\nQB 236,145,35840\nQA 32,240,52\nQG 4\nQA 16,244,48\nQAH 12\ns_barrier\nQB 240,144,61440\nQA 36,244,52\nQA 0,248,56\n"
"QB 244,144,62464\nQA 20,248,60\nQG 4\nQA 4,252,56\nQB 248,144,63488\nQA 24,252,60\nQA 8,228,56\nQB 252,144,64512\nQA 28,228,60\nQG 4\nQA 12,232,56\nQB 228,145,0\nQA 32,232,60\nQA 16,236,56\n"
"QB 232,145,1024\nQA 36,236,60\nQG 4\nQA 0,240,64\nQB 236,145,2048\nQA 20,240,68\nQA 4,244,64\nQB 240,145,3072\nQA 24,244,68\nQG 4\nQA 8,248,64\nQB 244,145,4096\nQA 28,248,68\nQA 12,252,64\n"
"QB 248,145,5120\nQA 32,252,68\nQG 4\nQA 16,228,64\nQB 252,145,6144\nQA 36,228,68\nQA 0,232,72\nQB 228,145,7168\nQA 20,232,76\nQG 4\nQA 4,236,72\nQB 232,145,8192\nQA 24,236,76\nQA 8,240,72\n"
"QB 236,145,9216\nQA 28,240,76\nQG 4\nQA 12,244,72\nQB 240,145,10240\nQA 32,244,76\nQA 16,248,72\nQB 244,145,11264\nQA 36,248,76\nQG 4\nQA 0,252,80\nQB 248,145,12288\nQA 20,252,84\nQA 4,228,80\n"
"QB 252,145,13312\nQA 24,228,84\nQG 4\nQA 8,232,80\nQB 228,145,14336\nQA 28,232,84\nQA 12,236,80\nQB 232,145,15360\nQA 32,236,84\nQG 4\nQA 16,240,80\nQA 36,240,84\nQA 0,244,88\nQA 20,244,92\nQG 3\n"
"QA 4,248,88\nQA 24,248,92\nQG 2\nQA 8,252,88\nQA 28,252,92\nQG 1\nQA 12,228,88\nQA 32,228,92\nQG 0\nQA 16,232,88\nQA 36,232,92\nQF 40,0,1\nQF 41,2,3\nQF 42,4,5\nQF 43,6,7\nQAB 40,44,185,52,0\n"
"QF 44,8,9\nQF 45,10,11\nQF 46,12,13\nQF 47,14,15\nQAB 44,44,185,52,1024\nQF 48,16,17\nQF 49,18,19\nQFM 48,44,185,53\nQF 50,20,21\nQF 51,22,23\nQF 52,24,25\nQF 53,26,27\nQAB 50,44,147,52,0\n"
"QF 54,28,29\nQF 55,30,31\nQF 56,32,33\nQF 57,34,35\nQAB 54,44,147,52,1024\nQF 58,36,37\nQF 59,38,39\nQFM 58,44,147,53\nQU 0,40\nQY 1,40\nQU 2,41\nQY 3,41\nQU 4,42\nQY 5,42\nQU 6,43\nQY 7,43\nQU 8,44\n"
"QY 9,44\nQU 10,45\nQY 11,45\nQU 12,46\nQY 13,46\nQU 14,47\nQY 15,47\nQU 16,48\nQY 17,48\nQU 18,49\nQY 19,49\nQU 20,50\nQY 21,50\nQU 22,51\nQY 23,51\nQU 24,52\nQY 25,52\nQU 26,53\nQY 27,53\nQU 28,54\n"
"QY 29,54\nQU 30,55\nQY 31,55\nQU 32,56\nQY 33,56\nQU 34,57\nQY 35,57\nQU 36,58\nQY 37,58\nQU 38,59\nQY 39,59\nQN 64,0\nQN 70,20\nQN 65,64\nQN 71,70\nQBE \nQBS 64,65\nQBS 70,71\nQAZ 64,65\nQAZ 70,71\n"
"QN 65,64\nQN 71,70\nQBE \nQBN 64,65\nQBN 70,71\nQAZ 64,65\nQAZ 70,71\nQCF 0,64\nQCF 20,70\nQCF 2,64\nQCF 22,70\nQCF 4,64\nQCF 24,70\nQCF 6,64\nQCF 26,70\nQCF 8,64\nQCF 28,70\nQCF 10,64\nQCF 30,70\n"
"QCF 12,64\nQCF 32,70\nQCF 14,64\nQCF 34,70\nQCF 16,64\nQCF 36,70\nQCF 18,64\nQCF 38,70\nQGP 60,0,2\nQGP 66,20,22\nQAP 62,0\nQAP 68,20\nQBF 62,2\nQBF 68,22\nQAI 60,4\nQAI 66,24\nQBF 62,4\nQBF 68,24\n"
"QAI 60,6\nQAI 66,26\nQBF 62,6\nQBF 68,26\nQAI 60,8\nQAI 66,28\nQBF 62,8\nQBF 68,28\nQAI 60,10\nQAI 66,30\nQBF 62,10\nQBF 68,30\nQAI 60,12\nQAI 66,32\nQBF 62,12\nQBF 68,32\nQAI 60,14\nQAI 66,34\n"
"QBF 62,14\nQBF 68,34\nQAI 60,16\nQAI 66,36\nQBF 62,16\nQBF 68,36\nQAI 60,18\nQAI 66,38\nQBF 62,18\nQBF 68,38\nQAJ 60,61\nQAJ 66,67\nQEW 61,62,63\nQEW 67,68,69\nQN 62,60\nQN 68,66\nQN 63,61\nQN 69,67\n"
"QBS 60,62\nQBS 66,68\nQBS 61,63\nQBS 67,69\nQAJ 60,62\nQAJ 66,68\nQAJ 61,63\nQAJ 67,69\nQN 62,60\nQN 68,66\nQN 63,61\nQN 69,67\nQBN 60,62\nQBN 66,68\nQBN 61,63\nQBN 67,69\nQAJ 60,62\nQAJ 66,68\n"
"QAJ 61,63\nQAJ 67,69\nv_mul_f32 v65, 0x3c4ccccd, v60\nv_mul_f32 v71, 0x3c4ccccd, v66\nv_fma_f32 v61, -v65, v60, v61\nv_fma_f32 v67, -v71, v66, v67\nQEW 60,65,64\nQEW 66,71,70\nQCV 72,185\nQEV 72\n"
"QBU 54\nQGA 55\nQZ 54,55\ns_add_u32 s54, s54, 21430272\nbuffer_store_dwordx2 v[60:61], v72, s[44:47], s54 offen offset:0 sc1\nbuffer_store_dwordx2 v[66:67], v72, s[44:47], s54 offen offset:128 sc1\n"
"QAH 8\nL_att:\nQDO \ns_barrier\nQB 192,180,0\nQB 196,180,1024\nQB 200,180,4096\nQB 204,180,5120\nQB 208,180,2048\nQB 212,180,3072\nQB 216,180,6144\nQB 220,180,7168\ns_and_b32 s83, %1, 1\nQCK 83\n"
"s_bfe_u32 s84, %1, 196611\nQZ 83,84\ns_mul_i32 s83, s83, 143360\ns_add_u32 s48, s83, 69795840\nv_lshrrev_b32 v164, 8, v185\nv_lshlrev_b32 v164, 2, v164\nv_bfe_u32 v165, v185, 4, 4\nQEU 166,164\n"
"v_cmp_gt_u32 s[36:37], v166, v165\nv_add_u32 v166, 1, v164\nv_cmp_gt_u32 s[38:39], v166, v165\nv_add_u32 v166, 2, v164\nv_cmp_gt_u32 s[40:41], v166, v165\nv_add_u32 v166, 3, v164\n"
"v_cmp_gt_u32 s[42:43], v166, v165\ns_cmp_lg_u32 s66, 0\ns_cbranch_scc1 L_f1_m0\nQAT 36,96,176,184\nQAT 38,97,176,184\nQAT 40,98,176,184\nQAT 42,99,176,184\nQN 100,177\nQN 101,177\nQN 102,177\n"
"QN 103,177\nQN 104,178\nQN 105,178\nQN 106,178\nQN 107,178\nQN 108,179\nQN 109,179\nQN 110,179\nQN 111,179\nQN 112,184\nQN 113,184\nQN 114,184\nQN 115,184\nQAT 36,116,177,184\nQAT 38,117,177,184\n"
"QAT 40,118,177,184\nQAT 42,119,177,184\nQN 120,178\nQN 121,178\nQN 122,178\nQN 123,178\nQN 124,179\nQN 125,179\nQN 126,179\nQN 127,179\nL_f1_m0:\nQG 0\ns_cmp_ge_u32 s80, 2\ns_cbranch_scc1 L_f1_b\n"
"QAR 0,192,192,96\nQDJ 56\nQGY 83,81\nQFY 80\ns_cselect_b32 s84, s83, s81\nQC 0,196,196\nQCK 84\nQBX 85,80\nQBH 85\nQC 0,200,200\nQCH 192,186,0\nQZ 84,85\nQC 0,204,204\nQDJ 57\n"
"s_lshl_b32 s90, s84, 17\nQGI 86,82\nQAR 4,192,208,100\nQCH 196,186,1024\nQZ 90,86\nQC 4,196,212\nQDK 58\nQCJ 85,80\ns_xor_b32 s85, s85, s80\nQC 4,200,216\nQCH 200,186,2048\ns_and_b32 s85, s85, 1\n"
"QC 4,204,220\nQDK 59\nQBH 85\nQCK 83\nQAR 8,192,224,104\nQCH 204,186,3072\nQZ 83,85\nQC 8,196,228\nQBW 60\ns_lshl_b32 s91, s83, 17\nQZ 91,86\nQC 8,200,232\nQCH 208,186,4096\nQCJ 83,80\nQC 8,204,236\n"
"QBW 61\nQGA 83\nQBX 84,80\nQAR 12,192,240,108\nQCH 212,186,5120\ns_lshl_b32 s84, s84, 11\nQC 12,196,244\nQBW 62\nQZ 83,84\ns_add_u32 s97, s83, 143360\nQC 12,200,248\nQCH 216,186,6144\nQCL 190,185\n"
"QC 12,204,252\nQBW 63\nv_add_u32 v190, s97, v190\ns_mov_b32 s72, 4\nQAR 16,208,192,112\nQCH 220,186,7168\ns_mov_b32 s86, 0x40000000\nQC 16,212,196\nQHB 49,48\ns_mov_b32 s87, 0x40000000\n"
"s_add_u32 s72, s72, 4\nQC 16,216,200\nQHC 98,48\ns_min_u32 s72, s72, 60\ns_add_u32 s82, s72, 1\nQC 16,220,204\nQHD 64\nQBI 56,48\nQBI 58,49\nQBI 48,98\nQAR 20,208,208,116\nQHE 64\nQBI 57,48\n"
"QBI 59,71\nQHF 63\nQC 20,212,212\ns_add_u32 s83, s72, 2\ns_add_u32 s84, s72, 3\nQCO 88,187,72\nQC 20,216,216\nQB 192,180,8192\nQCO 89,187,82\nQC 20,220,220\nQCO 81,187,83\nQCO 85,187,84\n"
"QAR 24,208,224,120\nQB 196,180,9216\ns_bfe_u32 s99, %1, 262144\nQBU 99\nQHG 99\nQZ 99,96\nQC 24,212,228\nQC 24,216,232\nQB 200,180,12288\nQC 24,220,236\nQAR 28,208,240,124\nQB 204,180,13312\n"
"QC 28,212,244\nQC 28,216,248\nQCH 224,186,8192\nQC 28,220,252\nQCH 228,186,9216\nQCH 232,186,10240\nQCH 236,186,11264\nQCH 240,186,12288\nQCH 244,186,13312\nQCH 248,186,14336\nQCH 252,186,15360\n"
"QB 208,180,10240\nQB 212,180,11264\nQB 216,180,14336\nQB 220,180,15360\ns_branch L_f1_c\nL_f1_b:\nQG 0\nQAR 0,192,192,96\nQDJ 56\nQGY 83,81\nQFY 80\ns_cselect_b32 s84, s83, s81\nQC 0,196,196\nQDJ 57\n"
"QCK 84\nQBX 85,80\nQC 0,200,200\nQDK 58\nQBH 85\nQZ 84,85\nQC 0,204,204\nQDK 59\ns_lshl_b32 s90, s84, 17\nQGI 86,82\nQAR 4,192,208,100\nQBW 60\nQZ 90,86\nQCJ 85,80\nQC 4,196,212\nQBW 61\n"
"s_xor_b32 s85, s85, s80\ns_and_b32 s85, s85, 1\nQC 4,200,216\nQBW 62\nQBH 85\nQCK 83\nQC 4,204,220\nQBW 63\nQZ 83,85\ns_lshl_b32 s91, s83, 17\nQAR 8,192,224,104\nQHB 49,48\nQZ 91,86\nQCJ 83,80\n"
"QC 8,196,228\nQHC 98,48\nQGA 83\nQBX 84,80\nQC 8,200,232\nQHD 64\nQBI 56,48\nQBI 58,49\nQBI 48,98\nQC 8,204,236\nQHE 64\nQBI 57,48\nQBI 59,71\nQHF 63\nQAR 12,192,240,108\ns_lshl_b32 s84, s84, 11\n"
"QZ 83,84\ns_add_u32 s97, s83, 143360\nQC 12,196,244\nQCL 190,185\nv_add_u32 v190, s97, v190\nQC 12,200,248\ns_mov_b32 s72, 4\ns_mov_b32 s86, 0x40000000\ns_mov_b32 s87, 0x40000000\nQC 12,204,252\n"
"s_add_u32 s72, s72, 4\ns_min_u32 s72, s72, 60\ns_add_u32 s82, s72, 1\nQAR 16,208,192,112\nQB 192,180,8192\ns_add_u32 s83, s72, 2\nQC 16,212,196\nQB 196,180,9216\ns_add_u32 s84, s72, 3\nQC 16,216,200\n"
"QB 200,180,12288\nQCO 88,187,72\nQC 16,220,204\nQB 204,180,13312\nQCO 89,187,82\nQAR 20,208,208,116\nQCO 81,187,83\nQCO 85,187,84\nQC 20,212,212\ns_bfe_u32 s99, %1, 262144\nQBU 99\nQHG 99\nQZ 99,96\n"
"QC 20,216,216\nQC 20,220,220\nQAR 24,208,224,120\nQC 24,212,228\nQC 24,216,232\nQC 24,220,236\nQAR 28,208,240,124\nQC 28,212,244\nQC 28,216,248\nQC 28,220,252\nQB 208,180,10240\nQB 212,180,11264\n"
"QB 216,180,14336\nQB 220,180,15360\nL_f1_c:\nQAM 8,0\ns_barrier\nQEE 180\nQEE 181\nQCP 0,176,160\nQCI 60\nQBY 164,0,1,2\nQBY 165,4,5,6\nQBY 166,8,9,10\nQBY 167,12,13,14\nQBK 164,3,16\nQL 76,182,56,0\n"
"QCP 16,176,160\nQBK 165,7,20\nQBK 166,11,24\nQBK 167,15,28\nQBK 164,17,18\nQBK 165,21,22\nQL 76,182,56,1024\nQCP 32,176,160\nQBK 166,25,26\nQBK 167,29,30\nQAZ 164,19\nQAZ 165,23\nQAZ 166,27\n"
"QL 76,182,56,2048\nQCP 48,176,160\nQAZ 167,31\nQN 172,164\nQN 173,165\nQN 174,166\nQN 175,167\nQL 76,182,56,3072\nQCP 64,176,160\nQCI 62\nQBS 164,172\nQBS 165,173\nQBS 166,174\nQBS 167,175\n"
"QAZ 164,172\nQL 76,182,58,0\nQCP 80,176,160\nQAZ 165,173\nQAZ 166,174\nQAZ 167,175\nQN 172,164\nQN 173,165\nQL 76,182,58,1024\nQCP 96,176,160\nQN 174,166\nQN 175,167\nQBN 164,172\nQBN 165,173\n"
"QBN 166,174\nQL 76,182,58,2048\nQCP 112,176,160\nQBN 167,175\nQAZ 164,172\nQAZ 165,173\nQAZ 166,174\nQAZ 167,175\nQL 76,182,58,3072\nQEF 176,164\nQEF 177,165\nQEF 178,166\nQEF 179,167\n"
"s_cmp_eq_u32 s66, s64\ns_cbranch_scc1 L_f1_m1\nQN 96,176\nQN 97,176\nQN 98,176\nQN 99,176\nQN 100,177\nQN 101,177\nQN 102,177\nQN 103,177\nQN 104,178\nQN 105,178\nQN 106,178\nQN 107,178\nQN 108,179\n"
"QN 109,179\nQN 110,179\nQN 111,179\nQN 112,176\nQN 113,176\nQN 114,176\nQN 115,176\nQN 116,177\nQN 117,177\nQN 118,177\nQN 119,177\nQN 120,178\nQN 121,178\nQN 122,178\nQN 123,178\nQN 124,179\n"
"QN 125,179\nQN 126,179\nQN 127,179\ns_branch L_f1_m1d\nL_f1_m1:\nQN 96,184\nQN 97,184\nQN 98,184\nQN 99,184\nQN 100,184\nQN 101,184\nQN 102,184\nQN 103,184\nQAT 36,104,178,184\nQAT 38,105,178,184\n"
"QAT 40,106,178,184\nQAT 42,107,178,184\nQN 108,179\nQN 109,179\nQN 110,179\nQN 111,179\nQN 112,184\nQN 113,184\nQN 114,184\nQN 115,184\nQN 116,184\nQN 117,184\nQN 118,184\nQN 119,184\nQN 120,184\n"
"QN 121,184\nQN 122,184\nQN 123,184\nQAT 36,124,179,184\nQAT 38,125,179,184\nQAT 40,126,179,184\nQAT 42,127,179,184\nL_f1_m1d:\nQAR 32,192,192,96\nQCI 61\nQAJ 0,176\nQAJ 1,176\nQC 32,196,196\n"
"QB 176,186,12288\nQAJ 2,176\nQC 32,200,200\nQL 76,182,57,0\nQC 32,204,204\nQB 180,186,13312\nQAJ 3,176\nQAR 36,192,208,100\nQL 76,182,57,1024\nQC 36,196,212\nQB 184,186,14336\nQAJ 16,176\n"
"QC 36,200,216\nQL 76,182,57,2048\nQC 36,204,220\nQB 188,186,15360\nQAJ 17,176\nQAR 40,192,224,104\nQL 76,182,57,3072\nQC 40,196,228\nQB 224,181,0\nQAJ 18,176\nQC 40,200,232\nQFR 92,168,71\n"
"QC 40,204,236\nQB 228,181,1024\nQAJ 19,176\nQAR 44,192,240,108\nQFR 92,168,71\nQC 44,196,244\nQB 232,181,2048\nQAJ 4,177\nQC 44,200,248\nQFR 92,168,71\nQC 44,204,252\nQB 236,181,3072\nQAJ 5,177\n"
"QAR 48,208,192,112\nQFR 92,168,71\nQC 48,212,196\nQB 240,181,4096\nQAJ 6,177\nQC 48,216,200\nQB 244,181,5120\nQAJ 7,177\nQC 48,220,204\nQB 248,181,6144\nQAJ 20,177\nQAR 52,208,208,116\n"
"QB 252,181,7168\nQAJ 21,177\nQC 52,212,212\nQB 192,180,0\nQAJ 22,177\nQC 52,216,216\nQB 196,180,1024\nQAJ 23,177\nQC 52,220,220\nQB 200,180,4096\nQAJ 8,178\nQAR 56,208,224,120\nQB 204,180,5120\n"
"QAJ 9,178\nQC 56,212,228\nQP 164,0\nQC 56,216,232\nQP 165,1\nQC 56,220,236\nQP 166,2\nQAR 60,208,240,124\nQP 167,3\nQC 60,212,244\nQP 168,16\nQC 60,216,248\nQP 169,17\nQC 60,220,252\nQP 170,18\n"
"QB 208,180,2048\nQB 212,180,3072\nQB 216,180,6144\nQB 220,180,7168\nQP 171,19\nQF 64,164,165\nQF 65,166,167\nQF 66,168,169\nQF 67,170,171\nQP 172,4\nQP 173,5\nQFS 128,160,64\nQP 174,6\nQP 175,7\n"
"QP 164,20\nQP 165,21\nQP 166,22\nQP 167,23\nQF 68,172,173\nQF 69,174,175\nQF 70,164,165\nQF 71,166,167\nQAJ 10,178\nQAJ 11,178\nQFS 132,160,68\nQAJ 24,178\nQAJ 25,178\nQAJ 26,178\nQAJ 27,178\n"
"QP 168,8\nQP 169,9\nQP 170,10\nQP 171,11\nQP 172,24\nQP 173,25\nQP 174,26\nQP 175,27\nQF 72,168,169\nQF 73,170,171\nQF 74,172,173\nQF 75,174,175\nQAJ 12,179\nQAJ 13,179\nQFS 136,160,72\nQAJ 14,179\n"
"QAJ 15,179\nQAJ 28,179\nQAJ 29,179\nQAJ 30,179\nQAJ 31,179\nQP 164,12\nQP 165,13\nQP 166,14\nQP 167,15\nQP 168,28\nQP 169,29\nQP 170,30\nQP 171,31\nQF 76,164,165\nQF 77,166,167\nQF 78,168,169\n"
"QF 79,170,171\nQBD \nQFS 140,160,76\ns_mov_b64 s[44:45], %2\ns_add_u32 s44, s44, 33554432\nQAX 45\ns_mov_b32 s46, 16777216\nQBA 47\ns_mov_b64 s[52:53], %2\ns_add_u32 s52, s52, 68157440\nQAX 53\n"
"s_mov_b32 s54, 11468800\nQBA 55\nQCV 186,185\nQCW 186\ns_mov_b32 s96, 4161009664\nQCG 100\nL_pcd:\nQGI 83,80\ns_add_u32 s83, s83, L_drain-L_pcd\nQZ 100,83\nQAX 101\ns_and_b32 s100, s100, 0xffffff80\n"
"s_movk_i32 s83, 128\nv_writelane_b32 v187, s100, 52\nv_writelane_b32 v187, s101, 53\nv_writelane_b32 v187, s83, 54\nv_writelane_b32 v187, s100, 56\nv_writelane_b32 v187, s101, 57\n"
"v_writelane_b32 v187, s83, 58\nv_writelane_b32 v187, s100, 60\nv_writelane_b32 v187, s101, 61\nv_writelane_b32 v187, s83, 62\nQCE 64\nQG 0\nL_disp0:\ns_cmp_eq_u32 s64, s65\ns_cbranch_scc1 L_switch\n"
"s_cmp_lg_u32 s64, s66\ns_cbranch_scc1 L_d0b\nQAT 36,96,176,184\nQAT 38,97,176,184\nQAT 40,98,176,184\nQAT 42,99,176,184\nQN 100,177\nQN 101,177\nQN 102,177\nQN 103,177\nQN 104,178\nQN 105,178\n"
"QN 106,178\nQN 107,178\nQN 108,179\nQN 109,179\nQN 110,179\nQN 111,179\nQN 112,184\nQN 113,184\nQN 114,184\nQN 115,184\nQAT 36,116,177,184\nQAT 38,117,177,184\nQAT 40,118,177,184\nQAT 42,119,177,184\n"
"QN 120,178\nQN 121,178\nQN 122,178\nQN 123,178\nQN 124,179\nQN 125,179\nQN 126,179\nQN 127,179\nL_d0b:\ns_cmp_ge_u32 s64, s67\ns_cbranch_scc1 L_loop1\n.p2align 4\nL_loop0:\nQAR 0,192,192,96\n"
"s_lshr_b32 s82, s81, 16\nQP 164,32\nQC 0,196,196\ns_and_b32 s81, s81, 0xffff\nQP 165,33\nQC 0,200,200\nQB 128,181,8192\nQF 80,164,165\nQC 0,204,204\nQDJ 56\nQP 166,34\nQAR 4,192,208,100\nQDJ 57\n"
"QP 167,35\nQC 4,196,212\nQB 132,181,9216\nQF 81,166,167\nQC 4,200,216\nQDK 58\nQP 168,48\nQC 4,204,220\nQDK 59\nQP 169,49\nQAR 8,192,224,104\nQB 136,181,10240\nQF 82,168,169\nQC 8,196,228\nQBW 60\n"
"QP 170,50\nQC 8,200,232\nQBW 61\nQP 171,51\nQC 8,204,236\nQB 140,181,11264\nQF 83,170,171\nQAR 12,192,240,108\nQBW 62\nQP 172,36\nQC 12,196,244\nQBW 63\nQP 173,37\nQBO 144,160,80,128\n"
"QB 144,181,12288\nQF 84,172,173\nQC 12,200,248\nQG 5\nQP 174,38\nQC 12,204,252\nQB 192,180,8192\nQDS 188,185\nQAR 16,208,192,112\nQHB 49,48\nQP 175,39\nQC 16,212,196\nQB 148,181,13312\nQF 85,174,175\n"
"QC 16,216,200\nQB 196,180,9216\nQGU 189,188\nQC 16,220,204\nQHC 98,48\nQP 164,52\nQAR 20,208,208,116\nQB 152,181,14336\nQED 188,189,82\nQC 20,212,212\nQB 200,180,12288\nQGU 189,185\nQC 20,216,216\n"
"QHD 64\nQP 165,53\nQC 20,220,220\nQB 156,181,15360\nQF 86,164,165\nQAR 24,208,224,120\nQB 204,180,13312\nv_mad_u32_u24 v188, v188, s81, v189\nQC 24,212,228\nQBI 56,48\nQP 166,54\nQC 24,216,232\n"
"QBI 58,49\nQP 167,55\nQC 24,220,236\nQBI 48,98\nQF 87,166,167\nQI 189,164\nQAR 28,208,240,124\nQHE 64\nQP 168,40\nQBO 148,160,84,132\nQBI 57,48\nQP 169,41\nQC 28,212,244\nQBI 59,71\nQP 170,42\n"
"QC 28,216,248\nQHF 63\nQP 171,43\nQC 28,220,252\ns_bitcmp1_b32 s68, s64\nQP 172,56\nQR 0,224,64\nQB 208,180,10240\nQF 88,168,169\nQR 4,224,68\nQB 212,180,11264\nQF 89,170,171\nQR 8,224,72\n"
"QB 216,180,14336\nv_cmp_ne_u32 s[50:51], s96, v189\nQR 12,224,76\nQB 220,180,15360\nQU 189,185\nQR 16,228,64\ns_cselect_b32 s70, -1.0, s71\nQP 173,57\nQR 20,228,68\nQP 174,58\nQR 24,228,72\n"
"QF 90,172,173\nQR 28,228,76\nQP 175,59\nQR 32,232,64\nQP 164,44\nQR 36,232,68\nQAM 4,0\nQP 165,45\nQR 40,232,72\ns_barrier\nQR 44,232,76\nQCI 60\nQP 166,46\nQR 48,236,64\nQL 76,182,56,0\n"
"QR 52,236,68\nQP 167,47\nQR 56,236,72\nQP 168,60\nQR 60,236,76\nQF 91,174,175\nQF 92,164,165\nQR 64,240,64\nQL 76,182,56,1024\nQBO 152,160,88,136\nQP 169,61\nQR 68,240,68\nQP 170,62\nQR 72,240,72\n"
"QF 93,166,167\nQF 94,168,169\nQR 76,240,76\nQL 76,182,56,2048\nQR 80,244,64\nQP 171,63\nQR 84,244,68\nQF 95,170,171\nQBY 183,144,148,152\nQR 88,244,72\nQEE 180\nQEE 181\nQR 92,244,76\n"
"QL 76,182,56,3072\nQBO 156,160,92,140\nQR 96,248,64\nQR 100,248,68\nQCI 61\nQR 104,248,72\nQL 76,182,57,0\nQR 108,248,76\nQAZ 183,156\nQEO 70,183\nQR 112,252,64\nQR 116,252,68\nQR 120,252,72\n"
"QL 76,182,57,1024\nQR 124,252,76\ns_cbranch_vccnz L_stubA0\nL_mid0:\nQAR 32,192,192,96\nQP 164,0\nQC 32,196,196\nQP 165,1\nQC 32,200,200\nQL 76,182,57,2048\nQC 32,204,204\nQE 160,190,0\n"
"QF 64,164,165\nQAR 36,192,208,100\ns_and_b32 s73, s64, 4\nQP 166,2\nQC 36,196,212\ns_lshl_b32 s73, s73, 9\nQP 167,3\nQC 36,200,216\nQL 76,182,57,3072\nQC 36,204,220\nQE 162,190,512\nQF 65,166,167\n"
"QAR 40,192,224,104\nQZ 73,99\nQP 168,16\nQC 40,196,228\ns_andn2_b32 s98, s64, 4\nQP 169,17\nQC 40,200,232\ns_cmp_eq_u32 s98, 10\nQF 66,168,169\nQC 40,204,236\nQB 224,181,0\nQAR 44,192,240,108\n"
"s_cselect_b32 s73, s73, s71\nQP 170,18\nQC 44,196,244\ns_cmp_ge_u32 s64, 12\nQP 171,19\nQC 44,200,248\ns_cselect_b32 s98, 6144, s71\nQF 67,170,171\nQC 44,204,252\nQB 228,181,1024\nQAR 48,208,192,112\n"
"QCE 64\nQP 172,4\nQBO 128,160,64,144\ns_cmp_lg_u64 s[50:51], 0\nQP 173,5\nQC 48,212,196\ns_cselect_b32 s70, 32768, 0\nQF 68,172,173\nQC 48,216,200\nQB 232,181,2048\nQC 48,220,204\n"
"s_or_b32 s70, s70, s69\nQP 174,6\nQAR 52,208,208,116\ns_bitcmp1_b32 s70, s64\nQP 175,7\nQC 52,212,212\ns_cselect_b32 s70, -1.0, s71\nQF 69,174,175\nQC 52,216,216\nQB 236,181,3072\nQC 52,220,220\n"
"QP 164,20\nQAR 56,208,224,120\nQP 165,21\nQC 56,212,228\nQF 70,164,165\nQC 56,216,232\nQB 240,181,4096\nQC 56,220,236\nQP 166,22\nQAR 60,208,240,124\nQP 167,23\nQC 60,212,244\nQF 71,166,167\n"
"QC 60,216,248\nQB 244,181,5120\nQC 60,220,252\nQP 168,8\nQBO 132,160,68,148\nQP 169,9\nQR 0,128,80\nQF 72,168,169\nQR 4,128,84\nQB 248,181,6144\nQR 8,128,88\nQG 7\nQP 170,10\nQR 12,128,92\nQP 171,11\n"
"QR 16,132,80\ns_add_u32 s72, s72, 4\nQF 73,170,171\nQR 20,132,84\nQB 252,181,7168\nQR 24,132,88\ns_min_u32 s72, s72, 60\nQP 172,24\nQR 28,132,92\nQCI 97\nQP 173,25\nQR 32,136,80\nQGT 88,188\n"
"QR 36,136,84\nQB 192,180,0\nQF 74,172,173\nQR 40,136,88\ns_add_u32 s82, s72, 1\nQP 174,26\nQR 44,136,92\ns_add_u32 s83, s72, 2\nQP 175,27\nQR 48,140,80\n"
"buffer_store_dwordx4 a[160:163], v185, s[52:55], s87 offen sc1\nQR 52,140,84\nQB 196,180,1024\nQF 75,174,175\nQR 56,140,88\ns_add_u32 s84, s72, 3\nQP 164,12\nQBO 136,160,72,152\n"
"s_xor_b32 s97, s97, 1024\nQP 165,13\nQR 60,140,92\nbuffer_atomic_add v160, v189, s[92:95], s73 offen sc1\nQR 64,144,80\nQB 200,180,4096\nQF 76,164,165\nQR 68,144,84\ns_mov_b32 s87, s86\nQP 166,14\n"
"QR 72,144,88\ns_mov_b32 s86, s85\nQP 167,15\nQR 76,144,92\nbuffer_load_dword a164, v186, s[92:95], s98 offen sc1\nQR 80,148,80\nQB 204,180,5120\nQF 77,166,167\nQR 84,148,84\nQP 168,28\nQR 88,148,88\n"
"QP 169,29\nQR 92,148,92\nQP 170,30\nQR 96,152,80\nQB 208,180,2048\nQF 78,168,169\nQR 100,152,84\nQP 171,31\nQR 104,152,88\nQF 79,170,171\nQBY 183,128,132,136\nQR 108,152,92\nQB 212,180,3072\n"
"v_xor_b32 v190, 1024, v190\nQBO 140,160,76,156\nQB 216,180,6144\nQCO 88,187,72\nQR 112,156,80\nQB 220,180,7168\nQCO 89,187,82\nQR 116,156,84\nQCO 81,187,83\nQCO 85,187,84\nQR 120,156,88\nQAZ 183,140\n"
"QEO 70,183\nQR 124,156,92\nQG 4\ns_cbranch_vccz L_loop0\nQEO 71,183\nQBD \ns_cbranch_vccnz L_ovf0\nL_ret0:\ns_cmp_eq_u32 s64, s66\ns_cbranch_scc0 L_j1\nQAT 36,96,176,184\nQAT 38,97,176,184\n"
"QAT 40,98,176,184\nQAT 42,99,176,184\nQN 100,177\nQN 101,177\nQN 102,177\nQN 103,177\nQN 104,178\nQN 105,178\nQN 106,178\nQN 107,178\nQN 108,179\nQN 109,179\nQN 110,179\nQN 111,179\nQN 112,184\n"
"QN 113,184\nQN 114,184\nQN 115,184\nQAT 36,116,177,184\nQAT 38,117,177,184\nQAT 40,118,177,184\nQAT 42,119,177,184\nQN 120,178\nQN 121,178\nQN 122,178\nQN 123,178\nQN 124,179\nQN 125,179\nQN 126,179\n"
"QN 127,179\nL_j1:\n.p2align 4\nL_loop1:\nQAR 0,192,192,96\ns_lshr_b32 s82, s81, 16\nQP 164,32\nQC 0,196,196\ns_and_b32 s81, s81, 0xffff\nQP 165,33\nQC 0,200,200\nQB 128,181,8192\nQF 80,164,165\n"
"QC 0,204,204\nQDJ 56\nQP 166,34\nQAR 4,192,208,100\nQDJ 57\nQP 167,35\nQC 4,196,212\nQB 132,181,9216\nQF 81,166,167\nQC 4,200,216\nQDK 58\nQP 168,48\nQC 4,204,220\nQDK 59\nQP 169,49\n"
"QAR 8,192,224,104\nQB 136,181,10240\nQF 82,168,169\nQC 8,196,228\nQBW 60\nQP 170,50\nQC 8,200,232\nQBW 61\nQP 171,51\nQC 8,204,236\nQB 140,181,11264\nQF 83,170,171\nQAR 12,192,240,108\nQBW 62\n"
"QP 172,36\nQC 12,196,244\nQBW 63\nQP 173,37\nQBO 144,160,80,128\nQB 144,181,12288\nQF 84,172,173\nQC 12,200,248\nQG 5\nQP 174,38\nQC 12,204,252\nQB 192,180,8192\nQDS 188,185\nQAR 16,208,192,112\n"
"QHB 49,48\nQP 175,39\nQC 16,212,196\nQB 148,181,13312\nQF 85,174,175\nQC 16,216,200\nQB 196,180,9216\nQGU 189,188\nQC 16,220,204\nQHC 98,48\nQP 164,52\nQAR 20,208,208,116\nQB 152,181,14336\n"
"QED 188,189,82\nQC 20,212,212\nQB 200,180,12288\nQGU 189,185\nQC 20,216,216\nQHD 64\nQP 165,53\nQC 20,220,220\nQB 156,181,15360\nQF 86,164,165\nQAR 24,208,224,120\nQB 204,180,13312\n"
"v_mad_u32_u24 v188, v188, s81, v189\nQC 24,212,228\nQBI 56,48\nQP 166,54\nQC 24,216,232\nQBI 58,49\nQP 167,55\nQC 24,220,236\nQBI 48,98\nQF 87,166,167\nQI 189,164\nQAR 28,208,240,124\nQHE 64\n"
"QP 168,40\nQBO 148,160,84,132\nQBI 57,48\nQP 169,41\nQC 28,212,244\nQBI 59,71\nQP 170,42\nQC 28,216,248\nQHF 63\nQP 171,43\nQC 28,220,252\ns_bitcmp1_b32 s68, s64\nQP 172,56\nQR 0,224,64\n"
"QB 208,180,10240\nQF 88,168,169\nQR 4,224,68\nQB 212,180,11264\nQF 89,170,171\nQR 8,224,72\nQB 216,180,14336\nv_cmp_ne_u32 s[50:51], s96, v189\nQR 12,224,76\nQB 220,180,15360\nQU 189,185\n"
"QR 16,228,64\ns_cselect_b32 s70, -1.0, s71\nQP 173,57\nQR 20,228,68\nQP 174,58\nQR 24,228,72\nQF 90,172,173\nQR 28,228,76\nQP 175,59\nQR 32,232,64\nQP 164,44\nQR 36,232,68\nQAM 4,0\nQP 165,45\n"
"QR 40,232,72\ns_barrier\nQR 44,232,76\nQCI 60\nQP 166,46\nQR 48,236,64\nQL 76,182,56,0\nQR 52,236,68\nQP 167,47\nQR 56,236,72\nQP 168,60\nQR 60,236,76\nQF 91,174,175\nQF 92,164,165\nQR 64,240,64\n"
"QL 76,182,56,1024\nQBO 152,160,88,136\nQP 169,61\nQR 68,240,68\nQP 170,62\nQR 72,240,72\nQF 93,166,167\nQF 94,168,169\nQR 76,240,76\nQL 76,182,56,2048\nQR 80,244,64\nQP 171,63\nQR 84,244,68\n"
"QF 95,170,171\nQBY 183,144,148,152\nQR 88,244,72\nQEE 180\nQEE 181\nQR 92,244,76\nQL 76,182,56,3072\nQBO 156,160,92,140\nQR 96,248,64\nQR 100,248,68\nQCI 62\nQR 104,248,72\nQL 76,182,58,0\n"
"QR 108,248,76\nQAZ 183,156\nQEO 70,183\nQR 112,252,64\nQR 116,252,68\nQR 120,252,72\nQL 76,182,58,1024\nQR 124,252,76\ns_cbranch_vccnz L_stubA1\nL_mid1:\nQAR 32,192,192,96\nQP 164,0\nQC 32,196,196\n"
"QP 165,1\nQC 32,200,200\nQL 76,182,58,2048\nQC 32,204,204\nQE 160,190,0\nQF 64,164,165\nQAR 36,192,208,100\ns_and_b32 s73, s64, 4\nQP 166,2\nQC 36,196,212\ns_lshl_b32 s73, s73, 9\nQP 167,3\n"
"QC 36,200,216\nQL 76,182,58,3072\nQC 36,204,220\nQE 162,190,512\nQF 65,166,167\nQAR 40,192,224,104\nQZ 73,99\nQP 168,16\nQC 40,196,228\nQCI 61\nQP 169,17\nQC 40,200,232\nQL 76,182,57,0\n"
"QC 40,204,236\nQB 224,181,0\nQF 66,168,169\nQAR 44,192,240,108\ns_andn2_b32 s98, s64, 4\nQP 170,18\nQC 44,196,244\ns_cmp_eq_u32 s98, 10\nQP 171,19\nQC 44,200,248\nQL 76,182,57,1024\nQC 44,204,252\n"
"QB 228,181,1024\nQF 67,170,171\nQAR 48,208,192,112\ns_cselect_b32 s73, s73, s71\nQP 172,4\nQBO 128,160,64,144\ns_cmp_ge_u32 s64, 12\nQP 173,5\nQC 48,212,196\nQL 76,182,57,2048\nQC 48,216,200\n"
"QB 232,181,2048\nQF 68,172,173\nQC 48,220,204\ns_cselect_b32 s98, 6144, s71\nQP 174,6\nQAR 52,208,208,116\nQCE 64\nQP 175,7\nQC 52,212,212\nQL 76,182,57,3072\nQC 52,216,216\nQB 236,181,3072\n"
"QF 69,174,175\nQC 52,220,220\ns_cmp_lg_u64 s[50:51], 0\nQP 164,20\nQAR 56,208,224,120\nQCI 63\nQP 165,21\nQC 56,212,228\nQL 76,182,59,0\nQC 56,216,232\nQB 240,181,4096\nQF 70,164,165\nQC 56,220,236\n"
"s_cselect_b32 s70, 32768, 0\nQP 166,22\nQAR 60,208,240,124\ns_or_b32 s70, s70, s69\nQP 167,23\nQC 60,212,244\nQL 76,182,59,1024\nQC 60,216,248\nQB 244,181,5120\nQF 71,166,167\nQC 60,220,252\n"
"s_bitcmp1_b32 s70, s64\nQP 168,8\nQBO 132,160,68,148\ns_cselect_b32 s70, -1.0, s71\nQP 169,9\nQR 0,128,80\nQL 76,182,59,2048\nQR 4,128,84\nQB 248,181,6144\nQF 72,168,169\nQR 8,128,88\nQG 7\n"
"QP 170,10\nQR 12,128,92\nQP 171,11\nQR 16,132,80\nQL 76,182,59,3072\nQR 20,132,84\nQB 252,181,7168\nQF 73,170,171\nQR 24,132,88\ns_add_u32 s72, s72, 4\nQP 172,24\nQR 28,132,92\nQCI 97\nQP 173,25\n"
"QR 32,136,80\nQGT 88,188\nQR 36,136,84\nQB 192,180,0\nQF 74,172,173\nQR 40,136,88\ns_min_u32 s72, s72, 60\nQP 174,26\nQR 44,136,92\ns_add_u32 s82, s72, 1\nQP 175,27\nQR 48,140,80\n"
"buffer_store_dwordx4 a[160:163], v185, s[52:55], s87 offen sc1\nQR 52,140,84\nQB 196,180,1024\nQF 75,174,175\nQR 56,140,88\ns_add_u32 s83, s72, 2\nQP 164,12\nQBO 136,160,72,152\n"
"s_add_u32 s84, s72, 3\nQP 165,13\nQR 60,140,92\nbuffer_atomic_add v160, v189, s[92:95], s73 offen sc1\nQR 64,144,80\nQB 200,180,4096\nQF 76,164,165\nQR 68,144,84\ns_xor_b32 s97, s97, 1024\nQP 166,14\n"
"QR 72,144,88\ns_mov_b32 s87, s86\nQP 167,15\nQR 76,144,92\nbuffer_load_dword a164, v186, s[92:95], s98 offen sc1\nQR 80,148,80\nQB 204,180,5120\nQF 77,166,167\nQR 84,148,84\ns_mov_b32 s86, s85\n"
"QP 168,28\nQR 88,148,88\nQP 169,29\nQR 92,148,92\nQP 170,30\nQR 96,152,80\nQB 208,180,2048\nQF 78,168,169\nQR 100,152,84\nQP 171,31\nQR 104,152,88\nQF 79,170,171\nQBY 183,128,132,136\nQR 108,152,92\n"
"QB 212,180,3072\nv_xor_b32 v190, 1024, v190\nQBO 140,160,76,156\nQB 216,180,6144\nQCO 88,187,72\nQR 112,156,80\nQB 220,180,7168\nQCO 89,187,82\nQR 116,156,84\nQCO 81,187,83\nQCO 85,187,84\n"
"QR 120,156,88\nQAZ 183,140\nQEO 70,183\nQR 124,156,92\nQG 4\ns_cbranch_vccz L_loop1\nQEO 71,183\nQBD \ns_cbranch_vccnz L_ovf1\nL_disp1:\ns_cmp_lt_u32 s64, 15\ns_cbranch_scc1 L_d1a\n"
"s_cmp_eq_u32 s64, 17\ns_cbranch_scc1 L_drain\nQI 164,164\nQHH 164\nQBD \ns_cbranch_vccnz L_ps18\nL_pr18:\nL_d1a:\ns_cmp_eq_u32 s64, s65\ns_cbranch_scc1 L_switch\ns_cmp_lg_u32 s64, s66\n"
"s_cbranch_scc1 L_loop1\nQAT 36,96,176,184\nQAT 38,97,176,184\nQAT 40,98,176,184\nQAT 42,99,176,184\nQN 100,177\nQN 101,177\nQN 102,177\nQN 103,177\nQN 104,178\nQN 105,178\nQN 106,178\nQN 107,178\n"
"QN 108,179\nQN 109,179\nQN 110,179\nQN 111,179\nQN 112,184\nQN 113,184\nQN 114,184\nQN 115,184\nQAT 36,116,177,184\nQAT 38,117,177,184\nQAT 40,118,177,184\nQAT 42,119,177,184\nQN 120,178\nQN 121,178\n"
"QN 122,178\nQN 123,178\nQN 124,179\nQN 125,179\nQN 126,179\nQN 127,179\ns_branch L_loop1\nL_stubA0:\nQEO 71,183\nQBD \ns_cbranch_vccnz L_ova0\nL_sa0:\ns_cmp_lg_u32 s64, s66\ns_cbranch_scc1 L_mid0\n"
"QBD \ns_branch L_mid0\nL_ova0:\ns_call_b64 s[74:75], L_rescale1\ns_branch L_sa0\nL_ovf0:\ns_call_b64 s[74:75], L_rescale0\ns_branch L_disp0\nL_stubA1:\nQEO 71,183\nQBD \ns_cbranch_vccnz L_ova1\n"
"L_sa1:\ns_cmp_lg_u32 s64, s66\ns_cbranch_scc1 L_mid1\nQN 96,184\nQN 97,184\nQN 98,184\nQN 99,184\nQN 100,184\nQN 101,184\nQN 102,184\nQN 103,184\nQAT 36,104,178,184\nQAT 38,105,178,184\n"
"QAT 40,106,178,184\nQAT 42,107,178,184\nQN 108,179\nQN 109,179\nQN 110,179\nQN 111,179\nQN 112,184\nQN 113,184\nQN 114,184\nQN 115,184\nQN 116,184\nQN 117,184\nQN 118,184\nQN 119,184\nQN 120,184\n"
"QN 121,184\nQN 122,184\nQN 123,184\nQAT 36,124,179,184\nQAT 38,125,179,184\nQAT 40,126,179,184\nQAT 42,127,179,184\nQBD \ns_branch L_mid1\nL_ova1:\ns_call_b64 s[74:75], L_rescale1\ns_branch L_sa1\n"
"L_ovf1:\ns_call_b64 s[74:75], L_rescale0\ns_branch L_disp1\nL_switch:\ns_sub_u32 s83, 3, s80\nQGA 83\nQEC 83\nQBM 80,83,185\nQG 0\nQR 0,224,64\nQB 128,181,8192\nQR 16,228,64\nQDJ 56\nQP 168,40\n"
"QR 32,232,64\nQP 169,41\nQR 48,236,64\nQB 132,181,9216\nQF 88,168,169\nQR 64,240,64\nQDJ 57\nQP 170,42\nQR 80,244,64\nQP 171,43\nQR 96,248,64\nQB 136,181,10240\nQF 89,170,171\nQR 112,252,64\nQDK 58\n"
"QP 172,56\nQR 4,224,68\nQP 173,57\nQR 20,228,68\nQB 140,181,11264\nQF 90,172,173\nQR 36,232,68\nQDK 59\nQP 174,58\nQR 52,236,68\nQP 175,59\nQR 68,240,68\nQB 144,181,12288\nQF 91,174,175\n"
"QR 84,244,68\nQBW 60\nQP 164,44\nQR 100,248,68\nQP 165,45\nQR 116,252,68\nQB 148,181,13312\nQF 92,164,165\nQR 8,224,72\nQBW 61\nQP 166,46\nQR 24,228,72\nQP 167,47\nQR 40,232,72\nQB 152,181,14336\n"
"QF 93,166,167\nQR 56,236,72\nQBW 62\nQP 168,60\nQR 72,240,72\nQP 169,61\nQR 88,244,72\nQB 156,181,15360\nQF 94,168,169\nQR 104,248,72\nQBW 63\nQP 170,62\nQR 120,252,72\nQHB 49,48\nQP 171,63\n"
"QHC 98,48\nQHD 64\nQBI 56,48\nQBI 58,49\nQBI 48,98\nQHE 64\nQBI 57,48\nQBI 59,71\nQHF 63\nQF 95,170,171\nQR 12,224,76\nQH 192,80,0\nQI 240,176\nQR 28,228,76\nQH 196,80,1024\nQI 241,177\n"
"QBO 152,160,88,136\nQH 200,80,2048\nQI 242,178\nQBO 156,160,92,140\nQH 204,80,3072\nQI 243,179\nQR 44,232,76\nQH 208,80,4096\nQI 244,180\nQR 60,236,76\nQH 212,80,5120\nQI 245,181\nQR 76,240,76\n"
"QH 216,80,6144\nQI 246,182\nQR 92,244,76\nQH 220,80,7168\nQI 247,183\nQR 108,248,76\nQH 224,80,8192\nQI 248,184\nQR 124,252,76\nQH 228,80,9216\nQI 249,185\nQH 232,80,10240\nQH 236,80,11264\n"
"v_max_f32 v183, v152, v156\nQEO 71,183\nQI 250,186\nQI 251,187\nQI 252,188\nQI 253,189\nQI 254,190\nQI 255,191\nQAM 4,0\ns_barrier\ns_cbranch_vccz L_sw_ok\ns_call_b64 s[74:75], L_rescale1\nL_sw_ok:\n"
"QEE 180\nQEE 181\nv_add_u32 v180, 65536, v180\nv_add_u32 v181, 65536, v181\nv_rcp_f32 v130, v128\nv_rcp_f32 v134, v132\nv_rcp_f32 v138, v152\nv_rcp_f32 v142, v156\nQR 8,128,88\nQCI 60\nQAG 176\n"
"QAG 177\nQR 24,132,88\nQL 76,182,56,0\nQR 40,136,88\nQL 76,182,56,1024\nQR 56,140,88\nQL 76,182,56,2048\nQR 72,144,88\nQL 76,182,56,3072\nQR 88,148,88\nQCI 62\nQAG 178\nQAG 179\nQR 104,152,88\n"
"QL 76,182,58,0\nQR 120,156,88\nQL 76,182,58,1024\nQR 12,128,92\nQL 76,182,58,2048\nQR 28,132,92\nQL 76,182,58,3072\nQR 44,136,92\nQCI 61\nQR 60,140,92\nQL 76,182,57,0\nQR 76,144,92\n"
"QL 76,182,57,1024\nQR 92,148,92\nQL 76,182,57,2048\nQR 108,152,92\nQL 76,182,57,3072\nQR 124,156,92\nQCI 63\nQBE \nQL 76,182,59,0\nQL 76,182,59,1024\nQL 76,182,59,2048\nQL 76,182,59,3072\n"
"QB 192,180,0\nQB 196,180,1024\nQB 200,180,4096\nQB 204,180,5120\nQB 208,180,2048\nQB 212,180,3072\nQB 216,180,6144\nQB 220,180,7168\nQI 0,0\nQI 1,1\nQI 2,2\nQI 3,3\nQI 4,16\nQI 5,17\nQI 6,18\n"
"QI 7,19\nQAC 0,130\nQAC 2,130\nQAC 4,130\nQAC 6,130\nQAY 0,1\nQF 1,2,3\nQF 2,4,5\nQF 3,6,7\nQAB 0,44,185,90,0\nQI 8,32\nQI 9,33\nQI 10,34\nQI 11,35\nQI 12,48\nQI 13,49\nQI 14,50\nQI 15,51\nQAC 8,130\n"
"QAC 10,130\nQAC 12,130\nQAC 14,130\nQAY 8,9\nQF 9,10,11\nQF 10,12,13\nQF 11,14,15\nQAB 8,44,185,90,1024\nQI 16,64\nQI 17,65\nQI 18,66\nQI 19,67\nQI 20,80\nQI 21,81\nQI 22,82\nQI 23,83\nQAC 16,130\n"
"QAC 18,130\nQAC 20,130\nQAC 22,130\nQAY 16,17\nQF 17,18,19\nQF 18,20,21\nQF 19,22,23\nQAB 16,44,185,90,2048\nQI 24,96\nQI 25,97\nQI 26,98\nQI 27,99\nQI 28,112\nQI 29,113\nQI 30,114\nQI 31,115\n"
"QAC 24,130\nQAC 26,130\nQAC 28,130\nQAC 30,130\nQAY 24,25\nQF 25,26,27\nQF 26,28,29\nQF 27,30,31\nQAB 24,44,185,90,3072\nQEC 90\nQI 32,4\nQI 33,5\nQI 34,6\nQI 35,7\nQI 36,20\nQI 37,21\nQI 38,22\n"
"QI 39,23\nQAC 32,134\nQAC 34,134\nQAC 36,134\nQAC 38,134\nQAY 32,33\nQF 33,34,35\nQF 34,36,37\nQF 35,38,39\nQAB 32,44,185,90,0\nQI 40,36\nQI 41,37\nQI 42,38\nQI 43,39\nQI 44,52\nQI 45,53\nQI 46,54\n"
"QI 47,55\nQAC 40,134\nQAC 42,134\nQAC 44,134\nQAC 46,134\nQAY 40,41\nQF 41,42,43\nQF 42,44,45\nQF 43,46,47\nQAB 40,44,185,90,1024\nQI 48,68\nQI 49,69\nQI 50,70\nQI 51,71\nQI 52,84\nQI 53,85\n"
"QI 54,86\nQI 55,87\nQAC 48,134\nQAC 50,134\nQAC 52,134\nQAC 54,134\nQAY 48,49\nQF 49,50,51\nQF 50,52,53\nQF 51,54,55\nQAB 48,44,185,90,2048\nQI 56,100\nQI 57,101\nQI 58,102\nQI 59,103\nQI 60,116\n"
"QI 61,117\nQI 62,118\nQI 63,119\nQAC 56,134\nQAC 58,134\nQAC 60,134\nQAC 62,134\nQAY 56,57\nQF 57,58,59\nQF 58,60,61\nQF 59,62,63\nQAB 56,44,185,90,3072\nQEC 90\nQI 0,8\nQI 1,9\nQI 2,10\nQI 3,11\n"
"QI 4,24\nQI 5,25\nQI 6,26\nQI 7,27\nQAC 0,138\nQAC 2,138\nQAC 4,138\nQAC 6,138\nQAY 0,1\nQF 1,2,3\nQF 2,4,5\nQF 3,6,7\nQAB 0,44,185,90,0\nQI 8,40\nQI 9,41\nQI 10,42\nQI 11,43\nQI 12,56\nQI 13,57\n"
"QI 14,58\nQI 15,59\nQAC 8,138\nQAC 10,138\nQAC 12,138\nQAC 14,138\nQAY 8,9\nQF 9,10,11\nQF 10,12,13\nQF 11,14,15\nQAB 8,44,185,90,1024\nQI 16,72\nQI 17,73\nQI 18,74\nQI 19,75\nQI 20,88\nQI 21,89\n"
"QI 22,90\nQI 23,91\nQAC 16,138\nQAC 18,138\nQAC 20,138\nQAC 22,138\nQAY 16,17\nQF 17,18,19\nQF 18,20,21\nQF 19,22,23\nQAB 16,44,185,90,2048\nQI 24,104\nQI 25,105\nQI 26,106\nQI 27,107\nQI 28,120\n"
"QI 29,121\nQI 30,122\nQI 31,123\nQAC 24,138\nQAC 26,138\nQAC 28,138\nQAC 30,138\nQAY 24,25\nQF 25,26,27\nQF 26,28,29\nQF 27,30,31\nQAB 24,44,185,90,3072\nQEC 90\nQAT 36,96,176,184\nQAT 38,97,176,184\n"
"QAT 40,98,176,184\nQAT 42,99,176,184\nQN 100,177\nQN 101,177\nQN 102,177\nQN 103,177\nQN 104,178\nQN 105,178\nQN 106,178\nQN 107,178\nQN 108,179\nQN 109,179\nQN 110,179\nQN 111,179\nQN 16,184\n"
"QN 17,184\nQN 18,184\nQN 19,184\nQAT 36,116,177,184\nQAT 38,117,177,184\nQAT 40,118,177,184\nQAT 42,119,177,184\nQN 120,178\nQN 121,178\nQN 122,178\nQN 123,178\nQN 124,179\nQN 125,179\nQN 126,179\n"
"QN 127,179\nQCE 64\ns_cmp_lt_u32 s64, 15\ns_cbranch_scc1 L_sw_go\ns_cmp_eq_u32 s64, 17\ns_cbranch_scc1 L_empty\nQI 164,164\nQHH 164\nQBD \ns_cbranch_vccnz L_ps19\nL_pr19:\nL_sw_go:\nQG 0\n"
"QAR 0,192,192,96\nQDJ 56\nQI 64,12\nQI 65,13\nQC 0,196,196\nQDJ 57\nQI 66,14\nQI 67,15\nQC 0,200,200\nQDK 58\nQI 68,28\nQI 69,29\nQC 0,204,204\nQDK 59\nQI 70,30\nQI 71,31\nQAR 4,192,208,100\nQBW 60\n"
"QM 64,142\nQM 65,142\nQC 4,196,212\nQBW 61\nQM 66,142\nQM 67,142\nQC 4,200,216\nQBW 62\nQM 68,142\nQM 69,142\nQC 4,204,220\nQBW 63\nQM 70,142\nQM 71,142\nQAR 8,192,224,104\nQHB 49,48\nQAY 64,65\n"
"QF 65,66,67\nQC 8,196,228\nQHC 98,48\nQF 66,68,69\nQF 67,70,71\nQC 8,200,232\nQHD 64\nQBI 56,48\nQBI 58,49\nQBI 48,98\nQC 8,204,236\nQHE 64\nQBI 57,48\nQBI 59,71\nQHF 63\nQAR 12,192,240,108\n"
"QAB 64,44,185,90,0\nQC 12,196,244\nQI 72,44\nQI 73,45\nQC 12,200,248\nQI 74,46\nQI 75,47\nQC 12,204,252\nQI 76,60\nQI 77,61\nQAR 20,208,208,116\nQB 192,180,8192\nQI 78,62\nQC 20,212,212\n"
"QB 196,180,9216\nQI 79,63\nQC 20,216,216\nQB 200,180,12288\nQM 72,142\nQC 20,220,220\nQB 204,180,13312\nQM 73,142\nQAR 24,208,224,120\nQM 74,142\nQM 75,142\nQC 24,212,228\nQM 76,142\nQM 77,142\n"
"QC 24,216,232\nQM 78,142\nQM 79,142\nQC 24,220,236\nQAY 72,73\nQF 73,74,75\nQAR 28,208,240,124\nQF 74,76,77\nQF 75,78,79\nQC 28,212,244\nQAB 72,44,185,90,1024\nQC 28,216,248\nQI 80,76\nQI 81,77\n"
"QC 28,220,252\nQI 82,78\nQI 83,79\nQB 208,180,10240\nQB 212,180,11264\nQB 216,180,14336\nQB 220,180,15360\nQI 84,92\nQI 85,93\nQI 86,94\nQI 87,95\nQM 80,142\nQM 81,142\nQM 82,142\nQM 83,142\n"
"QM 84,142\nQM 85,142\nQM 86,142\nQM 87,142\nQAY 80,81\nQF 81,82,83\nQF 82,84,85\nQF 83,86,87\nQAB 80,44,185,90,2048\nQI 88,108\nQI 89,109\nQI 90,110\nQI 91,111\nQI 92,124\nQI 93,125\nQI 94,126\n"
"QI 95,127\nQM 88,142\nQM 89,142\nQM 90,142\nQM 91,142\nQM 92,142\nQM 93,142\nQM 94,142\nQM 95,142\nQAY 88,89\nQF 89,90,91\nQF 90,92,93\nQF 91,94,95\nQAB 88,44,185,90,3072\nQEC 90\nQAM 16,0\n"
"s_barrier\nQEE 180\nQEE 181\nQCP 0,176,160\nQCI 60\nQBY 164,0,1,2\nQBY 165,4,5,6\nQBY 166,8,9,10\nQBY 167,12,13,14\nQBK 164,3,16\nQL 76,182,56,0\nQCP 16,176,160\nQBK 165,7,20\nQBK 166,11,24\n"
"QBK 167,15,28\nQBK 164,17,18\nQBK 165,21,22\nQL 76,182,56,1024\nQCP 32,176,160\nQBK 166,25,26\nQBK 167,29,30\nQAZ 164,19\nQAZ 165,23\nQAZ 166,27\nQL 76,182,56,2048\nQCP 48,176,160\nQAZ 167,31\n"
"QN 172,164\nQN 173,165\nQN 174,166\nQN 175,167\nQL 76,182,56,3072\nQCP 64,176,160\nQCI 62\nQBS 164,172\nQBS 165,173\nQBS 166,174\nQBS 167,175\nQAZ 164,172\nQL 76,182,58,0\nQCP 80,176,160\n"
"QAZ 165,173\nQAZ 166,174\nQAZ 167,175\nQN 172,164\nQN 173,165\nQL 76,182,58,1024\nQCP 96,176,160\nQN 174,166\nQN 175,167\nQBN 164,172\nQBN 165,173\nQBN 166,174\nQL 76,182,58,2048\nQCP 112,176,160\n"
"QBN 167,175\nQAZ 164,172\nQAZ 165,173\nQAZ 166,174\nQAZ 167,175\nQL 76,182,58,3072\nQEF 176,164\nQEF 177,165\nQEF 178,166\nQEF 179,167\nQN 32,184\nQN 33,184\nQN 34,184\nQN 35,184\nQN 36,184\n"
"QN 37,184\nQN 38,184\nQN 39,184\nQAT 36,104,178,184\nQAT 38,105,178,184\nQAT 40,106,178,184\nQAT 42,107,178,184\nQN 108,179\nQN 109,179\nQN 110,179\nQN 111,179\nQN 48,184\nQN 49,184\nQN 50,184\n"
"QN 51,184\nQN 52,184\nQN 53,184\nQN 54,184\nQN 55,184\nQN 56,184\nQN 57,184\nQN 58,184\nQN 59,184\nQAT 36,124,179,184\nQAT 38,125,179,184\nQAT 40,126,179,184\nQAT 42,127,179,184\nQAR 40,192,224,104\n"
"QCI 61\nQAJ 0,176\nQAJ 1,176\nQC 40,196,228\nQB 224,181,0\nQAJ 2,176\nQC 40,200,232\nQL 76,182,57,0\nQC 40,204,236\nQB 228,181,1024\nQAJ 3,176\nQAR 44,192,240,108\nQL 76,182,57,1024\nQC 44,196,244\n"
"QB 232,181,2048\nQAJ 16,176\nQC 44,200,248\nQL 76,182,57,2048\nQC 44,204,252\nQB 236,181,3072\nQAJ 17,176\nQAR 60,208,240,124\nQL 76,182,57,3072\nQC 60,212,244\nQB 240,181,4096\nQAJ 18,176\n"
"QC 60,216,248\nQCI 63\nQP 164,0\nQC 60,220,252\nQB 244,181,5120\nQAJ 19,176\nQL 76,182,59,0\nQB 248,181,6144\nQL 76,182,59,1024\nQB 252,181,7168\nQL 76,182,59,2048\nQB 192,180,0\nQL 76,182,59,3072\n"
"QB 196,180,1024\nQFR 92,168,71\nQB 200,180,4096\nQFR 92,168,71\nQB 204,180,5120\nQFR 92,168,71\nQFR 92,168,71\nQB 208,180,2048\nQB 212,180,3072\nQB 216,180,6144\nQB 220,180,7168\nQP 165,1\nQP 166,2\n"
"QP 167,3\nQP 168,16\nQP 169,17\nQP 170,18\nQP 171,19\nQF 64,164,165\nQF 65,166,167\nQF 66,168,169\nQF 67,170,171\nQAJ 4,177\nQAJ 5,177\nQFS 128,160,64\nQAJ 6,177\nQAJ 7,177\nQAJ 20,177\nQAJ 21,177\n"
"QAJ 22,177\nQAJ 23,177\nQP 172,4\nQP 173,5\nQP 174,6\nQP 175,7\nQP 164,20\nQP 165,21\nQP 166,22\nQP 167,23\nQF 68,172,173\nQF 69,174,175\nQF 70,164,165\nQF 71,166,167\nQAJ 8,178\nQAJ 9,178\n"
"QFS 132,160,68\nQAJ 10,178\nQAJ 11,178\nQAJ 24,178\nQAJ 25,178\nQAJ 26,178\nQAJ 27,178\nQP 168,8\nQP 169,9\nQP 170,10\nQP 171,11\nQP 172,24\nQP 173,25\nQP 174,26\nQP 175,27\nQF 72,168,169\n"
"QF 73,170,171\nQF 74,172,173\nQF 75,174,175\nQAJ 12,179\nQAJ 13,179\nQFS 136,160,72\nQAJ 14,179\nQAJ 15,179\nQAJ 28,179\nQAJ 29,179\nQAJ 30,179\nQAJ 31,179\nQP 164,12\nQP 165,13\nQP 166,14\n"
"QP 167,15\nQP 168,28\nQP 169,29\nQP 170,30\nQP 171,31\nQF 76,164,165\nQF 77,166,167\nQF 78,168,169\nQF 79,170,171\nQN 96,176\nQN 97,176\nQFS 140,160,76\nQN 98,176\nQN 99,176\nQN 100,177\nQN 101,177\n"
"QN 102,177\nQN 103,177\nQN 104,178\nQN 105,178\nQN 106,178\nQN 107,178\nQN 108,179\nQN 109,179\nQN 110,179\nQN 111,179\nQN 112,176\nQN 113,176\nQN 114,176\nQN 115,176\nQN 116,177\nQN 117,177\n"
"QN 118,177\nQN 119,177\nQN 120,178\nQN 121,178\nQN 122,178\nQN 123,178\nQN 124,179\nQN 125,179\nQN 126,179\nQN 127,179\nQCE 64\nQG 0\ns_cmp_lt_u32 s64, 15\ns_cbranch_scc1 L_loop1\ns_branch L_disp1\n"
"L_empty:\nQI 64,12\nQI 65,13\nQI 66,14\nQI 67,15\nQI 68,28\nQI 69,29\nQI 70,30\nQI 71,31\nQAC 64,142\nQAC 66,142\nQAC 68,142\nQAC 70,142\nQAY 64,65\nQF 65,66,67\nQF 66,68,69\nQF 67,70,71\n"
"QAB 64,44,185,90,0\nQI 72,44\nQI 73,45\nQI 74,46\nQI 75,47\nQI 76,60\nQI 77,61\nQI 78,62\nQI 79,63\nQAC 72,142\nQAC 74,142\nQAC 76,142\nQAC 78,142\nQAY 72,73\nQF 73,74,75\nQF 74,76,77\nQF 75,78,79\n"
"QAB 72,44,185,90,1024\nQI 80,76\nQI 81,77\nQI 82,78\nQI 83,79\nQI 84,92\nQI 85,93\nQI 86,94\nQI 87,95\nQAC 80,142\nQAC 82,142\nQAC 84,142\nQAC 86,142\nQAY 80,81\nQF 81,82,83\nQF 82,84,85\n"
"QF 83,86,87\nQAB 80,44,185,90,2048\nQI 88,108\nQI 89,109\nQI 90,110\nQI 91,111\nQI 92,124\nQI 93,125\nQI 94,126\nQI 95,127\nQAC 88,142\nQAC 90,142\nQAC 92,142\nQAC 94,142\nQAY 88,89\nQF 89,90,91\n"
"QF 90,92,93\nQF 91,94,95\nQAB 88,44,185,90,3072\nQEC 90\nQG 0\nQCG 84\nL_pc20:\ns_lshr_b32 s86, %1, 3\nQBH 86\ns_add_u32 s86, s86, %0\ns_mul_i32 s86, s86, ((L_done-L_attn_end-1024)/127+63)&-64\n"
"s_min_u32 s86, s86, L_done-L_attn_end-1024\ns_add_u32 s86, s86, L_attn_end-L_pc20\nQZ 84,86\nQAX 85\nQCL 198,185\nQFG 196,84,198\ns_and_b32 s83, %1, 1\nQCK 83\ns_bfe_u32 s82, %1, 196611\nQZ 82,83\n"
"s_bfe_u32 s83, %1, 131073\nQBH 83\ns_lshr_b32 s84, %1, 6\nQZ 83,84\nQEG 81,83\ns_add_u32 s81, s81, %0\ns_lshl_b32 s81, s81, 1\ns_mov_b64 s[56:57], %2\ns_add_u32 s56, s56, 69795840\nQAX 57\n"
"s_mov_b32 s58, 2293760\nQBA 59\ns_mov_b64 s[60:61], %2\ns_add_u32 s60, s60, 58195968\nQAX 61\nQDL 62\nQBA 63\ns_mov_b64 s[64:65], %2\ns_add_u32 s64, s64, 79626240\nQAX 65\ns_mov_b32 s66, 131072\n"
"QBA 67\ns_lshl_b32 s69, %0, 12\ns_mul_i32 s68, s82, 143360\ns_add_u32 s68, s68, 71680\nQGE 70,81\ns_add_u32 s71, s70, 20480\ns_lshl_b32 s87, %0, 10\nQCJ 83,82\ns_mul_i32 s83, s83, 7168\nQBX 84,82\n"
"s_mul_i32 s72, s84, 3072\nQZ 72,83\nQCK 84\nQBT 73,83,84\nQHG 73\ns_mul_i32 s83, s82, 3072\nQHG 83\ns_cmp_lt_u32 s82, 12\ns_cselect_b32 s72, s72, s83\ns_cselect_b32 s73, s73, 55296\n"
"s_mul_i32 s83, s81, 57344\nQZ 72,83\nQZ 73,83\nQAD 0\nQAD 1\nQAD 2\nQAD 3\nQAD 4\nQAD 5\nQAD 6\nQAD 7\nQAD 8\nQAD 9\nQAD 10\nQAD 11\nQAD 12\nQAD 13\nQAD 14\nQAD 15\nQAD 16\nQAD 17\nQAD 18\nQAD 19\n"
"QAD 20\nQAD 21\nQAD 22\nQAD 23\nQAD 24\nQAD 25\nQAD 26\nQAD 27\nQAD 28\nQAD 29\nQAD 30\nQAD 31\nQAD 32\nQAD 33\nQAD 34\nQAD 35\nQAD 36\nQAD 37\nQAD 38\nQAD 39\nQAD 40\nQAD 41\nQAD 42\nQAD 43\nQAD 44\n"
"QAD 45\nQAD 46\nQAD 47\nQAD 48\nQAD 49\nQAD 50\nQAD 51\nQAD 52\nQAD 53\nQAD 54\nQAD 55\nQAD 56\nQAD 57\nQAD 58\nQAD 59\nQAD 60\nQAD 61\nQAD 62\nQAD 63\nQAD 64\nQAD 65\nQAD 66\nQAD 67\nQAD 68\nQAD 69\n"
"QAD 70\nQAD 71\nQAD 72\nQAD 73\nQAD 74\nQAD 75\nQAD 76\nQAD 77\nQAD 78\nQAD 79\nQAD 80\nQAD 81\nQAD 82\nQAD 83\nQAD 84\nQAD 85\nQAD 86\nQAD 87\nQAD 88\nQAD 89\nQAD 90\nQAD 91\nQAD 92\nQAD 93\nQAD 94\n"
"QAD 95\nQAD 96\nQAD 97\nQAD 98\nQAD 99\nQAD 100\nQAD 101\nQAD 102\nQAD 103\nQAD 104\nQAD 105\nQAD 106\nQAD 107\nQAD 108\nQAD 109\nQAD 110\nQAD 111\nQAD 112\nQAD 113\nQAD 114\nQAD 115\nQAD 116\n"
"QAD 117\nQAD 118\nQAD 119\nQAD 120\nQAD 121\nQAD 122\nQAD 123\nQAD 124\nQAD 125\nQAD 126\nQAD 127\nQAG 80\nQAG 81\nQAG 82\nQAG 83\nQAG 84\nQAG 85\nQAG 86\nQAG 87\nQAG 88\nQAG 89\nQAG 90\nQAG 91\n"
"QAG 92\nQAG 93\nQAG 94\nQAG 95\nv_mov_b32 v176, 0x7f61b1e6\nQAG 144\nv_mov_b32 v177, 0x7f61b1e6\nQAG 148\nv_mov_b32 v178, 0x7f61b1e6\nQAG 152\nv_mov_b32 v179, 0x7f61b1e6\nQAG 156\ns_branch L_combine\n"
"L_ps3:\nQDB 101\nL_pl3:\nQCE 98\nQBJ 242,98\nQDX 92,242,97\ns_sleep 4\nQGK 99\nQCZ 92,127,99\nQCS 101\nQBZ 101\nQAH 0\ns_cbranch_scc1 L_pr3\nv_xor_b32 v242, v127, v126\nQBE \nQGS 100,242\nQBE \n"
"QBZ 100\ns_cbranch_scc1 L_pl3\ns_branch L_pr3\nL_ps5:\nQDB 101\nL_pl5:\nQCE 98\nQBJ 243,98\nQDX 92,243,97\nQAH 0\nQDP \nQCV 243,241\nQCW 243\nQDC 99\nQEB 92,242,243,99\nQCS 101\nQBZ 101\nQAH 0\n"
"s_cbranch_scc1 L_pr5\nQFF 242\nQDR \ns_cbranch_vccnz L_pl5\ns_branch L_pr5\nL_ps8:\nQDB 101\nL_pl8:\nQCE 98\nQBJ 242,98\nQDX 92,242,97\ns_sleep 4\nQGK 99\nQCZ 92,127,99\nQCS 101\nQBZ 101\nQAH 0\n"
"s_cbranch_scc1 L_pr8\nv_xor_b32 v242, v127, v126\nQBE \nQGS 100,242\nQBE \nQBZ 100\ns_cbranch_scc1 L_pl8\ns_branch L_pr8\nL_ps10:\nQDB 101\nL_pl10:\nQCE 98\nQBJ 243,98\nQDX 92,243,97\nQAH 0\nQDP \n"
"QCV 243,241\nQCW 243\nQDC 99\nQEB 92,242,243,99\nQCS 101\nQBZ 101\nQAH 0\ns_cbranch_scc1 L_pr10\nQFF 242\nQDR \ns_cbranch_vccnz L_pl10\ns_branch L_pr10\nL_ps13:\nQDB 101\nL_pl13:\nQDP \n"
"s_bfe_u32 s99, %1, 196608\nQBH 99\ns_bfe_u32 s100, %1, 131078\nQZ 99,100\nQBU 99\nbuffer_load_dword v156, off, s[92:95], s99 offset:2048 sc1\nQCS 101\nQBZ 101\nQAH 0\ns_cbranch_scc1 L_pr13\n"
"v_cmp_ne_u32 vcc, 8, v156\nQDR \ns_cbranch_vccnz L_pl13\ns_branch L_pr13\nL_ps16:\nQDB 101\nL_pl16:\nQDP \ns_bfe_u32 s99, %1, 196608\nQBH 99\ns_bfe_u32 s100, %1, 131078\nQZ 99,100\n"
"s_sub_u32 s100, s99, 17\nQDU 156,185\nQBM 157,100,156\nv_med3_i32 v157, v157, 0, 15\nv_cmp_lt_u32 vcc, 2, v156\nQBJ 156,99\nQBE \nQDG 157,156\nv_lshlrev_b32 v157, 7, v157\n"
"buffer_load_dword v156, v157, s[92:95], 0 offen offset:2048 sc1\nQCS 101\nQBZ 101\nQAH 0\ns_cbranch_scc1 L_pr16\nv_cmp_ne_u32 vcc, 8, v156\nQDR \ns_cbranch_vccnz L_pl16\ns_branch L_pr16\nL_rescale0:\n"
"QDO \ns_nop 3\nQBY 164,0,1,2\nQBK 164,3,16\nQBK 164,17,18\nQFT 164,19\nQBY 165,4,5,6\nQBK 165,7,20\nQBK 165,21,22\nQFT 165,23\nQBY 166,8,9,10\nQBK 166,11,24\nQBK 166,25,26\nQFT 166,27\n"
"QBY 167,12,13,14\nQBK 167,15,28\nQBK 167,29,30\nQFT 167,31\nQN 172,164\nQN 173,165\nQN 174,166\nQN 175,167\nQBS 164,172\nQBS 165,173\nQBS 166,174\nQBS 167,175\nQAZ 164,172\nQAZ 165,173\nQAZ 166,174\n"
"QAZ 167,175\nQN 172,164\nQN 173,165\nQN 174,166\nQN 175,167\nQBN 164,172\nQBN 165,173\nQBN 166,174\nQBN 167,175\nQAZ 164,172\nQAZ 165,173\nQAZ 166,174\nQAZ 167,175\nQT 176,164\nQT 0,164\nQT 1,164\n"
"QT 2,164\nQT 3,164\nQT 16,164\nQT 17,164\nQT 18,164\nQT 19,164\nQT 32,164\nQT 33,164\nQT 34,164\nQT 35,164\nQT 48,164\nQT 49,164\nQT 50,164\nQT 51,164\nQT 96,164\nQT 97,164\nQT 98,164\nQT 99,164\n"
"QT 112,164\nQT 113,164\nQT 114,164\nQT 115,164\nQT 177,165\nQT 4,165\nQT 5,165\nQT 6,165\nQT 7,165\nQT 20,165\nQT 21,165\nQT 22,165\nQT 23,165\nQT 36,165\nQT 37,165\nQT 38,165\nQT 39,165\nQT 52,165\n"
"QT 53,165\nQT 54,165\nQT 55,165\nQT 100,165\nQT 101,165\nQT 102,165\nQT 103,165\nQT 116,165\nQT 117,165\nQT 118,165\nQT 119,165\nQT 178,166\nQT 8,166\nQT 9,166\nQT 10,166\nQT 11,166\nQT 24,166\n"
"QT 25,166\nQT 26,166\nQT 27,166\nQT 40,166\nQT 41,166\nQT 42,166\nQT 43,166\nQT 56,166\nQT 57,166\nQT 58,166\nQT 59,166\nQT 104,166\nQT 105,166\nQT 106,166\nQT 107,166\nQT 120,166\nQT 121,166\n"
"QT 122,166\nQT 123,166\nQT 179,167\nQT 12,167\nQT 13,167\nQT 14,167\nQT 15,167\nQT 28,167\nQT 29,167\nQT 30,167\nQT 31,167\nQT 44,167\nQT 45,167\nQT 46,167\nQT 47,167\nQT 60,167\nQT 61,167\n"
"QT 62,167\nQT 63,167\nQT 108,167\nQT 109,167\nQT 110,167\nQT 111,167\nQT 124,167\nQT 125,167\nQT 126,167\nQT 127,167\nQFU 168,164\nQFU 169,165\nQFU 170,166\nQFU 171,167\nQM 144,168\nQM 145,168\n"
"QM 146,168\nQM 147,168\nQM 148,169\nQM 149,169\nQM 150,169\nQM 151,169\nQM 152,170\nQM 153,170\nQM 154,170\nQM 155,170\nQM 156,171\nQM 157,171\nQM 158,171\nQM 159,171\nQI 172,0\nQI 173,1\nQI 174,2\n"
"QI 175,3\nQM 172,168\nQM 173,168\nQM 174,168\nQM 175,168\nQO 0,172\nQO 1,173\nQO 2,174\nQO 3,175\nQI 172,4\nQI 173,5\nQI 174,6\nQI 175,7\nQM 172,169\nQM 173,169\nQM 174,169\nQM 175,169\nQO 4,172\n"
"QO 5,173\nQO 6,174\nQO 7,175\nQI 172,8\nQI 173,9\nQI 174,10\nQI 175,11\nQM 172,170\nQM 173,170\nQM 174,170\nQM 175,170\nQO 8,172\nQO 9,173\nQO 10,174\nQO 11,175\nQI 172,12\nQI 173,13\nQI 174,14\n"
"QI 175,15\nQM 172,171\nQM 173,171\nQM 174,171\nQM 175,171\nQO 12,172\nQO 13,173\nQO 14,174\nQO 15,175\nQI 172,16\nQI 173,17\nQI 174,18\nQI 175,19\nQM 172,168\nQM 173,168\nQM 174,168\nQM 175,168\n"
"QO 16,172\nQO 17,173\nQO 18,174\nQO 19,175\nQI 172,20\nQI 173,21\nQI 174,22\nQI 175,23\nQM 172,169\nQM 173,169\nQM 174,169\nQM 175,169\nQO 20,172\nQO 21,173\nQO 22,174\nQO 23,175\nQI 172,24\n"
"QI 173,25\nQI 174,26\nQI 175,27\nQM 172,170\nQM 173,170\nQM 174,170\nQM 175,170\nQO 24,172\nQO 25,173\nQO 26,174\nQO 27,175\nQI 172,28\nQI 173,29\nQI 174,30\nQI 175,31\nQM 172,171\nQM 173,171\n"
"QM 174,171\nQM 175,171\nQO 28,172\nQO 29,173\nQO 30,174\nQO 31,175\nQI 172,32\nQI 173,33\nQI 174,34\nQI 175,35\nQM 172,168\nQM 173,168\nQM 174,168\nQM 175,168\nQO 32,172\nQO 33,173\nQO 34,174\n"
"QO 35,175\nQI 172,36\nQI 173,37\nQI 174,38\nQI 175,39\nQM 172,169\nQM 173,169\nQM 174,169\nQM 175,169\nQO 36,172\nQO 37,173\nQO 38,174\nQO 39,175\nQI 172,40\nQI 173,41\nQI 174,42\nQI 175,43\n"
"QM 172,170\nQM 173,170\nQM 174,170\nQM 175,170\nQO 40,172\nQO 41,173\nQO 42,174\nQO 43,175\nQI 172,44\nQI 173,45\nQI 174,46\nQI 175,47\nQM 172,171\nQM 173,171\nQM 174,171\nQM 175,171\nQO 44,172\n"
"QO 45,173\nQO 46,174\nQO 47,175\nQI 172,48\nQI 173,49\nQI 174,50\nQI 175,51\nQM 172,168\nQM 173,168\nQM 174,168\nQM 175,168\nQO 48,172\nQO 49,173\nQO 50,174\nQO 51,175\nQI 172,52\nQI 173,53\n"
"QI 174,54\nQI 175,55\nQM 172,169\nQM 173,169\nQM 174,169\nQM 175,169\nQO 52,172\nQO 53,173\nQO 54,174\nQO 55,175\nQI 172,56\nQI 173,57\nQI 174,58\nQI 175,59\nQM 172,170\nQM 173,170\nQM 174,170\n"
"QM 175,170\nQO 56,172\nQO 57,173\nQO 58,174\nQO 59,175\nQI 172,60\nQI 173,61\nQI 174,62\nQI 175,63\nQM 172,171\nQM 173,171\nQM 174,171\nQM 175,171\nQO 60,172\nQO 61,173\nQO 62,174\nQO 63,175\n"
"QI 172,64\nQI 173,65\nQI 174,66\nQI 175,67\nQM 172,168\nQM 173,168\nQM 174,168\nQM 175,168\nQO 64,172\nQO 65,173\nQO 66,174\nQO 67,175\nQI 172,68\nQI 173,69\nQI 174,70\nQI 175,71\nQM 172,169\n"
"QM 173,169\nQM 174,169\nQM 175,169\nQO 68,172\nQO 69,173\nQO 70,174\nQO 71,175\nQI 172,72\nQI 173,73\nQI 174,74\nQI 175,75\nQM 172,170\nQM 173,170\nQM 174,170\nQM 175,170\nQO 72,172\nQO 73,173\n"
"QO 74,174\nQO 75,175\nQI 172,76\nQI 173,77\nQI 174,78\nQI 175,79\nQM 172,171\nQM 173,171\nQM 174,171\nQM 175,171\nQO 76,172\nQO 77,173\nQO 78,174\nQO 79,175\nQI 172,80\nQI 173,81\nQI 174,82\n"
"QI 175,83\nQM 172,168\nQM 173,168\nQM 174,168\nQM 175,168\nQO 80,172\nQO 81,173\nQO 82,174\nQO 83,175\nQI 172,84\nQI 173,85\nQI 174,86\nQI 175,87\nQM 172,169\nQM 173,169\nQM 174,169\nQM 175,169\n"
"QO 84,172\nQO 85,173\nQO 86,174\nQO 87,175\nQI 172,88\nQI 173,89\nQI 174,90\nQI 175,91\nQM 172,170\nQM 173,170\nQM 174,170\nQM 175,170\nQO 88,172\nQO 89,173\nQO 90,174\nQO 91,175\nQI 172,92\n"
"QI 173,93\nQI 174,94\nQI 175,95\nQM 172,171\nQM 173,171\nQM 174,171\nQM 175,171\nQO 92,172\nQO 93,173\nQO 94,174\nQO 95,175\nQI 172,96\nQI 173,97\nQI 174,98\nQI 175,99\nQM 172,168\nQM 173,168\n"
"QM 174,168\nQM 175,168\nQO 96,172\nQO 97,173\nQO 98,174\nQO 99,175\nQI 172,100\nQI 173,101\nQI 174,102\nQI 175,103\nQM 172,169\nQM 173,169\nQM 174,169\nQM 175,169\nQO 100,172\nQO 101,173\nQO 102,174\n"
"QO 103,175\nQI 172,104\nQI 173,105\nQI 174,106\nQI 175,107\nQM 172,170\nQM 173,170\nQM 174,170\nQM 175,170\nQO 104,172\nQO 105,173\nQO 106,174\nQO 107,175\nQI 172,108\nQI 173,109\nQI 174,110\n"
"QI 175,111\nQM 172,171\nQM 173,171\nQM 174,171\nQM 175,171\nQO 108,172\nQO 109,173\nQO 110,174\nQO 111,175\nQI 172,112\nQI 173,113\nQI 174,114\nQI 175,115\nQM 172,168\nQM 173,168\nQM 174,168\n"
"QM 175,168\nQO 112,172\nQO 113,173\nQO 114,174\nQO 115,175\nQI 172,116\nQI 173,117\nQI 174,118\nQI 175,119\nQM 172,169\nQM 173,169\nQM 174,169\nQM 175,169\nQO 116,172\nQO 117,173\nQO 118,174\n"
"QO 119,175\nQI 172,120\nQI 173,121\nQI 174,122\nQI 175,123\nQM 172,170\nQM 173,170\nQM 174,170\nQM 175,170\nQO 120,172\nQO 121,173\nQO 122,174\nQO 123,175\nQI 172,124\nQI 173,125\nQI 174,126\n"
"QI 175,127\nQM 172,171\nQM 173,171\nQM 174,171\nQM 175,171\nQO 124,172\nQO 125,173\nQO 126,174\nQO 127,175\nQP 164,0\nQP 165,1\nQP 166,2\nQP 167,3\nQP 168,16\nQP 169,17\nQP 170,18\nQP 171,19\n"
"QF 64,164,165\nQF 65,166,167\nQF 66,168,169\nQF 67,170,171\nQP 172,4\nQP 173,5\nQP 174,6\nQP 175,7\nQP 164,20\nQP 165,21\nQP 166,22\nQP 167,23\nQBO 128,160,64,144\nQF 68,172,173\nQF 69,174,175\n"
"QF 70,164,165\nQF 71,166,167\nQP 168,8\nQP 169,9\nQP 170,10\nQP 171,11\nQP 172,24\nQP 173,25\nQP 174,26\nQP 175,27\nQBO 132,160,68,148\nQF 72,168,169\nQF 73,170,171\nQF 74,172,173\nQF 75,174,175\n"
"QP 164,12\nQP 165,13\nQP 166,14\nQP 167,15\nQP 168,28\nQP 169,29\nQP 170,30\nQP 171,31\nQBO 136,160,72,152\nQF 76,164,165\nQF 77,166,167\nQF 78,168,169\nQF 79,170,171\nQBD \nQBO 140,160,76,156\nQDO \n"
"s_setpc_b64 s[74:75]\nL_rescale1:\nQDO \ns_nop 3\nQBY 164,32,33,34\nQBK 164,35,48\nQBK 164,49,50\nQFT 164,51\nQBY 165,36,37,38\nQBK 165,39,52\nQBK 165,53,54\nQFT 165,55\nQBY 166,40,41,42\n"
"QBK 166,43,56\nQBK 166,57,58\nQFT 166,59\nQBY 167,44,45,46\nQBK 167,47,60\nQBK 167,61,62\nQFT 167,63\nQN 172,164\nQN 173,165\nQN 174,166\nQN 175,167\nQBS 164,172\nQBS 165,173\nQBS 166,174\n"
"QBS 167,175\nQAZ 164,172\nQAZ 165,173\nQAZ 166,174\nQAZ 167,175\nQN 172,164\nQN 173,165\nQN 174,166\nQN 175,167\nQBN 164,172\nQBN 165,173\nQBN 166,174\nQBN 167,175\nQAZ 164,172\nQAZ 165,173\n"
"QAZ 166,174\nQAZ 167,175\nQT 176,164\nQT 32,164\nQT 33,164\nQT 34,164\nQT 35,164\nQT 48,164\nQT 49,164\nQT 50,164\nQT 51,164\nQT 0,164\nQT 1,164\nQT 2,164\nQT 3,164\nQT 16,164\nQT 17,164\nQT 18,164\n"
"QT 19,164\nQT 96,164\nQT 97,164\nQT 98,164\nQT 99,164\nQT 112,164\nQT 113,164\nQT 114,164\nQT 115,164\nQT 177,165\nQT 36,165\nQT 37,165\nQT 38,165\nQT 39,165\nQT 52,165\nQT 53,165\nQT 54,165\n"
"QT 55,165\nQT 4,165\nQT 5,165\nQT 6,165\nQT 7,165\nQT 20,165\nQT 21,165\nQT 22,165\nQT 23,165\nQT 100,165\nQT 101,165\nQT 102,165\nQT 103,165\nQT 116,165\nQT 117,165\nQT 118,165\nQT 119,165\n"
"QT 178,166\nQT 40,166\nQT 41,166\nQT 42,166\nQT 43,166\nQT 56,166\nQT 57,166\nQT 58,166\nQT 59,166\nQT 8,166\nQT 9,166\nQT 10,166\nQT 11,166\nQT 24,166\nQT 25,166\nQT 26,166\nQT 27,166\nQT 104,166\n"
"QT 105,166\nQT 106,166\nQT 107,166\nQT 120,166\nQT 121,166\nQT 122,166\nQT 123,166\nQT 179,167\nQT 44,167\nQT 45,167\nQT 46,167\nQT 47,167\nQT 60,167\nQT 61,167\nQT 62,167\nQT 63,167\nQT 12,167\n"
"QT 13,167\nQT 14,167\nQT 15,167\nQT 28,167\nQT 29,167\nQT 30,167\nQT 31,167\nQT 108,167\nQT 109,167\nQT 110,167\nQT 111,167\nQT 124,167\nQT 125,167\nQT 126,167\nQT 127,167\nQFU 168,164\nQFU 169,165\n"
"QFU 170,166\nQFU 171,167\nQM 128,168\nQM 129,168\nQM 130,168\nQM 131,168\nQM 132,169\nQM 133,169\nQM 134,169\nQM 135,169\nQM 136,170\nQM 137,170\nQM 138,170\nQM 139,170\nQM 140,171\nQM 141,171\n"
"QM 142,171\nQM 143,171\nQI 172,0\nQI 173,1\nQI 174,2\nQI 175,3\nQM 172,168\nQM 173,168\nQM 174,168\nQM 175,168\nQO 0,172\nQO 1,173\nQO 2,174\nQO 3,175\nQI 172,4\nQI 173,5\nQI 174,6\nQI 175,7\n"
"QM 172,169\nQM 173,169\nQM 174,169\nQM 175,169\nQO 4,172\nQO 5,173\nQO 6,174\nQO 7,175\nQI 172,8\nQI 173,9\nQI 174,10\nQI 175,11\nQM 172,170\nQM 173,170\nQM 174,170\nQM 175,170\nQO 8,172\nQO 9,173\n"
"QO 10,174\nQO 11,175\nQI 172,12\nQI 173,13\nQI 174,14\nQI 175,15\nQM 172,171\nQM 173,171\nQM 174,171\nQM 175,171\nQO 12,172\nQO 13,173\nQO 14,174\nQO 15,175\nQI 172,16\nQI 173,17\nQI 174,18\n"
"QI 175,19\nQM 172,168\nQM 173,168\nQM 174,168\nQM 175,168\nQO 16,172\nQO 17,173\nQO 18,174\nQO 19,175\nQI 172,20\nQI 173,21\nQI 174,22\nQI 175,23\nQM 172,169\nQM 173,169\nQM 174,169\nQM 175,169\n"
"QO 20,172\nQO 21,173\nQO 22,174\nQO 23,175\nQI 172,24\nQI 173,25\nQI 174,26\nQI 175,27\nQM 172,170\nQM 173,170\nQM 174,170\nQM 175,170\nQO 24,172\nQO 25,173\nQO 26,174\nQO 27,175\nQI 172,28\n"
"QI 173,29\nQI 174,30\nQI 175,31\nQM 172,171\nQM 173,171\nQM 174,171\nQM 175,171\nQO 28,172\nQO 29,173\nQO 30,174\nQO 31,175\nQI 172,32\nQI 173,33\nQI 174,34\nQI 175,35\nQM 172,168\nQM 173,168\n"
"QM 174,168\nQM 175,168\nQO 32,172\nQO 33,173\nQO 34,174\nQO 35,175\nQI 172,36\nQI 173,37\nQI 174,38\nQI 175,39\nQM 172,169\nQM 173,169\nQM 174,169\nQM 175,169\nQO 36,172\nQO 37,173\nQO 38,174\n"
"QO 39,175\nQI 172,40\nQI 173,41\nQI 174,42\nQI 175,43\nQM 172,170\nQM 173,170\nQM 174,170\nQM 175,170\nQO 40,172\nQO 41,173\nQO 42,174\nQO 43,175\nQI 172,44\nQI 173,45\nQI 174,46\nQI 175,47\n"
"QM 172,171\nQM 173,171\nQM 174,171\nQM 175,171\nQO 44,172\nQO 45,173\nQO 46,174\nQO 47,175\nQI 172,48\nQI 173,49\nQI 174,50\nQI 175,51\nQM 172,168\nQM 173,168\nQM 174,168\nQM 175,168\nQO 48,172\n"
"QO 49,173\nQO 50,174\nQO 51,175\nQI 172,52\nQI 173,53\nQI 174,54\nQI 175,55\nQM 172,169\nQM 173,169\nQM 174,169\nQM 175,169\nQO 52,172\nQO 53,173\nQO 54,174\nQO 55,175\nQI 172,56\nQI 173,57\n"
"QI 174,58\nQI 175,59\nQM 172,170\nQM 173,170\nQM 174,170\nQM 175,170\nQO 56,172\nQO 57,173\nQO 58,174\nQO 59,175\nQI 172,60\nQI 173,61\nQI 174,62\nQI 175,63\nQM 172,171\nQM 173,171\nQM 174,171\n"
"QM 175,171\nQO 60,172\nQO 61,173\nQO 62,174\nQO 63,175\nQI 172,64\nQI 173,65\nQI 174,66\nQI 175,67\nQM 172,168\nQM 173,168\nQM 174,168\nQM 175,168\nQO 64,172\nQO 65,173\nQO 66,174\nQO 67,175\n"
"QI 172,68\nQI 173,69\nQI 174,70\nQI 175,71\nQM 172,169\nQM 173,169\nQM 174,169\nQM 175,169\nQO 68,172\nQO 69,173\nQO 70,174\nQO 71,175\nQI 172,72\nQI 173,73\nQI 174,74\nQI 175,75\nQM 172,170\n"
"QM 173,170\nQM 174,170\nQM 175,170\nQO 72,172\nQO 73,173\nQO 74,174\nQO 75,175\nQI 172,76\nQI 173,77\nQI 174,78\nQI 175,79\nQM 172,171\nQM 173,171\nQM 174,171\nQM 175,171\nQO 76,172\nQO 77,173\n"
"QO 78,174\nQO 79,175\nQI 172,80\nQI 173,81\nQI 174,82\nQI 175,83\nQM 172,168\nQM 173,168\nQM 174,168\nQM 175,168\nQO 80,172\nQO 81,173\nQO 82,174\nQO 83,175\nQI 172,84\nQI 173,85\nQI 174,86\n"
"QI 175,87\nQM 172,169\nQM 173,169\nQM 174,169\nQM 175,169\nQO 84,172\nQO 85,173\nQO 86,174\nQO 87,175\nQI 172,88\nQI 173,89\nQI 174,90\nQI 175,91\nQM 172,170\nQM 173,170\nQM 174,170\nQM 175,170\n"
"QO 88,172\nQO 89,173\nQO 90,174\nQO 91,175\nQI 172,92\nQI 173,93\nQI 174,94\nQI 175,95\nQM 172,171\nQM 173,171\nQM 174,171\nQM 175,171\nQO 92,172\nQO 93,173\nQO 94,174\nQO 95,175\nQI 172,96\n"
"QI 173,97\nQI 174,98\nQI 175,99\nQM 172,168\nQM 173,168\nQM 174,168\nQM 175,168\nQO 96,172\nQO 97,173\nQO 98,174\nQO 99,175\nQI 172,100\nQI 173,101\nQI 174,102\nQI 175,103\nQM 172,169\nQM 173,169\n"
"QM 174,169\nQM 175,169\nQO 100,172\nQO 101,173\nQO 102,174\nQO 103,175\nQI 172,104\nQI 173,105\nQI 174,106\nQI 175,107\nQM 172,170\nQM 173,170\nQM 174,170\nQM 175,170\nQO 104,172\nQO 105,173\n"
"QO 106,174\nQO 107,175\nQI 172,108\nQI 173,109\nQI 174,110\nQI 175,111\nQM 172,171\nQM 173,171\nQM 174,171\nQM 175,171\nQO 108,172\nQO 109,173\nQO 110,174\nQO 111,175\nQI 172,112\nQI 173,113\n"
"QI 174,114\nQI 175,115\nQM 172,168\nQM 173,168\nQM 174,168\nQM 175,168\nQO 112,172\nQO 113,173\nQO 114,174\nQO 115,175\nQI 172,116\nQI 173,117\nQI 174,118\nQI 175,119\nQM 172,169\nQM 173,169\n"
"QM 174,169\nQM 175,169\nQO 116,172\nQO 117,173\nQO 118,174\nQO 119,175\nQI 172,120\nQI 173,121\nQI 174,122\nQI 175,123\nQM 172,170\nQM 173,170\nQM 174,170\nQM 175,170\nQO 120,172\nQO 121,173\n"
"QO 122,174\nQO 123,175\nQI 172,124\nQI 173,125\nQI 174,126\nQI 175,127\nQM 172,171\nQM 173,171\nQM 174,171\nQM 175,171\nQO 124,172\nQO 125,173\nQO 126,174\nQO 127,175\nQP 164,32\nQP 165,33\n"
"QP 166,34\nQP 167,35\nQP 168,48\nQP 169,49\nQP 170,50\nQP 171,51\nQF 80,164,165\nQF 81,166,167\nQF 82,168,169\nQF 83,170,171\nQP 172,36\nQP 173,37\nQP 174,38\nQP 175,39\nQP 164,52\nQP 165,53\n"
"QP 166,54\nQP 167,55\nQBO 144,160,80,128\nQF 84,172,173\nQF 85,174,175\nQF 86,164,165\nQF 87,166,167\nQP 168,40\nQP 169,41\nQP 170,42\nQP 171,43\nQP 172,56\nQP 173,57\nQP 174,58\nQP 175,59\n"
"QBO 148,160,84,132\nQF 88,168,169\nQF 89,170,171\nQF 90,172,173\nQF 91,174,175\nQP 164,44\nQP 165,45\nQP 166,46\nQP 167,47\nQP 168,60\nQP 169,61\nQP 170,62\nQP 171,63\nQBO 152,160,88,136\n"
"QF 92,164,165\nQF 93,166,167\nQF 94,168,169\nQF 95,170,171\nQBD \nQBO 156,160,92,140\nQDO \ns_setpc_b64 s[74:75]\nL_drain:\nQCG 84\nL_pc21:\ns_lshr_b32 s86, %1, 3\nQBH 86\ns_add_u32 s86, s86, %0\n"
"s_mul_i32 s86, s86, ((L_done-L_attn_end-1024)/127+63)&-64\ns_min_u32 s86, s86, L_done-L_attn_end-1024\ns_add_u32 s86, s86, L_attn_end-L_pc21\nQZ 84,86\nQAX 85\nQCL 198,185\nQFG 196,84,198\nQP 164,32\n"
"QP 165,33\nQP 166,34\nQP 167,35\nQG 0\nQR 0,224,64\nQB 128,181,8192\nQF 80,164,165\nQR 4,224,68\ns_and_b32 s83, %1, 1\nQP 168,48\nQR 8,224,72\nQB 132,181,9216\nQF 81,166,167\nQR 12,224,76\nQCK 83\n"
"QP 169,49\nQR 16,228,64\ns_bfe_u32 s82, %1, 196611\nQP 170,50\nQR 20,228,68\nQB 136,181,10240\nQF 82,168,169\nQR 24,228,72\nQZ 82,83\nQP 171,51\nQR 28,228,76\nQB 140,181,11264\nQF 83,170,171\n"
"QR 32,232,64\ns_bfe_u32 s83, %1, 131073\nQP 172,36\nQBO 144,160,80,128\nQBH 83\nQP 173,37\nQR 36,232,68\ns_lshr_b32 s84, %1, 6\nQP 174,38\nQR 40,232,72\nQB 144,181,12288\nQF 84,172,173\nQR 44,232,76\n"
"QZ 83,84\nQP 175,39\nQR 48,236,64\nQEG 81,83\nQP 164,52\nQR 52,236,68\nQB 148,181,13312\nQF 85,174,175\nQR 56,236,72\ns_add_u32 s81, s81, %0\nQP 165,53\nQR 60,236,76\ns_lshl_b32 s81, s81, 1\n"
"QP 166,54\nQR 64,240,64\nQB 152,181,14336\nQF 86,164,165\nQR 68,240,68\ns_mov_b64 s[56:57], %2\nQP 167,55\nQR 72,240,72\nQB 156,181,15360\nQF 87,166,167\nQR 76,240,76\ns_add_u32 s56, s56, 69795840\n"
"QAX 57\nQP 168,40\nQBO 148,160,84,132\ns_mov_b32 s58, 2293760\nQP 169,41\nQR 80,244,64\nQBA 59\nQP 170,42\nQR 84,244,68\ns_mov_b64 s[60:61], %2\nQP 171,43\nQR 88,244,72\ns_add_u32 s60, s60, 58195968\n"
"QAX 61\nQF 88,168,169\nQF 89,170,171\nQR 92,244,76\nQDL 62\nQP 172,56\nQR 96,248,64\nQBA 63\nQP 173,57\nQR 100,248,68\ns_mov_b64 s[64:65], %2\nQF 90,172,173\nQR 104,248,72\n"
"s_add_u32 s64, s64, 79626240\nQAX 65\nQP 174,58\nQR 108,248,76\ns_mov_b32 s66, 131072\nQP 175,59\nQR 112,252,64\nQBA 67\nQF 91,174,175\nQR 116,252,68\ns_lshl_b32 s69, %0, 12\nQP 164,44\n"
"QR 120,252,72\ns_mul_i32 s68, s82, 143360\nQP 165,45\nQBO 152,160,88,136\ns_add_u32 s68, s68, 71680\nQP 166,46\nQR 124,252,76\nQGE 70,81\nQP 167,47\ns_add_u32 s71, s70, 20480\ns_lshl_b32 s87, %0, 10\n"
"QCJ 83,82\ns_mul_i32 s83, s83, 7168\nQBX 84,82\ns_mul_i32 s72, s84, 3072\nQZ 72,83\nQCK 84\nQBT 73,83,84\nQHG 73\ns_mul_i32 s83, s82, 3072\nQHG 83\ns_cmp_lt_u32 s82, 12\ns_cselect_b32 s72, s72, s83\n"
"s_cselect_b32 s73, s73, 55296\ns_mul_i32 s83, s81, 57344\nQZ 72,83\nQZ 73,83\nQP 168,60\nQP 169,61\nQF 92,164,165\nQF 93,166,167\nQP 170,62\nQP 171,63\nQF 94,168,169\nQF 95,170,171\nQBD \n"
"QBO 156,160,92,140\nQBY 183,144,148,152\ns_nop 6\nQAZ 183,156\nv_cmp_nge_f32 vcc, 0x53800000, v183\nQG 0\nQBD \ns_cbranch_vccz L_dr_ok\ns_call_b64 s[74:75], L_rescale1\nL_dr_ok:\nL_combine:\n"
"QN 168,144\nQN 169,148\nQN 170,152\nQN 171,156\nQFZ 83,80\ns_add_u32 s83, s83, 114688\nQBM 187,83,185\nds_write_b128 v187, v[176:179]\nQCH 168,187,1024\nv_xor_b32 v187, 6144, v187\nQAM 0,0\n"
"s_barrier\nds_read_b128 v[164:167], v187\nQH 172,187,1024\nQBZ 80\ns_cselect_b32 s96, 0, 1048576\nQCR 196\ns_bfe_u32 s99, %1, 262144\nQBU 99\ns_add_u32 s99, s99, 24576\nQZ 99,96\nQEI \nQEJ 92,196,99\n"
"QEK \ns_cmp_ge_u32 s80, 2\ns_cbranch_scc1 L_comb_hi\ns_mov_b32 s83, 98304\ns_mov_b32 s84, 131072\nQBZ 80\ns_cselect_b32 s83, 32768, s83\ns_cselect_b32 s84, 147456, s84\nQBM 187,83,185\n"
"QBM 188,84,185\nQR 8,128,88\nQJ 192,60,185,70,0\nQR 24,132,88\nQJ 196,60,185,71,0\nQR 40,136,88\nQJ 200,60,185,70,1024\nQR 56,140,88\nQJ 204,60,185,71,1024\nQR 72,144,88\nQJ 208,60,185,70,2048\n"
"QR 88,148,88\nQJ 212,60,185,71,2048\nQR 104,152,88\nQJ 216,60,185,70,3072\nQR 120,156,88\nQJ 220,60,185,71,3072\nQR 12,128,92\nQAA 70\nQR 28,132,92\nQJ 160,60,185,70,0\nQR 44,136,92\nQAA 71\n"
"QR 60,140,92\nQJ 164,60,185,71,0\nQR 76,144,92\nQJ 168,60,185,70,1024\nQR 92,148,92\nQJ 172,60,185,71,1024\nQR 108,152,92\nQJ 176,60,185,70,2048\nQR 124,156,92\nQJ 180,60,185,71,2048\n"
"QJ 184,60,185,70,3072\nQJ 188,60,185,71,3072\nQG 0\nQR 0,128,80\nQBL 8,187,0\nv_min_f32 v129, v176, v164\nQR 16,132,80\nQBL 24,187,1024\nv_min_f32 v133, v177, v165\nQR 32,136,80\nQBL 40,187,2048\n"
"QFV 128,129,176\nQR 48,140,80\nQBL 56,187,3072\nQFV 132,133,177\nQR 64,144,80\nQBL 72,187,4096\nQFV 130,129,164\nQR 80,148,80\nQBL 88,187,5120\nQFV 134,133,165\nQR 96,152,80\nQBL 104,187,6144\n"
"QR 112,156,80\nQBL 120,187,7168\nQR 4,128,84\nQBL 12,187,8192\nQR 20,132,84\nQBL 28,187,9216\nQR 36,136,84\nQBL 44,187,10240\nQR 52,140,84\nQBL 60,187,11264\nQR 68,144,84\nQBL 76,187,12288\n"
"QR 84,148,84\nQBL 92,187,13312\nQR 100,152,84\nQBL 108,187,14336\nQR 116,156,84\nQBL 124,187,15360\nQW 128\nQW 132\nQW 130\nQW 134\nv_mul_f32 v129, v128, v168\nv_mul_f32 v133, v132, v169\n"
"v_fmac_f32 v129, v130, v172\nv_fmac_f32 v133, v134, v173\nQV 129\nQV 133\nQM 128,129\nQM 132,133\nQM 130,129\nQM 134,133\nQBM 170,69,185\nQEU 167,185\nQBM 164,87,167\nv_add_u32 v168, 65536, v185\n"
"QBM 165,87,168\nQGL 169,185\nQBM 166,87,169\nQCN 160\nQCN 161\nQCN 162\nQCN 163\nQCV 220,185\nQEV 220\nv_lshrrev_b32 v221, 8, v185\nv_lshl_add_u32 v220, v221, 15, v220\nv_add_u32 v221, 16384, v220\n"
"s_lshl_b32 s83, s81, 7\nQFW 172,64,220,83,0\nQFW 174,64,221,83,0\nQFW 176,64,220,83,128\nQFW 178,64,221,83,128\nQG 0\ns_barrier\nQH 64,188,0\nQH 68,188,1024\nQH 72,188,2048\nQH 76,188,3072\n"
"QH 80,188,4096\nQH 84,188,5120\nQH 88,188,6144\nQH 92,188,7168\nQH 96,188,8192\nQH 100,188,9216\nQH 104,188,10240\nQH 108,188,11264\nQH 112,188,12288\nQH 116,188,13312\nQH 120,188,14336\n"
"QH 124,188,15360\ns_add_u32 s91, s91, 0\nQI 0,0\nQI 1,1\nQI 2,2\nQI 3,3\nQI 4,16\nQI 5,17\nQI 6,18\nQI 7,19\nQAC 0,128\nQAC 2,128\nQAC 4,128\nQAC 6,128\nQG 14\nQAF 0,64,130\nQAF 2,66,130\n"
"QAF 4,68,130\nQAF 6,70,130\nQAY 0,1\nQF 1,2,3\nQF 2,4,5\nQF 3,6,7\nQAB 0,44,185,91,0\nQI 8,32\nQI 9,33\nQI 10,34\nQI 11,35\nQI 12,48\nQI 13,49\nQI 14,50\nQI 15,51\nQAC 8,128\nQAC 10,128\nQAC 12,128\n"
"QAC 14,128\nQG 12\nQAF 8,72,130\nQAF 10,74,130\nQAF 12,76,130\nQAF 14,78,130\nQAY 8,9\nQF 9,10,11\nQF 10,12,13\nQF 11,14,15\nQAB 8,44,185,91,1024\nQI 16,64\nQI 17,65\nQI 18,66\nQI 19,67\nQI 20,80\n"
"QI 21,81\nQI 22,82\nQI 23,83\nQAC 16,128\nQAC 18,128\nQAC 20,128\nQAC 22,128\nQG 10\nQAF 16,80,130\nQAF 18,82,130\nQAF 20,84,130\nQAF 22,86,130\nQAY 16,17\nQF 17,18,19\nQF 18,20,21\nQF 19,22,23\n"
"QAB 16,44,185,91,2048\nQI 24,96\nQI 25,97\nQI 26,98\nQI 27,99\nQI 28,112\nQI 29,113\nQI 30,114\nQI 31,115\nQAC 24,128\nQAC 26,128\nQAC 28,128\nQAC 30,128\nQG 8\nQAF 24,88,130\nQAF 26,90,130\n"
"QAF 28,92,130\nQAF 30,94,130\nQAY 24,25\nQF 25,26,27\nQF 26,28,29\nQF 27,30,31\nQAB 24,44,185,91,3072\nQEC 91\nQI 32,4\nQI 33,5\nQI 34,6\nQI 35,7\nQI 36,20\nQI 37,21\nQI 38,22\nQI 39,23\nQAC 32,132\n"
"QAC 34,132\nQAC 36,132\nQAC 38,132\nQG 6\nQAF 32,96,134\nQAF 34,98,134\nQAF 36,100,134\nQAF 38,102,134\nQAY 32,33\nQF 33,34,35\nQF 34,36,37\nQF 35,38,39\nQAB 32,44,185,91,0\nQI 40,36\nQI 41,37\n"
"QI 42,38\nQI 43,39\nQI 44,52\nQI 45,53\nQI 46,54\nQI 47,55\nQAC 40,132\nQAC 42,132\nQAC 44,132\nQAC 46,132\nQG 4\nQAF 40,104,134\nQAF 42,106,134\nQAF 44,108,134\nQAF 46,110,134\nQAY 40,41\n"
"QF 41,42,43\nQF 42,44,45\nQF 43,46,47\nQAB 40,44,185,91,1024\nQI 48,68\nQI 49,69\nQI 50,70\nQI 51,71\nQI 52,84\nQI 53,85\nQI 54,86\nQI 55,87\nQAC 48,132\nQAC 50,132\nQAC 52,132\nQAC 54,132\nQG 2\n"
"QAF 48,112,134\nQAF 50,114,134\nQAF 52,116,134\nQAF 54,118,134\nQAY 48,49\nQF 49,50,51\nQF 50,52,53\nQF 51,54,55\nQAB 48,44,185,91,2048\nQI 56,100\nQI 57,101\nQI 58,102\nQI 59,103\nQI 60,116\n"
"QI 61,117\nQI 62,118\nQI 63,119\nQAC 56,132\nQAC 58,132\nQAC 60,132\nQAC 62,132\nQG 0\nQAF 56,120,134\nQAF 58,122,134\nQAF 60,124,134\nQAF 62,126,134\nQAY 56,57\nQF 57,58,59\nQF 58,60,61\n"
"QF 59,62,63\nQAB 56,44,185,91,3072\ns_branch L_attn_end\nL_comb_hi:\ns_mov_b32 s83, 147456\nQGK 84\ns_cmp_eq_u32 s80, 2\ns_cselect_b32 s83, 131072, s83\ns_cselect_b32 s84, 98304, s84\nQBM 187,83,185\n"
"QBM 188,84,185\nQR 0,128,80\nQJ 192,60,185,70,0\nQR 16,132,80\nQJ 196,60,185,71,0\nQR 32,136,80\nQJ 200,60,185,70,1024\nQR 48,140,80\nQJ 204,60,185,71,1024\nQR 64,144,80\nQJ 208,60,185,70,2048\n"
"QR 80,148,80\nQJ 212,60,185,71,2048\nQR 96,152,80\nQJ 216,60,185,70,3072\nQR 112,156,80\nQJ 220,60,185,71,3072\nQR 4,128,84\nQAA 70\nQR 20,132,84\nQJ 160,60,185,70,0\nQR 36,136,84\nQAA 71\n"
"QR 52,140,84\nQJ 164,60,185,71,0\nQR 68,144,84\nQJ 168,60,185,70,1024\nQR 84,148,84\nQJ 172,60,185,71,1024\nQR 100,152,84\nQJ 176,60,185,70,2048\nQR 116,156,84\nQJ 180,60,185,71,2048\n"
"QJ 184,60,185,70,3072\nQJ 188,60,185,71,3072\nQG 0\nQR 8,128,88\nQBL 0,187,0\nv_min_f32 v137, v178, v166\nQR 24,132,88\nQBL 16,187,1024\nv_min_f32 v141, v179, v167\nQR 40,136,88\nQBL 32,187,2048\n"
"QFV 136,137,178\nQR 56,140,88\nQBL 48,187,3072\nQFV 140,141,179\nQR 72,144,88\nQBL 64,187,4096\nQFV 138,137,166\nQR 88,148,88\nQBL 80,187,5120\nQFV 142,141,167\nQR 104,152,88\nQBL 96,187,6144\n"
"QR 120,156,88\nQBL 112,187,7168\nQR 12,128,92\nQBL 4,187,8192\nQR 28,132,92\nQBL 20,187,9216\nQR 44,136,92\nQBL 36,187,10240\nQR 60,140,92\nQBL 52,187,11264\nQR 76,144,92\nQBL 68,187,12288\n"
"QR 92,148,92\nQBL 84,187,13312\nQR 108,152,92\nQBL 100,187,14336\nQR 124,156,92\nQBL 116,187,15360\nQW 136\nQW 140\nQW 138\nQW 142\nv_mul_f32 v137, v136, v170\nv_mul_f32 v141, v140, v171\n"
"v_fmac_f32 v137, v138, v174\nv_fmac_f32 v141, v142, v175\nQV 137\nQV 141\nQM 136,137\nQM 140,141\nQM 138,137\nQM 142,141\nQBM 170,69,185\nQEU 167,185\nQBM 164,87,167\nv_add_u32 v168, 65536, v185\n"
"QBM 165,87,168\nQGL 169,185\nQBM 166,87,169\nQCN 160\nQCN 161\nQCN 162\nQCN 163\nQCV 220,185\nQEV 220\nv_lshrrev_b32 v221, 8, v185\nv_lshl_add_u32 v220, v221, 15, v220\nv_add_u32 v221, 16384, v220\n"
"s_lshl_b32 s83, s81, 7\nQFW 172,64,220,83,0\nQFW 174,64,221,83,0\nQFW 176,64,220,83,128\nQFW 178,64,221,83,128\nQG 0\ns_barrier\nQH 64,188,0\nQH 68,188,1024\nQH 72,188,2048\nQH 76,188,3072\n"
"QH 80,188,4096\nQH 84,188,5120\nQH 88,188,6144\nQH 92,188,7168\nQH 96,188,8192\nQH 100,188,9216\nQH 104,188,10240\nQH 108,188,11264\nQH 112,188,12288\nQH 116,188,13312\nQH 120,188,14336\n"
"QH 124,188,15360\nQAO 91\nQI 0,8\nQI 1,9\nQI 2,10\nQI 3,11\nQI 4,24\nQI 5,25\nQI 6,26\nQI 7,27\nQAC 0,136\nQAC 2,136\nQAC 4,136\nQAC 6,136\nQG 14\nQAF 0,64,138\nQAF 2,66,138\nQAF 4,68,138\n"
"QAF 6,70,138\nQAY 0,1\nQF 1,2,3\nQF 2,4,5\nQF 3,6,7\nQAB 0,44,185,91,0\nQI 8,40\nQI 9,41\nQI 10,42\nQI 11,43\nQI 12,56\nQI 13,57\nQI 14,58\nQI 15,59\nQAC 8,136\nQAC 10,136\nQAC 12,136\nQAC 14,136\n"
"QG 12\nQAF 8,72,138\nQAF 10,74,138\nQAF 12,76,138\nQAF 14,78,138\nQAY 8,9\nQF 9,10,11\nQF 10,12,13\nQF 11,14,15\nQAB 8,44,185,91,1024\nQI 16,72\nQI 17,73\nQI 18,74\nQI 19,75\nQI 20,88\nQI 21,89\n"
"QI 22,90\nQI 23,91\nQAC 16,136\nQAC 18,136\nQAC 20,136\nQAC 22,136\nQG 10\nQAF 16,80,138\nQAF 18,82,138\nQAF 20,84,138\nQAF 22,86,138\nQAY 16,17\nQF 17,18,19\nQF 18,20,21\nQF 19,22,23\n"
"QAB 16,44,185,91,2048\nQI 24,104\nQI 25,105\nQI 26,106\nQI 27,107\nQI 28,120\nQI 29,121\nQI 30,122\nQI 31,123\nQAC 24,136\nQAC 26,136\nQAC 28,136\nQAC 30,136\nQG 8\nQAF 24,88,138\nQAF 26,90,138\n"
"QAF 28,92,138\nQAF 30,94,138\nQAY 24,25\nQF 25,26,27\nQF 26,28,29\nQF 27,30,31\nQAB 24,44,185,91,3072\nQEC 91\nQI 32,12\nQI 33,13\nQI 34,14\nQI 35,15\nQI 36,28\nQI 37,29\nQI 38,30\nQI 39,31\n"
"QAC 32,140\nQAC 34,140\nQAC 36,140\nQAC 38,140\nQG 6\nQAF 32,96,142\nQAF 34,98,142\nQAF 36,100,142\nQAF 38,102,142\nQAY 32,33\nQF 33,34,35\nQF 34,36,37\nQF 35,38,39\nQAB 32,44,185,91,0\nQI 40,44\n"
"QI 41,45\nQI 42,46\nQI 43,47\nQI 44,60\nQI 45,61\nQI 46,62\nQI 47,63\nQAC 40,140\nQAC 42,140\nQAC 44,140\nQAC 46,140\nQG 4\nQAF 40,104,142\nQAF 42,106,142\nQAF 44,108,142\nQAF 46,110,142\nQAY 40,41\n"
"QF 41,42,43\nQF 42,44,45\nQF 43,46,47\nQAB 40,44,185,91,1024\nQI 48,76\nQI 49,77\nQI 50,78\nQI 51,79\nQI 52,92\nQI 53,93\nQI 54,94\nQI 55,95\nQAC 48,140\nQAC 50,140\nQAC 52,140\nQAC 54,140\nQG 2\n"
"QAF 48,112,142\nQAF 50,114,142\nQAF 52,116,142\nQAF 54,118,142\nQAY 48,49\nQF 49,50,51\nQF 50,52,53\nQF 51,54,55\nQAB 48,44,185,91,2048\nQI 56,108\nQI 57,109\nQI 58,110\nQI 59,111\nQI 60,124\n"
"QI 61,125\nQI 62,126\nQI 63,127\nQAC 56,140\nQAC 58,140\nQAC 60,140\nQAC 62,140\nQG 0\nQAF 56,120,142\nQAF 58,122,142\nQAF 60,124,142\nQAF 62,126,142\nQAY 56,57\nQF 57,58,59\nQF 58,60,61\n"
"QF 59,62,63\nQAB 56,44,185,91,3072\nL_attn_end:\nQDO \nQAH 20\ns_barrier\nQB 100,167,16384\nQB 104,167,17408\nQB 108,167,18432\nQB 112,167,19456\nQB 116,167,20480\nQB 120,167,21504\nQG 4\n"
"QAE 96,100,192\nQB 124,167,22528\nQDF 69\nQAE 124,100,196\nQL 56,170,68,0\nQAE 100,104,192\nQB 100,164,16384\nQAH 9\nQAE 128,104,196\nQG 4\nQEW 222,172,174\nQN 223,222\nQAE 104,108,192\n"
"QB 104,164,20480\nQBS 222,223\nQAE 132,108,196\nQL 56,170,68,1024\nQAE 108,112,192\nQB 108,167,23552\nQAJ 222,223\nQAE 136,112,196\nQG 4\nQN 223,222\nQBD \nQBN 222,223\nQAE 112,116,192\n"
"QB 112,167,24576\nQAJ 222,223\nQAE 140,116,196\nQL 56,170,68,2048\nQAE 116,120,192\nQB 116,167,25600\nv_mul_f32 v222, 0x3e000000, v222\nQAE 144,120,196\nQG 4\nQT 172,222\nQT 174,222\nQAE 120,124,192\n"
"QB 120,167,26624\nv_mul_f32 v172, v172, v172\nQAE 148,124,196\nQL 56,170,68,3072\nQAN 152,100,160\nQB 124,167,27648\nQG 4\nQAN 156,104,160\nQB 100,167,28672\nQEP 68\nQA 96,108,200\nQB 104,167,29696\n"
"QAA 70\nQA 124,108,204\nQG 4\nv_fmac_f32 v172, v174, v174\nQAJ 173,175\nQA 100,112,200\nQB 108,164,23552\nv_fmac_f32 v173, 0x42a00000, v172\nQA 128,112,204\nQJ 128,60,185,70,0\nQA 104,116,200\n"
"QB 112,164,27648\nQAA 71\nQA 132,116,204\nQG 4\nQN 223,173\nQBD \nQBS 173,223\nQA 108,120,200\nQB 116,168,16384\nQAJ 173,223\nQA 136,120,204\nQJ 132,60,185,71,0\nQA 112,124,200\nQB 120,168,17408\n"
"QN 223,173\nQA 140,124,204\nQG 4\nQBN 173,223\nQAJ 173,223\nQA 116,100,200\nQB 124,168,18432\nv_mul_f32 v173, 0x3acccccd, v173\nQA 144,100,204\nQJ 136,60,185,70,1024\nQA 120,104,200\n"
"QB 100,168,19456\nQCM 173\nQA 148,104,204\nQG 4\nQBG 173\nv_add_f32 v173, 0x3727c5ac, v173\nQC 152,108,160\nQB 104,168,20480\nQCM 173\nQC 156,112,160\nQB 108,168,21504\nQG 4\nQA 96,116,208\n"
"QB 112,168,22528\nQBG 173\nQA 124,116,212\nQJ 140,60,185,71,1024\nQA 100,120,208\nQB 116,165,16384\nQCM 222\nQA 128,120,212\nQG 4\nv_sqrt_f32 v173, v173\nQA 104,124,208\nQB 120,165,20480\nQBG 222\n"
"QA 132,124,212\nQJ 144,60,185,70,2048\nQA 108,100,208\nQB 124,168,23552\nQCM 173\nQA 136,100,212\nQG 4\nQBG 173\nQEF 172,222\nQA 112,104,208\nQB 100,168,24576\nQEW 222,176,178\nQA 140,104,212\n"
"QJ 148,60,185,71,2048\nQA 116,108,208\nQB 104,168,25600\nQN 223,222\nQA 144,108,212\nQG 4\nQV 173\nQA 120,112,208\nQB 108,168,26624\nQBS 222,223\nQA 148,112,212\nQJ 152,60,185,70,3072\n"
"QC 152,116,160\nQB 112,168,27648\nQG 4\nQC 156,120,160\nQB 116,168,28672\nQAJ 222,223\nQA 96,124,216\nQB 120,168,29696\nQN 223,222\nQA 124,124,220\nQG 4\nQBN 222,223\nQAJ 222,223\nQA 100,100,216\n"
"QB 124,165,23552\nv_mul_f32 v222, 0x3e000000, v222\nQA 128,100,220\nQJ 156,60,185,71,3072\nQA 104,104,216\nQB 100,165,27648\nQT 176,222\nQA 132,104,220\nQG 4\nQT 178,222\nv_mul_f32 v176, v176, v176\n"
"QA 108,108,216\nQAH 20\ns_barrier\nQCV 85,185\nQCW 85\ns_mov_b32 s99, 8192\nQEB 92,84,85,99\nQB 104,167,0\ns_add_u32 m0, s69, 98304\nQA 136,108,220\nQL 56,170,68,0\nQA 112,112,216\nQB 108,167,1024\n"
"v_fmac_f32 v176, v178, v178\nQA 140,112,220\nQG 4\nQAJ 177,179\nv_fmac_f32 v177, 0x42a00000, v176\nQA 116,116,216\nQB 112,167,2048\nQN 223,177\nQA 144,116,220\nQL 56,170,68,1024\nQA 120,120,216\n"
"QB 116,167,3072\nQBS 177,223\nQA 148,120,220\nQG 4\nQAJ 177,223\nQN 223,177\nQC 152,124,160\nQB 120,167,4096\nQBN 177,223\nQC 156,100,160\nQB 124,167,5120\nQG 4\nQA 96,104,160\nQB 100,167,6144\n"
"QAJ 177,223\nQA 124,104,164\nQL 56,170,68,2048\nQA 100,108,160\nQB 104,164,0\nv_mul_f32 v177, 0x3acccccd, v177\nQA 128,108,164\nQG 4\nQCM 177\nQBG 177\nQA 104,112,160\nQB 108,164,4096\n"
"v_add_f32 v177, 0x3727c5ac, v177\nQA 132,112,164\nQL 56,170,68,3072\nQA 108,116,160\nQB 112,167,7168\nQEP 68\nQA 136,116,164\nQG 4\ns_add_u32 m0, s69, 114688\nQCM 177\nQA 112,120,160\n"
"QB 116,167,8192\nQBG 177\nQA 140,120,164\nQL 56,170,68,0\nQA 116,124,160\nQB 120,167,9216\nQCM 222\nQA 144,124,164\nQG 4\nv_sqrt_f32 v177, v177\nQA 120,100,160\nQB 124,167,10240\nQBG 222\n"
"QA 148,100,164\nQL 56,170,68,1024\nQC 152,104,160\nQB 100,167,11264\nQG 4\nQC 156,108,160\nQB 104,167,12288\nQCM 177\nQA 96,112,168\nQB 108,167,13312\nQBG 177\nQA 124,112,172\nQG 4\nQV 177\n"
"QA 100,116,168\nQB 112,164,7168\nQEF 176,222\nQA 128,116,172\nQL 56,170,68,2048\nQA 104,120,168\nQB 116,164,11264\ns_mov_b64 s[36:37], %2\nQA 132,120,172\nQG 4\ns_add_u32 s36, s36, 74383360\nQAX 37\n"
"QGR 38\nQA 108,124,168\nQB 120,168,0\nQBA 39\nQA 136,124,172\nQL 56,170,68,3072\nQA 112,100,168\nQB 124,168,1024\nQEP 68\nQA 140,100,172\nQG 4\nQAA 70\ns_mov_b64 s[40:41], %2\nQA 116,104,168\n"
"QB 100,168,2048\ns_add_u32 s40, s40, 33554432\nQAX 41\nQA 144,104,172\nQJ 224,60,185,70,0\nQA 120,108,168\nQB 104,168,3072\nQAA 71\nQA 148,108,172\nQG 4\ns_mov_b32 s42, 16777216\nQBA 43\n"
"QC 152,112,160\nQB 108,168,4096\ns_mov_b64 s[44:45], %2\nQC 156,116,160\nQB 112,168,5120\nQG 4\nQA 96,120,176\nQB 116,168,6144\ns_add_u32 s44, s44, 79757312\nQAX 45\nQA 124,120,180\n"
"QJ 228,60,185,71,0\nQA 100,124,176\nQB 120,165,0\nQG 4\nQA 128,124,180\nQJ 232,60,185,70,1024\nQA 104,100,176\nQB 124,165,4096\ns_mov_b32 s46, 20971520\nQA 132,100,180\nQJ 236,60,185,71,1024\n"
"QA 108,104,176\nQB 100,168,7168\nQBA 47\nQA 136,104,180\nQG 4\ns_bfe_u32 s54, %1, 65536\nQCK 54\nQA 112,108,176\nQB 104,168,8192\ns_bfe_u32 s83, %1, 196613\nQA 140,108,180\nQJ 240,60,185,70,2048\n"
"QA 116,112,176\nQB 108,168,9216\nQZ 54,83\nQA 144,112,180\nQG 4\ns_bfe_u32 s83, %1, 131072\nQBH 83\nQA 120,116,176\nQB 112,168,10240\ns_bfe_u32 s84, %1, 131078\nQA 148,116,180\nQJ 244,60,185,71,2048\n"
"QC 152,120,160\nQB 116,168,11264\nQG 4\nQC 156,124,160\nQB 120,168,12288\nQZ 83,84\nQA 96,100,184\nQB 124,168,13312\nQG 4\nQA 124,100,188\nQJ 248,60,185,70,3072\nQA 100,104,184\nQB 100,165,7168\n"
"s_bitcmp1_b32 %1, 2\nQBI 54,83\nQA 128,104,188\nQJ 252,60,185,71,3072\nQA 104,108,184\nQB 104,165,11264\ns_bfe_u32 s55, %1, 131075\nQA 132,108,188\nQG 4\ns_lshl_b32 s55, s55, 1\n"
"s_bfe_u32 s83, %1, 196611\nQA 108,112,184\nQAH 17\ns_barrier\nQCR 88\ns_bfe_u32 s99, %1, 196611\nQBU 99\ns_add_u32 s99, s99, 10240\nQZ 99,96\nQEI \nQEJ 92,88,99\nQEK \nQB 108,167,49152\n"
"s_add_u32 m0, s69, 131072\nQA 136,112,188\nQL 56,170,68,0\nQA 112,116,184\nQB 112,167,50176\ns_bitcmp1_b32 %1, 2\nQBI 55,83\nQA 140,116,188\nQG 4\ns_bfe_u32 s51, %1, 65537\ns_bitcmp1_b32 %1, 2\n"
"s_cselect_b32 s51, 2, s51\ns_cselect_b32 s53, 20480, 0\nQA 116,120,184\nQB 116,167,51200\nQBP 53\nQA 144,120,188\nQL 56,170,68,1024\nQA 120,124,184\nQB 120,167,52224\nQFE 52,54\nQA 148,124,188\nQG 4\n"
"s_andn2_b32 s83, s55, 1\nQZ 52,83\nQC 152,100,160\nQB 124,167,53248\nQBX 84,51\nQC 156,104,160\nQB 100,167,54272\nQG 4\nQA 96,108,128\nQB 104,167,55296\nQZ 52,84\nQA 124,108,132\nQL 56,170,68,2048\n"
"QA 100,112,128\nQB 108,164,49152\ns_mul_i32 s52, s52, 40960\nQA 128,112,132\nQG 4\ns_mul_i32 s84, %0, 10240\nQZ 52,84\nQA 104,116,128\nQB 112,164,53248\ns_mul_i32 s48, s83, 655360\nQA 132,116,132\n"
"QL 56,170,68,3072\nQA 108,120,128\nQB 116,167,56320\nQEP 68\nQA 136,120,132\nQG 4\nQFI 69\nQBX 83,55\nQA 112,124,128\nQB 120,167,57344\ns_mul_i32 s83, s83, 5120\nQA 140,124,132\nQL 56,170,68,0\n"
"QA 116,100,128\nQB 124,167,58368\nQZ 52,83\nQA 144,100,132\nQG 4\nQZ 48,83\ns_mul_i32 s83, s51, 491520\nQA 120,104,128\nQB 100,167,59392\nQZ 48,83\nQA 148,104,132\nQL 56,170,68,1024\nQC 152,108,160\n"
"QB 104,167,60416\nQG 4\nQC 156,112,160\nQB 108,167,61440\ns_mul_i32 s49, %0, 5120\nQA 96,116,136\nQB 112,167,62464\ns_bitcmp1_b32 %1, 2\ns_cselect_b32 s84, s84, s49\nQA 124,116,140\nQG 4\n"
"QBM 83,84,185\nQEG 83,54\nQA 100,120,136\nQB 116,164,56320\ns_add_u32 s83, s83, %0\nQA 128,120,140\nQL 56,170,68,2048\nQA 104,124,136\nQB 120,164,60416\ns_lshl_b32 s50, s83, 18\nQA 132,124,140\nQG 4\n"
"s_mul_i32 s83, s51, 49152\nQZ 50,83\nQA 108,100,136\nQB 124,167,32768\nQGO 51,50\nQA 136,100,140\nQL 56,170,68,3072\nQA 112,104,136\nQB 100,167,33792\nQEP 68\nQA 140,104,140\nQG 4\nQAA 70\n"
"QEU 80,185\nQA 116,108,136\nQB 104,167,34816\nv_add_u32 v81, 65536, v185\nQA 144,108,140\nQJ 192,60,185,70,0\nQA 120,112,136\nQB 108,167,35840\nQAA 71\nQA 148,112,140\nQG 4\nQGL 82,185\n"
"QC 152,116,160\nQB 112,167,36864\nQC 156,120,160\nQB 116,167,37888\nQG 4\nQA 96,124,144\nQB 120,167,38912\nQA 124,124,148\nQJ 196,60,185,71,0\nQA 100,100,144\nQB 124,164,32768\nQG 4\nQA 128,100,148\n"
"QJ 200,60,185,70,1024\nQA 104,104,144\nQB 100,164,36864\nQA 132,104,148\nQJ 204,60,185,71,1024\nQA 108,108,144\nQB 104,167,39936\nQA 136,108,148\nQG 4\nQA 112,112,144\nQB 108,167,40960\n"
"QA 140,112,148\nQJ 208,60,185,70,2048\nQA 116,116,144\nQB 112,167,41984\nQA 144,116,148\nQG 4\nQA 120,120,144\nQB 116,167,43008\nQA 148,120,148\nQJ 212,60,185,71,2048\nQC 152,124,160\n"
"QB 120,167,44032\nQG 4\nQC 156,100,160\nQB 124,167,45056\nQA 96,104,152\nQB 100,167,46080\nQG 4\nQA 124,104,156\nQJ 216,60,185,70,3072\nQA 100,108,152\nQB 104,164,39936\nQA 128,108,156\n"
"QJ 220,60,185,71,3072\nQA 104,112,152\nQB 108,164,44032\nQA 132,112,156\nQG 4\nQA 108,116,152\nQAH 17\ns_barrier\nQHH 84\nQBD \ns_cbranch_vccnz L_ps22\nL_pr22:\nQB 112,168,32768\nQBV 49\n"
"QA 136,116,156\nQL 36,83,48,0\nQA 112,120,152\nQB 116,168,33792\nQA 140,120,156\nQG 4\nQA 116,124,152\nQB 120,168,34816\nQA 144,124,156\nQL 36,83,48,1024\nQA 120,100,152\nQB 124,168,35840\n"
"QA 148,100,156\nQG 4\nQC 152,104,160\nQB 100,168,36864\nQC 156,108,160\nQB 104,168,37888\nQG 4\nQA 96,112,224\nQB 108,168,38912\nQA 124,112,228\nQL 36,83,48,2048\nQA 100,116,224\nQB 112,165,32768\n"
"QA 128,116,228\nQG 4\nQA 104,120,224\nQB 116,165,36864\nQA 132,120,228\nQL 36,83,48,3072\nQA 108,124,224\nQB 120,168,39936\nQCT 49\nQA 136,124,228\nQG 4\nQAA 48\nQA 112,100,224\nQB 124,168,40960\n"
"QA 140,100,228\nQL 36,83,48,0\nQA 116,104,224\nQB 100,168,41984\nQZ 48,53\nQA 144,104,228\nQG 4\nQA 120,108,224\nQB 104,168,43008\nQA 148,108,228\nQC 152,112,160\nQB 108,168,44032\nQG 4\n"
"QC 156,116,160\nQB 112,168,45056\nQA 96,120,232\nQB 116,168,46080\nQA 124,120,236\nQG 4\nQA 100,124,232\nQB 120,165,39936\nQA 128,124,236\nQA 104,100,232\nQB 124,165,44032\nQA 132,100,236\nQG 4\n"
"QA 108,104,232\nQB 100,168,49152\nQA 136,104,236\nQA 112,108,232\nQB 104,168,50176\nQA 140,108,236\nQG 4\nQA 116,112,232\nQB 108,168,51200\nQA 144,112,236\nQA 120,116,232\nQB 112,168,52224\n"
"QA 148,116,236\nQG 4\nQC 152,120,160\nQB 116,168,53248\nQC 156,124,160\nQB 120,168,54272\nQG 4\nQA 96,100,240\nQB 124,168,55296\nQA 124,100,244\nQA 100,104,240\nQB 100,165,49152\nQA 128,104,244\n"
"QG 4\nQA 104,108,240\nQB 104,165,53248\nQA 132,108,244\nQA 108,112,240\nQB 108,168,56320\nQA 136,112,244\nQG 4\nQA 112,116,240\nQB 112,168,57344\nQA 140,116,244\nQA 116,120,240\nQB 116,168,58368\n"
"QA 144,120,244\nQG 4\nQA 120,124,240\nQB 120,168,59392\nQA 148,124,244\nQC 152,100,160\nQB 124,168,60416\nQG 4\nQC 156,104,160\nQB 100,168,61440\nQA 96,108,248\nQB 104,168,62464\nQA 124,108,252\n"
"QG 4\nQA 100,112,248\nQB 108,165,56320\nQA 128,112,252\nQA 104,116,248\nQB 112,165,60416\nQA 132,116,252\nQG 4\nQA 108,120,248\nQAH 5\ns_barrier\nQB 116,169,0\nQEX 49\nQA 136,120,252\nQL 36,83,48,0\n"
"QA 112,124,248\nQB 120,169,1024\nQA 140,124,252\nQG 4\nQA 116,100,248\nQB 124,169,2048\nQA 144,100,252\nQL 36,83,48,1024\nQA 120,104,248\nQB 100,169,3072\nQA 148,104,252\nQG 4\nQC 152,108,160\n"
"QB 104,169,4096\nQC 156,112,160\nQB 108,169,5120\nQG 4\nQA 96,116,192\nQB 112,169,6144\nQA 124,116,196\nQL 36,83,48,2048\nQA 100,120,192\nQB 116,166,0\nQA 128,120,196\nQG 4\nQA 104,124,192\n"
"QB 120,166,4096\nQA 132,124,196\nQL 36,83,48,3072\nQA 108,100,192\nQB 124,169,7168\nQEM 49\nQA 136,100,196\nQG 4\nQAA 48\nQA 112,104,192\nQB 100,169,8192\nQA 140,104,196\nQL 36,83,48,0\n"
"QA 116,108,192\nQB 104,169,9216\nQZ 48,53\nQA 144,108,196\nQG 4\nQEN 49\nQA 120,112,192\nQB 108,169,10240\nQA 148,112,196\nQL 36,83,48,0\nQC 152,116,160\nQB 112,169,11264\nQG 4\nQC 156,120,160\n"
"QB 116,169,12288\nQA 96,124,200\nQB 120,169,13312\nQA 124,124,204\nQG 4\nQA 100,100,200\nQB 124,166,7168\nQA 128,100,204\nQL 36,83,48,1024\nQA 104,104,200\nQB 100,166,11264\nQA 132,104,204\nQG 4\n"
"QA 108,108,200\nQB 104,169,16384\nQA 136,108,204\nQL 36,83,48,2048\nQA 112,112,200\nQB 108,169,17408\nQA 140,112,204\nQG 4\nQA 116,116,200\nQB 112,169,18432\nQA 144,116,204\nQL 36,83,48,3072\n"
"QA 120,120,200\nQB 116,169,19456\ns_add_u32 m0, s49, 45056\nQA 148,120,204\nQG 4\nQAA 48\nQC 152,124,160\nQB 120,169,20480\nQC 156,100,160\nQB 124,169,21504\nQG 4\nQA 96,104,208\nQB 100,169,22528\n"
"QA 124,104,212\nQL 36,83,48,0\nQA 100,108,208\nQB 104,166,16384\nQZ 48,53\nQA 128,108,212\nQG 4\nQFL 49\nQA 104,112,208\nQB 108,166,20480\nQA 132,112,212\nQL 36,83,48,0\nQA 108,116,208\n"
"QB 112,169,23552\nQA 136,116,212\nQG 4\nQA 112,120,208\nQB 116,169,24576\nQA 140,120,212\nQL 36,83,48,1024\nQA 116,124,208\nQB 120,169,25600\nQA 144,124,212\nQG 4\nQA 120,100,208\nQB 124,169,26624\n"
"QA 148,100,212\nQL 36,83,48,2048\nQC 152,104,160\nQB 100,169,27648\nQG 4\nQC 156,108,160\nQB 104,169,28672\nQA 96,112,216\nQB 108,169,29696\nQG 4\nQA 124,112,220\nQL 36,83,48,3072\nQA 100,116,216\n"
"QB 112,166,23552\nQDY 49\nQA 128,116,220\nQAA 48\nQA 104,120,216\nQB 116,166,27648\nQG 4\nQA 132,120,220\nQL 36,83,48,0\nQA 108,124,216\nQZ 48,53\nQA 136,124,220\nQA 112,100,216\nQA 140,100,220\n"
"QG 3\nQA 116,104,216\nQA 144,104,220\nQG 2\nQA 120,108,216\nQA 148,108,220\nQG 1\nQC 152,112,160\nQG 0\nQC 156,116,160\nQGY 99,54\ns_min_u32 s99, s99, s54\nQBU 99\ns_add_u32 s99, s99, 10240\n"
"QCZ 92,84,99\nQBE \nQCH 152,165,16384\nQBE \nQCH 156,165,20480\nQG 0\ns_barrier\nQH 192,168,16384\nQH 196,168,17408\nQH 200,168,18432\nQH 204,168,19456\nQH 208,168,20480\nQH 212,168,21504\n"
"QH 216,168,22528\ns_mov_b64 s[64:65], %2\ns_add_u32 s64, s64, 60817408\nQAX 65\ns_mov_b32 s66, 7340032\nQBA 67\nv_add_u32 v170, 57344, v185\nQG 0\nv_mov_b32 v164, 0xbdd2d3e8\n"
"v_mov_b32 v165, 0xc0135761\nQGF 49\nQBE \nQL 36,83,48,0\nQAF 96,192,172\nQAF 98,194,172\nQAF 100,196,172\nQAF 102,198,172\nQAF 104,200,172\nQAF 106,202,172\nQAF 108,204,172\nQAU 96,172\nQAU 98,172\n"
"QAU 100,172\nQL 36,83,48,1024\nQAU 102,172\nQAU 104,172\nQAU 106,172\nQAU 108,172\nQAP 220,96\nQAP 222,98\nQAP 224,100\nQAP 226,102\nQAP 228,104\nQAP 230,106\nQL 36,83,48,2048\nQAP 232,108\n"
"QAV 220,164\nQAV 222,164\nQAV 224,164\nQAV 226,164\nQAV 228,164\nQAV 230,164\nQAV 232,164\nQX 220,96\nQX 222,98\nQL 36,83,48,3072\nQX 224,100\nQX 226,102\nQX 228,104\nQX 230,106\nQX 232,108\nQW 220\n"
"QW 221\nQW 222\nQW 223\nQW 224\nQDN 49\nQAA 48\nQL 36,83,48,0\nQW 225\nQW 226\nQW 227\nQW 228\nQW 229\nQW 230\nQW 231\nQW 232\nQW 233\nQAK 220\nQZ 48,53\nQDD 49\nQBE \nQL 36,83,48,0\nQAK 222\n"
"QAK 224\nQAK 226\nQAK 228\nQAK 230\nQAK 232\nQV 220\nQV 221\nQV 222\nQV 223\nQL 36,83,48,1024\nQV 224\nQV 225\nQV 226\nQV 227\nQV 228\nQV 229\nQV 230\nQV 231\nQV 232\nQV 233\nQL 36,83,48,2048\n"
"QX 96,220\nQX 98,222\nQX 100,224\nQX 102,226\nQX 104,228\nQX 106,230\nQX 108,232\nQAF 110,206,172\nQAF 112,208,172\nQAF 114,210,172\nQL 36,83,48,3072\nQAF 116,212,172\nQAF 118,214,172\n"
"QAF 120,216,172\nQAF 122,218,172\nQAU 110,172\nQAU 112,172\nQAU 114,172\nQAU 116,172\nQAU 118,172\nQAU 120,172\nQGG 49\nQAA 48\nQL 36,83,48,0\nQAU 122,172\nQAP 234,110\nQAP 236,112\nQAP 238,114\n"
"QAP 240,116\nQAP 242,118\nQAP 244,120\nQAP 246,122\nQAV 234,164\nQAV 236,164\nQZ 48,53\nQGZ 49\nQBE \nQL 36,83,48,0\nQAV 238,164\nQAV 240,164\nQAV 242,164\nQAV 244,164\nQAV 246,164\nQX 234,110\n"
"QX 236,112\nQX 238,114\nQX 240,116\nQX 242,118\nQL 36,83,48,1024\nQX 244,120\nQX 246,122\nQW 234\nQW 235\nQW 236\nQW 237\nQW 238\nQW 239\nQW 240\nQW 241\nQL 36,83,48,2048\nQW 242\nQW 243\nQW 244\n"
"QW 245\nQW 246\nQW 247\nQAK 234\nQAK 236\nQAK 238\nQAK 240\nQL 36,83,48,3072\nQAK 242\nQAK 244\nQAK 246\nQV 234\nQV 235\nQV 236\nQV 237\nQV 238\nQV 239\nQV 240\nQFI 49\nQAA 48\nQL 36,83,48,0\nQV 241\n"
"QV 242\nQV 243\nQV 244\nQV 245\nQV 246\nQV 247\nQX 110,234\nQX 112,236\nQX 114,238\nQZ 48,53\nQX 116,240\nQX 118,242\nQX 120,244\nQX 122,246\nQAH 15\nv_cmp_ne_u32 vcc, 32, v84\nQBD \n"
"s_cbranch_vccnz L_ps23\nL_pr23:\ns_bitcmp1_b32 %1, 2\ns_cbranch_scc1 L_roleB\nQDO \nQCG 86\nL_pc24:\ns_lshr_b32 s88, %1, 3\nQBH 88\ns_add_u32 s88, s88, %0\n"
"s_mul_i32 s88, s88, ((L_roleB-L_roleS-1024)/127+63)&-64\ns_min_u32 s88, s88, L_roleB-L_roleS-1024\ns_add_u32 s88, s88, L_roleS-L_pc24\nQZ 86,88\nQAX 87\nQCL 92,185\nQFG 90,86,92\nL_roleS:\n"
"QJ 0,40,185,50,0\nQJ 4,40,185,51,0\nQJ 8,40,185,50,1024\nQJ 12,40,185,51,1024\nQJ 16,40,185,50,2048\nQJ 20,40,185,51,2048\nQJ 24,40,185,50,3072\nQJ 28,40,185,51,3072\nQAA 50\nQJ 32,40,185,50,0\n"
"QAA 51\nQJ 36,40,185,51,0\nQJ 40,40,185,50,1024\nQJ 44,40,185,51,1024\nQAY 96,97\nQF 97,98,99\nQF 98,100,101\nQF 99,102,103\nQAB 96,64,185,72,0\nQF 100,104,105\nQF 101,106,107\nQF 102,108,109\n"
"QF 103,110,111\nQAB 100,64,185,72,1024\nQF 104,112,113\nQF 105,114,115\nQF 106,116,117\nQF 107,118,119\nQAB 104,64,185,72,2048\nQF 108,120,121\nQF 109,122,123\nQFM 108,64,185,73\n"
"QJ 48,40,185,50,2048\nQAF 124,192,176\nQAF 126,194,176\nQAF 128,196,176\nQAF 130,198,176\nQAF 132,200,176\nQAF 134,202,176\nQAF 136,204,176\nQAU 124,176\nQAU 126,176\nQAU 128,176\nQAU 130,176\n"
"QAU 132,176\nQAU 134,176\nQAU 136,176\nQAP 220,124\nQAP 222,126\nQAP 224,128\nQAP 226,130\nQAP 228,132\nQJ 52,40,185,51,2048\nQAP 230,134\nQAP 232,136\nQAV 220,164\nQAV 222,164\nQAV 224,164\n"
"QAV 226,164\nQAV 228,164\nQAV 230,164\nQAV 232,164\nQX 220,124\nQX 222,126\nQX 224,128\nQX 226,130\nQX 228,132\nQX 230,134\nQX 232,136\nQW 220\nQW 221\nQW 222\nQJ 56,40,185,50,3072\nQW 223\nQW 224\n"
"QW 225\nQW 226\nQW 227\nQW 228\nQW 229\nQW 230\nQW 231\nQW 232\nQW 233\nQAK 220\nQAK 222\nQAK 224\nQAK 226\nQAK 228\nQAK 230\nQAK 232\nQV 220\nQJ 60,40,185,51,3072\nQV 221\nQV 222\nQV 223\nQV 224\n"
"QV 225\nQV 226\nQV 227\nQV 228\nQV 229\nQV 230\nQV 231\nQV 232\nQV 233\nQX 124,220\nQX 126,222\nQX 128,224\nQX 130,226\nQX 132,228\nQX 134,230\nQAA 50\nQJ 64,40,185,50,0\nQX 136,232\nQAF 138,206,176\n"
"QAF 140,208,176\nQAF 142,210,176\nQAF 144,212,176\nQAF 146,214,176\nQAF 148,216,176\nQAF 150,218,176\nQAU 138,176\nQAU 140,176\nQAU 142,176\nQAU 144,176\nQAU 146,176\nQAU 148,176\nQAU 150,176\n"
"QAP 234,138\nQAP 236,140\nQAP 238,142\nQAP 240,144\nQAA 51\nQJ 68,40,185,51,0\nQAP 242,146\nQAP 244,148\nQAP 246,150\nQAV 234,164\nQAV 236,164\nQAV 238,164\nQAV 240,164\nQAV 242,164\nQAV 244,164\n"
"QAV 246,164\nQX 234,138\nQX 236,140\nQX 238,142\nQX 240,144\nQX 242,146\nQX 244,148\nQX 246,150\nQW 234\nQW 235\nQJ 72,40,185,50,1024\nQW 236\nQW 237\nQW 238\nQW 239\nQW 240\nQW 241\nQW 242\nQW 243\n"
"QW 244\nQW 245\nQW 246\nQW 247\nQAK 234\nQAK 236\nQAK 238\nQAK 240\nQAK 242\nQAK 244\nQAK 246\nQJ 76,40,185,51,1024\nQV 234\nQV 235\nQV 236\nQV 237\nQV 238\nQV 239\nQV 240\nQV 241\nQV 242\nQV 243\n"
"QV 244\nQV 245\nQV 246\nQV 247\nQX 138,234\nQX 140,236\nQX 142,238\nQX 144,240\nQX 146,242\nQX 148,244\nQX 150,246\nQAY 124,125\nQF 125,126,127\nQF 126,128,129\nQF 127,130,131\nQAB 124,64,170,72,0\n"
"QF 128,132,133\nQF 129,134,135\nQF 130,136,137\nQF 131,138,139\nQAB 128,64,170,72,1024\nQF 132,140,141\nQF 133,142,143\nQF 134,144,145\nQF 135,146,147\nQAB 132,64,170,72,2048\nQF 136,148,149\n"
"QF 137,150,151\nQFM 136,64,170,73\nQAH 24\ns_barrier\nQB 228,80,0\nQB 232,80,1024\nQB 236,80,2048\nQB 240,80,3072\nQB 244,80,4096\nQB 248,80,5120\nQG 4\nQAE 0,228,0\nQB 252,80,6144\nQAE 40,228,4\n"
"QAE 4,232,0\nQB 228,80,7168\nQAE 44,232,4\nQG 4\nQAE 8,236,0\nQB 232,80,8192\nQAE 48,236,4\nQAE 12,240,0\nQB 236,80,9216\nQAE 52,240,4\nQG 4\nQAE 16,244,0\nQB 240,80,10240\nQAE 56,244,4\n"
"QAE 20,248,0\nQB 244,80,11264\nQAE 60,248,4\nQG 4\nQAE 24,252,0\nQB 248,80,12288\nQAE 64,252,4\nQAE 28,228,0\nQB 252,80,13312\nQAE 68,228,4\nQG 4\nQAE 32,232,0\nQB 228,80,14336\nQAE 72,232,4\n"
"QAE 36,236,0\nQB 232,80,15360\nQAE 76,236,4\nQG 4\nQA 0,240,8\nQB 236,80,16384\nQA 40,240,12\nQA 4,244,8\nQB 240,80,17408\nQA 44,244,12\nQG 4\nQA 8,248,8\nQB 244,80,18432\nQA 48,248,12\nQA 12,252,8\n"
"QB 248,80,19456\nQA 52,252,12\nQG 4\nQA 16,228,8\nQAH 20\ns_barrier\nQB 252,80,20480\nQA 56,228,12\nQJ 80,40,185,50,2048\nQA 20,232,8\nQB 228,80,21504\nQA 60,232,12\nQG 4\nQA 24,236,8\n"
"QB 232,80,22528\nQA 64,236,12\nQJ 84,40,185,51,2048\nQA 28,240,8\nQB 236,80,23552\nQA 68,240,12\nQG 4\nQA 32,244,8\nQB 240,80,24576\nQA 72,244,12\nQJ 88,40,185,50,3072\nQA 36,248,8\nQB 244,80,25600\n"
"QA 76,248,12\nQG 4\nQA 0,252,16\nQB 248,80,26624\nQA 40,252,20\nQJ 92,40,185,51,3072\nQA 4,228,16\nQB 252,80,27648\nQFJ 49\nQA 44,228,20\nQG 4\nQA 8,232,16\nQB 228,80,28672\nQA 48,232,20\n"
"QL 36,83,48,0\nQA 12,236,16\nQB 232,80,29696\nQA 52,236,20\nQG 4\nQA 16,240,16\nQB 236,80,30720\nQA 56,240,20\nQL 36,83,48,1024\nQA 20,244,16\nQB 240,80,31744\nQA 60,244,20\nQG 4\nQA 24,248,16\n"
"QB 244,80,32768\nQA 64,248,20\nQL 36,83,48,2048\nQA 28,252,16\nQB 248,80,33792\nQA 68,252,20\nQG 4\nQA 32,228,16\nQB 252,80,34816\nQA 72,228,20\nQL 36,83,48,3072\nQA 36,232,16\nQB 228,80,35840\n"
"QGH 49\nQA 76,232,20\nQG 4\nQAA 48\nQA 0,236,24\nQB 232,80,36864\nQA 40,236,28\nQL 36,83,48,0\nQA 4,240,24\nQB 236,80,37888\nQZ 48,53\nQA 44,240,28\nQG 4\nQA 8,244,24\nQB 240,80,38912\nQA 48,244,28\n"
"QA 12,248,24\nQB 244,80,39936\nQA 52,248,28\nQG 4\nQA 16,252,24\nQAH 25\ns_barrier\nQB 248,80,40960\nQAA 50\nQA 56,252,28\nQJ 0,40,185,50,0\nQA 20,228,24\nQB 252,80,41984\nQAA 51\nQA 60,228,28\nQG 4\n"
"QA 24,232,24\nQB 228,80,43008\nQA 64,232,28\nQJ 4,40,185,51,0\nQA 28,236,24\nQB 232,80,44032\nQA 68,236,28\nQG 4\nQA 32,240,24\nQB 236,80,45056\nQA 72,240,28\nQJ 8,40,185,50,1024\nQA 36,244,24\n"
"QB 240,80,46080\nQA 76,244,28\nQG 4\nQA 0,248,32\nQB 244,80,47104\nQA 40,248,36\nQJ 12,40,185,51,1024\nQA 4,252,32\nQB 248,80,48128\nQBV 49\nQA 44,252,36\nQG 4\nQA 8,228,32\nQB 252,80,49152\n"
"QA 48,228,36\nQL 36,83,48,0\nQA 12,232,32\nQB 228,80,50176\nQA 52,232,36\nQG 4\nQA 16,236,32\nQB 232,80,51200\nQA 56,236,36\nQL 36,83,48,1024\nQA 20,240,32\nQB 236,80,52224\nQA 60,240,36\nQG 4\n"
"QA 24,244,32\nQB 240,80,53248\nQA 64,244,36\nQL 36,83,48,2048\nQA 28,248,32\nQB 244,80,54272\nQA 68,248,36\nQG 4\nQA 32,252,32\nQB 248,80,55296\nQA 72,252,36\nQL 36,83,48,3072\nQA 36,228,32\n"
"QB 252,80,56320\nQCT 49\nQA 76,228,36\nQG 4\nQAA 48\nQA 0,232,40\nQB 228,80,57344\nQA 40,232,44\nQL 36,83,48,0\nQA 4,236,40\nQB 232,80,58368\nQZ 48,53\nQA 44,236,44\nQG 4\nQA 8,240,40\n"
"QB 236,80,59392\nQA 48,240,44\nQA 12,244,40\nQB 240,80,60416\nQA 52,244,44\nQG 4\nQA 16,248,40\nQAH 18\ns_barrier\nQCR 88\ns_bfe_u32 s99, %1, 131073\nQBH 99\ns_bfe_u32 s100, %1, 131078\nQZ 99,100\n"
"QBU 99\nQEP 99\nQZ 99,96\nQEI \nQEJ 92,88,99\nQEK \nQB 244,80,61440\nQA 56,248,44\nQJ 16,40,185,50,2048\nQA 20,252,40\nQB 248,80,62464\nQA 60,252,44\nQG 4\nQA 24,228,40\nQB 252,80,63488\n"
"QA 64,228,44\nQJ 20,40,185,51,2048\nQA 28,232,40\nQB 228,80,64512\nQA 68,232,44\nQG 4\nQA 32,236,40\nQB 232,81,0\nQA 72,236,44\nQJ 24,40,185,50,3072\nQA 36,240,40\nQB 236,81,1024\nQA 76,240,44\nQG 4\n"
"QA 0,244,48\nQB 240,81,2048\nQA 40,244,52\nQJ 28,40,185,51,3072\nQA 4,248,48\nQB 244,81,3072\nQEX 49\nQA 44,248,52\nQG 4\nQA 8,252,48\nQB 248,81,4096\nQA 48,252,52\nQL 36,83,48,0\nQA 12,228,48\n"
"QB 252,81,5120\nQA 52,228,52\nQG 4\nQA 16,232,48\nQB 228,81,6144\nQA 56,232,52\nQL 36,83,48,1024\nQA 20,236,48\nQB 232,81,7168\nQA 60,236,52\nQG 4\nQA 24,240,48\nQB 236,81,8192\nQA 64,240,52\n"
"QL 36,83,48,2048\nQA 28,244,48\nQB 240,81,9216\nQA 68,244,52\nQG 4\nQA 32,248,48\nQB 244,81,10240\nQA 72,248,52\nQL 36,83,48,3072\nQA 36,252,48\nQB 248,81,11264\nQEM 49\nQA 76,252,52\nQG 4\nQAA 48\n"
"QA 0,228,56\nQB 252,81,12288\nQA 40,228,60\nQL 36,83,48,0\nQA 4,232,56\nQB 228,81,13312\nQZ 48,53\nQA 44,232,60\nQG 4\nQA 8,236,56\nQB 232,81,14336\nQA 48,236,60\nQA 12,240,56\nQB 236,81,15360\n"
"QA 52,240,60\nQG 4\nQA 16,244,56\nQAH 28\ns_barrier\nQB 240,81,36864\nQAA 50\nQA 56,244,60\nQJ 32,40,185,50,0\nQA 20,248,56\nQB 244,81,37888\nQAA 51\nQA 60,248,60\nQG 4\nQA 24,252,56\n"
"QB 248,81,38912\nQA 64,252,60\nQJ 36,40,185,51,0\nQA 28,228,56\nQB 252,81,39936\nQA 68,228,60\nQG 4\nQA 32,232,56\nQB 228,81,40960\nQA 72,232,60\nQJ 40,40,185,50,1024\nQA 36,236,56\nQB 232,81,41984\n"
"QA 76,236,60\nQG 4\nQA 0,240,64\nQB 236,81,43008\nQA 40,240,68\nQJ 44,40,185,51,1024\nQA 4,244,64\nQB 240,81,44032\nQEN 49\nQA 44,244,68\nQG 4\nQA 8,248,64\nQB 244,81,45056\nQA 48,248,68\n"
"QL 36,83,48,0\nQA 12,252,64\nQB 248,81,46080\nQA 52,252,68\nQG 4\nQA 16,228,64\nQB 252,81,47104\nQA 56,228,68\nQL 36,83,48,1024\nQA 20,232,64\nQB 228,81,48128\nQA 60,232,68\nQG 4\nQA 24,236,64\n"
"QB 232,81,49152\nQA 64,236,68\nQL 36,83,48,2048\nQA 28,240,64\nQB 236,81,50176\nQA 68,240,68\nQG 4\nQA 32,244,64\nQB 240,81,51200\nQA 72,244,68\nQL 36,83,48,3072\nQA 36,248,64\nQB 244,81,52224\n"
"s_add_u32 m0, s49, 45056\nQA 76,248,68\nQG 4\nQAA 48\nQA 0,252,72\nQB 248,81,53248\nQA 40,252,76\nQL 36,83,48,0\nQA 4,228,72\nQB 252,81,54272\nQZ 48,53\nQA 44,228,76\nQG 4\nQA 8,232,72\n"
"QB 228,81,55296\nQA 48,232,76\nQA 12,236,72\nQB 232,81,56320\nQA 52,236,76\nQG 4\nQA 16,240,72\nQAH 33\ns_barrier\nQB 236,81,57344\nQA 56,240,76\nQJ 48,40,185,50,2048\nQA 20,244,72\nQB 240,81,58368\n"
"QA 60,244,76\nQG 4\nQA 24,248,72\nQB 244,81,59392\nQA 64,248,76\nQJ 52,40,185,51,2048\nQA 28,252,72\nQB 248,81,60416\nQA 68,252,76\nQG 4\nQA 32,228,72\nQB 252,81,61440\nQA 72,228,76\n"
"QJ 56,40,185,50,3072\nQA 36,232,72\nQB 228,81,62464\nQA 76,232,76\nQG 4\nQA 0,236,80\nQB 232,81,63488\nQA 40,236,84\nQJ 60,40,185,51,3072\nQA 4,240,80\nQB 236,81,64512\nQFL 49\nQA 44,240,84\nQG 4\n"
"QA 8,244,80\nQB 240,82,0\nQA 48,244,84\nQL 36,83,48,0\nQA 12,248,80\nQB 244,82,1024\nQA 52,248,84\nQG 4\nQA 16,252,80\nQB 248,82,2048\nQA 56,252,84\nQL 36,83,48,1024\nQA 20,228,80\nQB 252,82,3072\n"
"QA 60,228,84\nQG 4\nQA 24,232,80\nQB 228,82,4096\nQA 64,232,84\nQL 36,83,48,2048\nQA 28,236,80\nQB 232,82,5120\nQA 68,236,84\nQG 4\nQA 32,240,80\nQB 236,82,6144\nQA 72,240,84\nQL 36,83,48,3072\n"
"QA 36,244,80\nQB 240,82,7168\nQDY 49\nQA 76,244,84\nQG 4\nQAA 48\nQA 0,248,88\nQB 244,82,8192\nQA 40,248,92\nQL 36,83,48,0\nQA 4,252,88\nQB 248,82,9216\nQZ 48,53\nQA 44,252,92\nQG 4\nQA 8,228,88\n"
"QB 252,82,10240\nQA 48,228,92\nQA 12,232,88\nQB 228,82,11264\nQA 52,232,92\nQG 4\nQA 16,236,88\nQAH 33\ns_barrier\nQB 232,82,12288\nQAA 50\nQA 56,236,92\nQJ 64,40,185,50,0\nQA 20,240,88\n"
"QB 236,82,13312\nQAA 51\nQA 60,240,92\nQG 4\nQA 24,244,88\nQB 240,82,14336\nQA 64,244,92\nQJ 68,40,185,51,0\nQA 28,248,88\nQB 244,82,15360\nQA 68,248,92\nQG 4\nQA 32,252,88\nQB 248,82,16384\n"
"QA 72,252,92\nQJ 72,40,185,50,1024\nQA 36,228,88\nQB 252,82,17408\nQA 76,228,92\nQG 4\nQA 0,232,0\nQB 228,82,18432\nQA 40,232,4\nQJ 76,40,185,51,1024\nQA 4,236,0\nQB 232,82,19456\nQGF 49\n"
"QA 44,236,4\nQG 4\nQA 8,240,0\nQB 236,82,20480\nQA 48,240,4\nQL 36,83,48,0\nQA 12,244,0\nQB 240,82,21504\nQA 52,244,4\nQG 4\nQA 16,248,0\nQB 244,82,22528\nQA 56,248,4\nQL 36,83,48,1024\nQA 20,252,0\n"
"QB 248,82,23552\nQA 60,252,4\nQG 4\nQA 24,228,0\nQB 252,82,24576\nQA 64,228,4\nQL 36,83,48,2048\nQA 28,232,0\nQB 228,82,25600\nQA 68,232,4\nQG 4\nQA 32,236,0\nQB 232,82,26624\nQA 72,236,4\n"
"QL 36,83,48,3072\nQA 36,240,0\nQB 236,82,27648\nQDN 49\nQA 76,240,4\nQG 4\nQAA 48\nQA 0,244,8\nQB 240,82,28672\nQA 40,244,12\nQL 36,83,48,0\nQA 4,248,8\nQB 244,82,29696\nQZ 48,53\nQA 44,248,12\nQG 4\n"
"QA 8,252,8\nQB 248,82,30720\nQA 48,252,12\nQA 12,228,8\nQB 252,82,31744\nQA 52,228,12\nQG 4\nQA 16,232,8\nQAH 32\ns_barrier\nQB 228,81,16384\nQA 56,232,12\nQJ 80,40,185,50,2048\nQA 20,236,8\n"
"QB 232,81,17408\nQA 60,236,12\nQG 4\nQA 24,240,8\nQB 236,81,18432\nQA 64,240,12\nQJ 84,40,185,51,2048\nQA 28,244,8\nQB 240,81,19456\nQA 68,244,12\nQG 4\nQA 32,248,8\nQB 244,81,20480\nQA 72,248,12\n"
"QJ 88,40,185,50,3072\nQA 36,252,8\nQB 248,81,21504\nQA 76,252,12\nQG 4\nQA 0,228,16\nQB 252,81,22528\nQA 40,228,20\nQJ 92,40,185,51,3072\nQA 4,232,16\nQB 228,81,23552\nQDD 49\nQA 44,232,20\nQG 4\n"
"QA 8,236,16\nQB 232,81,24576\nQA 48,236,20\nQL 36,83,48,0\nQA 12,240,16\nQB 236,81,25600\nQA 52,240,20\nQG 4\nQA 16,244,16\nQB 240,81,26624\nQA 56,244,20\nQL 36,83,48,1024\nQA 20,248,16\n"
"QB 244,81,27648\nQA 60,248,20\nQG 4\nQA 24,252,16\nQB 248,81,28672\nQA 64,252,20\nQL 36,83,48,2048\nQA 28,228,16\nQB 252,81,29696\nQA 68,228,20\nQG 4\nQA 32,232,16\nQB 228,81,30720\nQA 72,232,20\n"
"QL 36,83,48,3072\nQA 36,236,16\nQB 232,81,31744\nQGG 49\nQA 76,236,20\nQG 4\nQAA 48\nQA 0,240,24\nQB 236,81,32768\nQA 40,240,28\nQL 36,83,48,0\nQA 4,244,24\nQB 240,81,33792\nQZ 48,53\nQA 44,244,28\n"
"QG 4\nQA 8,248,24\nQB 244,81,34816\nQA 48,248,28\nQA 12,252,24\nQB 248,81,35840\nQA 52,252,28\nQG 4\nQA 16,228,24\nQAH 32\ns_barrier\nQCG 100\nL_pc25:\ns_lshr_b32 s99, %1, 2\nQGB 99\n"
"s_bitcmp1_b32 %0, 0\nQGC 99\nQER 99\ns_min_u32 s99, s99, L_done-L_pc25-512\nQZ 100,99\nQAX 101\nQES 92,185\nQGD 100,90,92\nQB 252,80,0\nQAA 50\nQA 56,228,28\nQJ 0,40,185,50,0\nQA 20,232,24\n"
"QB 228,80,1024\nQAA 51\nQA 60,232,28\nQG 4\nQA 24,236,24\nQB 232,80,2048\nQA 64,236,28\nQJ 4,40,185,51,0\nQA 28,240,24\nQB 236,80,3072\nQA 68,240,28\nQG 4\nQA 32,244,24\nQB 240,80,4096\nQA 72,244,28\n"
"QJ 8,40,185,50,1024\nQA 36,248,24\nQB 244,80,5120\nQA 76,248,28\nQG 4\nQA 0,252,32\nQB 248,80,6144\nQA 40,252,36\nQJ 12,40,185,51,1024\nQA 4,228,32\nQB 252,80,7168\nQGZ 49\nQA 44,228,36\nQG 4\n"
"QA 8,232,32\nQB 228,80,8192\nQA 48,232,36\nQL 36,83,48,0\nQA 12,236,32\nQB 232,80,9216\nQA 52,236,36\nQG 4\nQA 16,240,32\nQB 236,80,10240\nQA 56,240,36\nQL 36,83,48,1024\nQA 20,244,32\n"
"QB 240,80,11264\nQA 60,244,36\nQG 4\nQA 24,248,32\nQB 244,80,12288\nQA 64,248,36\nQL 36,83,48,2048\nQA 28,252,32\nQB 248,80,13312\nQA 68,252,36\nQG 4\nQA 32,228,32\nQB 252,80,14336\nQA 72,228,36\n"
"QL 36,83,48,3072\nQA 36,232,32\nQB 228,80,15360\nQFI 49\nQA 76,232,36\nQG 4\nQAA 48\nQA 0,236,40\nQB 232,80,16384\nQA 40,236,44\nQL 36,83,48,0\nQA 4,240,40\nQB 236,80,17408\nQZ 48,53\nQA 44,240,44\n"
"QG 4\nQA 8,244,40\nQB 240,80,18432\nQA 48,244,44\nQA 12,248,40\nQB 244,80,19456\nQA 52,248,44\nQG 4\nQA 16,252,40\nQAH 33\ns_barrier\nQB 248,80,20480\nQA 56,252,44\nQJ 16,40,185,50,2048\n"
"QA 20,228,40\nQB 252,80,21504\nQA 60,228,44\nQG 4\nQA 24,232,40\nQB 228,80,22528\nQA 64,232,44\nQJ 20,40,185,51,2048\nQA 28,236,40\nQB 232,80,23552\nQA 68,236,44\nQG 4\nQA 32,240,40\nQB 236,80,24576\n"
"QA 72,240,44\nQJ 24,40,185,50,3072\nQA 36,244,40\nQB 240,80,25600\nQA 76,244,44\nQG 4\nQA 0,248,48\nQB 244,80,26624\nQA 40,248,52\nQJ 28,40,185,51,3072\nQA 4,252,48\nQB 248,80,27648\nQFJ 49\n"
"QA 44,252,52\nQG 4\nQA 8,228,48\nQB 252,80,28672\nQA 48,228,52\nQL 36,83,48,0\nQA 12,232,48\nQB 228,80,29696\nQA 52,232,52\nQG 4\nQA 16,236,48\nQB 232,80,30720\nQA 56,236,52\nQL 36,83,48,1024\n"
"QA 20,240,48\nQB 236,80,31744\nQA 60,240,52\nQG 4\nQA 24,244,48\nQB 240,80,32768\nQA 64,244,52\nQL 36,83,48,2048\nQA 28,248,48\nQB 244,80,33792\nQA 68,248,52\nQG 4\nQA 32,252,48\nQB 248,80,34816\n"
"QA 72,252,52\nQL 36,83,48,3072\nQA 36,228,48\nQB 252,80,35840\nQGH 49\nQA 76,228,52\nQG 4\nQAA 48\nQA 0,232,56\nQB 228,80,36864\nQA 40,232,60\nQL 36,83,48,0\nQA 4,236,56\nQB 232,80,37888\nQZ 48,53\n"
"QA 44,236,60\nQG 4\nQA 8,240,56\nQB 236,80,38912\nQA 48,240,60\nQA 12,244,56\nQB 240,80,39936\nQA 52,244,60\nQG 4\nQA 16,248,56\nQAH 33\ns_barrier\nQB 244,80,40960\nQAA 50\nQA 56,248,60\n"
"QJ 32,40,185,50,0\nQA 20,252,56\nQB 248,80,41984\nQAA 51\nQA 60,252,60\nQG 4\nQA 24,228,56\nQB 252,80,43008\nQA 64,228,60\nQJ 36,40,185,51,0\nQA 28,232,56\nQB 228,80,44032\nQA 68,232,60\nQG 4\n"
"QA 32,236,56\nQB 232,80,45056\nQA 72,236,60\nQJ 40,40,185,50,1024\nQA 36,240,56\nQB 236,80,46080\nQA 76,240,60\nQG 4\nQA 0,244,64\nQB 240,80,47104\nQA 40,244,68\nQJ 44,40,185,51,1024\nQA 4,248,64\n"
"QB 244,80,48128\nQBV 49\nQA 44,248,68\nQG 4\nQA 8,252,64\nQB 248,80,49152\nQA 48,252,68\nQL 36,83,48,0\nQA 12,228,64\nQB 252,80,50176\nQA 52,228,68\nQG 4\nQA 16,232,64\nQB 228,80,51200\nQA 56,232,68\n"
"QL 36,83,48,1024\nQA 20,236,64\nQB 232,80,52224\nQA 60,236,68\nQG 4\nQA 24,240,64\nQB 236,80,53248\nQA 64,240,68\nQL 36,83,48,2048\nQA 28,244,64\nQB 240,80,54272\nQA 68,244,68\nQG 4\nQA 32,248,64\n"
"QB 244,80,55296\nQA 72,248,68\nQL 36,83,48,3072\nQA 36,252,64\nQB 248,80,56320\nQCT 49\nQA 76,252,68\nQG 4\nQAA 48\nQA 0,228,72\nQB 252,80,57344\nQA 40,228,76\nQL 36,83,48,0\nQA 4,232,72\n"
"QB 228,80,58368\nQZ 48,53\nQA 44,232,76\nQG 4\nQA 8,236,72\nQB 232,80,59392\nQA 48,236,76\nQA 12,240,72\nQB 236,80,60416\nQA 52,240,76\nQG 4\nQA 16,244,72\nQAH 33\ns_barrier\nQB 240,80,61440\n"
"QA 56,244,76\nQJ 48,40,185,50,2048\nQA 20,248,72\nQB 244,80,62464\nQA 60,248,76\nQG 4\nQA 24,252,72\nQB 248,80,63488\nQA 64,252,76\nQJ 52,40,185,51,2048\nQA 28,228,72\nQB 252,80,64512\nQA 68,228,76\n"
"QG 4\nQA 32,232,72\nQB 228,81,0\nQA 72,232,76\nQJ 56,40,185,50,3072\nQA 36,236,72\nQB 232,81,1024\nQA 76,236,76\nQG 4\nQA 0,240,80\nQB 236,81,2048\nQA 40,240,84\nQJ 60,40,185,51,3072\nQA 4,244,80\n"
"QB 240,81,3072\nQEX 49\nQA 44,244,84\nQG 4\nQA 8,248,80\nQB 244,81,4096\nQA 48,248,84\nQL 36,83,48,0\nQA 12,252,80\nQB 248,81,5120\nQA 52,252,84\nQG 4\nQA 16,228,80\nQB 252,81,6144\nQA 56,228,84\n"
"QL 36,83,48,1024\nQA 20,232,80\nQB 228,81,7168\nQA 60,232,84\nQG 4\nQA 24,236,80\nQB 232,81,8192\nQA 64,236,84\nQL 36,83,48,2048\nQA 28,240,80\nQB 236,81,9216\nQA 68,240,84\nQG 4\nQA 32,244,80\n"
"QB 240,81,10240\nQA 72,244,84\nQL 36,83,48,3072\nQA 36,248,80\nQB 244,81,11264\nQEM 49\nQA 76,248,84\nQG 4\nQAA 48\nQA 0,252,88\nQB 248,81,12288\nQA 40,252,92\nQL 36,83,48,0\nQA 4,228,88\n"
"QB 252,81,13312\nQZ 48,53\nQA 44,228,92\nQG 4\nQA 8,232,88\nQB 228,81,14336\nQA 48,232,92\nQA 12,236,88\nQB 232,81,15360\nQA 52,236,92\nQG 4\nQA 16,240,88\nQAH 32\ns_barrier\nQB 236,81,36864\nQAA 50\n"
"QA 56,240,92\nQJ 64,40,185,50,0\nQA 20,244,88\nQB 240,81,37888\nQAA 51\nQA 60,244,92\nQG 4\nQA 24,248,88\nQB 244,81,38912\nQA 64,248,92\nQJ 68,40,185,51,0\nQA 28,252,88\nQB 248,81,39936\n"
"QA 68,252,92\nQG 4\nQA 32,228,88\nQB 252,81,40960\nQA 72,228,92\nQJ 72,40,185,50,1024\nQA 36,232,88\nQB 228,81,41984\nQA 76,232,92\nQG 4\nQA 0,236,0\nQB 232,81,43008\nQA 40,236,4\n"
"QJ 76,40,185,51,1024\nQA 4,240,0\nQB 236,81,44032\nQEN 49\nQA 44,240,4\nQG 4\nQA 8,244,0\nQB 240,81,45056\nQA 48,244,4\nQL 36,83,48,0\nQA 12,248,0\nQB 244,81,46080\nQA 52,248,4\nQG 4\nQA 16,252,0\n"
"QB 248,81,47104\nQA 56,252,4\nQL 36,83,48,1024\nQA 20,228,0\nQB 252,81,48128\nQA 60,228,4\nQG 4\nQA 24,232,0\nQB 228,81,49152\nQA 64,232,4\nQL 36,83,48,2048\nQA 28,236,0\nQB 232,81,50176\n"
"QA 68,236,4\nQG 4\nQA 32,240,0\nQB 236,81,51200\nQA 72,240,4\nQL 36,83,48,3072\nQA 36,244,0\nQB 240,81,52224\ns_add_u32 m0, s49, 45056\nQA 76,244,4\nQG 4\nQAA 48\nQA 0,248,8\nQB 244,81,53248\n"
"QA 40,248,12\nQL 36,83,48,0\nQA 4,252,8\nQB 248,81,54272\nQZ 48,53\nQA 44,252,12\nQG 4\nQA 8,228,8\nQB 252,81,55296\nQA 48,228,12\nQA 12,232,8\nQB 228,81,56320\nQA 52,232,12\nQG 4\nQA 16,236,8\n"
"QAH 32\ns_barrier\nQB 232,81,57344\nQA 56,236,12\nQJ 80,40,185,50,2048\nQA 20,240,8\nQB 236,81,58368\nQA 60,240,12\nQG 4\nQA 24,244,8\nQB 240,81,59392\nQA 64,244,12\nQJ 84,40,185,51,2048\n"
"QA 28,248,8\nQB 244,81,60416\nQA 68,248,12\nQG 4\nQA 32,252,8\nQB 248,81,61440\nQA 72,252,12\nQJ 88,40,185,50,3072\nQA 36,228,8\nQB 252,81,62464\nQA 76,228,12\nQG 4\nQA 0,232,16\nQB 228,81,63488\n"
"QA 40,232,20\nQJ 92,40,185,51,3072\nQA 4,236,16\nQB 232,81,64512\nQFL 49\nQA 44,236,20\nQG 4\nQA 8,240,16\nQB 236,82,0\nQA 48,240,20\nQL 36,83,48,0\nQA 12,244,16\nQB 240,82,1024\nQA 52,244,20\nQG 4\n"
"QA 16,248,16\nQB 244,82,2048\nQA 56,248,20\nQL 36,83,48,1024\nQA 20,252,16\nQB 248,82,3072\nQA 60,252,20\nQG 4\nQA 24,228,16\nQB 252,82,4096\nQA 64,228,20\nQL 36,83,48,2048\nQA 28,232,16\n"
"QB 228,82,5120\nQA 68,232,20\nQG 4\nQA 32,236,16\nQB 232,82,6144\nQA 72,236,20\nQL 36,83,48,3072\nQA 36,240,16\nQB 236,82,7168\nQDY 49\nQA 76,240,20\nQG 4\nQAA 48\nQA 0,244,24\nQB 240,82,8192\n"
"QA 40,244,28\nQL 36,83,48,0\nQA 4,248,24\nQB 244,82,9216\nQZ 48,53\nQA 44,248,28\nQG 4\nQA 8,252,24\nQB 248,82,10240\nQA 48,252,28\nQA 12,228,24\nQB 252,82,11264\nQA 52,228,28\nQG 4\nQA 16,232,24\n"
"QAH 32\ns_barrier\nQB 228,82,12288\nQAA 50\nQA 56,232,28\nQJ 0,40,185,50,0\nQA 20,236,24\nQB 232,82,13312\nQAA 51\nQA 60,236,28\nQG 4\nQA 24,240,24\nQB 236,82,14336\nQA 64,240,28\nQJ 4,40,185,51,0\n"
"QA 28,244,24\nQB 240,82,15360\nQA 68,244,28\nQG 4\nQA 32,248,24\nQB 244,82,16384\nQA 72,248,28\nQJ 8,40,185,50,1024\nQA 36,252,24\nQB 248,82,17408\nQA 76,252,28\nQG 4\nQA 0,228,32\nQB 252,82,18432\n"
"QA 40,228,36\nQJ 12,40,185,51,1024\nQA 4,232,32\nQB 228,82,19456\nQGF 49\nQA 44,232,36\nQG 4\nQA 8,236,32\nQB 232,82,20480\nQA 48,236,36\nQL 36,83,48,0\nQA 12,240,32\nQB 236,82,21504\nQA 52,240,36\n"
"QG 4\nQA 16,244,32\nQB 240,82,22528\nQA 56,244,36\nQL 36,83,48,1024\nQA 20,248,32\nQB 244,82,23552\nQA 60,248,36\nQG 4\nQA 24,252,32\nQB 248,82,24576\nQA 64,252,36\nQL 36,83,48,2048\nQA 28,228,32\n"
"QB 252,82,25600\nQA 68,228,36\nQG 4\nQA 32,232,32\nQB 228,82,26624\nQA 72,232,36\nQL 36,83,48,3072\nQA 36,236,32\nQB 232,82,27648\nQDN 49\nQA 76,236,36\nQG 4\nQAA 48\nQA 0,240,40\nQB 236,82,28672\n"
"QA 40,240,44\nQL 36,83,48,0\nQA 4,244,40\nQB 240,82,29696\nQZ 48,53\nQA 44,244,44\nQG 4\nQA 8,248,40\nQB 244,82,30720\nQA 48,248,44\nQA 12,252,40\nQB 248,82,31744\nQA 52,252,44\nQG 4\nQA 16,228,40\n"
"QAH 32\ns_barrier\nQB 252,81,16384\nQA 56,228,44\nQJ 16,40,185,50,2048\nQA 20,232,40\nQB 228,81,17408\nQA 60,232,44\nQG 4\nQA 24,236,40\nQB 232,81,18432\nQA 64,236,44\nQJ 20,40,185,51,2048\n"
"QA 28,240,40\nQB 236,81,19456\nQA 68,240,44\nQG 4\nQA 32,244,40\nQB 240,81,20480\nQA 72,244,44\nQJ 24,40,185,50,3072\nQA 36,248,40\nQB 244,81,21504\nQA 76,248,44\nQG 4\nQA 0,252,48\nQB 248,81,22528\n"
"QA 40,252,52\nQJ 28,40,185,51,3072\nQA 4,228,48\nQB 252,81,23552\nQDD 49\nQA 44,228,52\nQG 4\nQA 8,232,48\nQB 228,81,24576\nQA 48,232,52\nQL 36,83,48,0\nQA 12,236,48\nQB 232,81,25600\nQA 52,236,52\n"
"QG 4\nQA 16,240,48\nQB 236,81,26624\nQA 56,240,52\nQL 36,83,48,1024\nQA 20,244,48\nQB 240,81,27648\nQA 60,244,52\nQG 4\nQA 24,248,48\nQB 244,81,28672\nQA 64,248,52\nQL 36,83,48,2048\nQA 28,252,48\n"
"QB 248,81,29696\nQA 68,252,52\nQG 4\nQA 32,228,48\nQB 252,81,30720\nQA 72,228,52\nQL 36,83,48,3072\nQA 36,232,48\nQB 228,81,31744\nQGG 49\nQA 76,232,52\nQG 4\nQAA 48\nQA 0,236,56\nQB 232,81,32768\n"
"QA 40,236,60\nQL 36,83,48,0\nQA 4,240,56\nQB 236,81,33792\nQZ 48,53\nQA 44,240,60\nQG 4\nQA 8,244,56\nQB 240,81,34816\nQA 48,244,60\nQA 12,248,56\nQB 244,81,35840\nQA 52,248,60\nQG 4\nQA 16,252,56\n"
"QAH 32\ns_barrier\nQB 248,80,0\nQAA 50\nQA 56,252,60\nQJ 32,40,185,50,0\nQA 20,228,56\nQB 252,80,1024\nQAA 51\nQA 60,228,60\nQG 4\nQA 24,232,56\nQB 228,80,2048\nQA 64,232,60\nQJ 36,40,185,51,0\n"
"QA 28,236,56\nQB 232,80,3072\nQA 68,236,60\nQG 4\nQA 32,240,56\nQB 236,80,4096\nQA 72,240,60\nQJ 40,40,185,50,1024\nQA 36,244,56\nQB 240,80,5120\nQA 76,244,60\nQG 4\nQA 0,248,64\nQB 244,80,6144\n"
"QA 40,248,68\nQJ 44,40,185,51,1024\nQA 4,252,64\nQB 248,80,7168\nQGZ 49\nQA 44,252,68\nQG 4\nQA 8,228,64\nQB 252,80,8192\nQA 48,228,68\nQL 36,83,48,0\nQA 12,232,64\nQB 228,80,9216\nQA 52,232,68\n"
"QG 4\nQA 16,236,64\nQB 232,80,10240\nQA 56,236,68\nQL 36,83,48,1024\nQA 20,240,64\nQB 236,80,11264\nQA 60,240,68\nQG 4\nQA 24,244,64\nQB 240,80,12288\nQA 64,244,68\nQL 36,83,48,2048\nQA 28,248,64\n"
"QB 244,80,13312\nQA 68,248,68\nQG 4\nQA 32,252,64\nQB 248,80,14336\nQA 72,252,68\nQL 36,83,48,3072\nQA 36,228,64\nQB 252,80,15360\nQFI 49\nQA 76,228,68\nQG 4\nQAA 48\nQA 0,232,72\nQB 228,80,16384\n"
"QA 40,232,76\nQL 36,83,48,0\nQA 4,236,72\nQB 232,80,17408\nQZ 48,53\nQA 44,236,76\nQG 4\nQA 8,240,72\nQB 236,80,18432\nQA 48,240,76\nQA 12,244,72\nQB 240,80,19456\nQA 52,244,76\nQG 4\nQA 16,248,72\n"
"QAH 32\ns_barrier\nQB 244,80,20480\nQA 56,248,76\nQJ 48,40,185,50,2048\nQA 20,252,72\nQB 248,80,21504\nQA 60,252,76\nQG 4\nQA 24,228,72\nQB 252,80,22528\nQA 64,228,76\nQJ 52,40,185,51,2048\n"
"QA 28,232,72\nQB 228,80,23552\nQA 68,232,76\nQG 4\nQA 32,236,72\nQB 232,80,24576\nQA 72,236,76\nQJ 56,40,185,50,3072\nQA 36,240,72\nQB 236,80,25600\nQA 76,240,76\nQG 4\nQA 0,244,80\nQB 240,80,26624\n"
"QA 40,244,84\nQJ 60,40,185,51,3072\nQA 4,248,80\nQB 244,80,27648\nQFJ 49\nQA 44,248,84\nQG 4\nQA 8,252,80\nQB 248,80,28672\nQA 48,252,84\nQL 36,83,48,0\nQA 12,228,80\nQB 252,80,29696\nQA 52,228,84\n"
"QG 4\nQA 16,232,80\nQB 228,80,30720\nQA 56,232,84\nQL 36,83,48,1024\nQA 20,236,80\nQB 232,80,31744\nQA 60,236,84\nQG 4\nQA 24,240,80\nQB 236,80,32768\nQA 64,240,84\nQL 36,83,48,2048\nQA 28,244,80\n"
"QB 240,80,33792\nQA 68,244,84\nQG 4\nQA 32,248,80\nQB 244,80,34816\nQA 72,248,84\nQL 36,83,48,3072\nQA 36,252,80\nQB 248,80,35840\nQGH 49\nQA 76,252,84\nQG 4\nQAA 48\nQA 0,228,88\nQB 252,80,36864\n"
"QA 40,228,92\nQL 36,83,48,0\nQA 4,232,88\nQB 228,80,37888\nQZ 48,53\nQA 44,232,92\nQG 4\nQA 8,236,88\nQB 232,80,38912\nQA 48,236,92\nQA 12,240,88\nQB 236,80,39936\nQA 52,240,92\nQG 4\nQA 16,244,88\n"
"QAH 32\ns_barrier\nQB 240,80,40960\nQAA 50\nQA 56,244,92\nQJ 64,40,185,50,0\nQA 20,248,88\nQB 244,80,41984\nQAA 51\nQA 60,248,92\nQG 4\nQA 24,252,88\nQB 248,80,43008\nQA 64,252,92\nQJ 68,40,185,51,0\n"
"QA 28,228,88\nQB 252,80,44032\nQA 68,228,92\nQG 4\nQA 32,232,88\nQB 228,80,45056\nQA 72,232,92\nQJ 72,40,185,50,1024\nQA 36,236,88\nQB 232,80,46080\nQA 76,236,92\nQG 4\nQA 0,240,0\nQB 236,80,47104\n"
"QA 40,240,4\nQJ 76,40,185,51,1024\nQA 4,244,0\nQB 240,80,48128\nQA 44,244,4\nQG 4\nQA 8,248,0\nQB 244,80,49152\nQA 48,248,4\nQA 12,252,0\nQB 248,80,50176\nQA 52,252,4\nQG 4\nQA 16,228,0\n"
"QB 252,80,51200\nQA 56,228,4\nQA 20,232,0\nQB 228,80,52224\nQA 60,232,4\nQG 4\nQA 24,236,0\nQB 232,80,53248\nQA 64,236,4\nQA 28,240,0\nQB 236,80,54272\nQA 68,240,4\nQG 4\nQA 32,244,0\n"
"QB 240,80,55296\nQA 72,244,4\nQA 36,248,0\nQB 244,80,56320\nQA 76,248,4\nQG 4\nQA 0,252,8\nQB 248,80,57344\nQA 40,252,12\nQA 4,228,8\nQB 252,80,58368\nQA 44,228,12\nQG 4\nQA 8,232,8\nQB 228,80,59392\n"
"QA 48,232,12\nQA 12,236,8\nQB 232,80,60416\nQA 52,236,12\nQG 4\nQA 16,240,8\nQAH 27\ns_barrier\nQB 236,80,61440\nQA 56,240,12\nQJ 80,40,185,50,2048\nQA 20,244,8\nQB 240,80,62464\nQA 60,244,12\nQG 4\n"
"QA 24,248,8\nQB 244,80,63488\nQA 64,248,12\nQJ 84,40,185,51,2048\nQA 28,252,8\nQB 248,80,64512\nQA 68,252,12\nQG 4\nQA 32,228,8\nQB 252,81,0\nQA 72,228,12\nQJ 88,40,185,50,3072\nQA 36,232,8\n"
"QB 228,81,1024\nQA 76,232,12\nQG 4\nQA 0,236,16\nQB 232,81,2048\nQA 40,236,20\nQJ 92,40,185,51,3072\nQA 4,240,16\nQB 236,81,3072\nQA 44,240,20\nQG 4\nQA 8,244,16\nQB 240,81,4096\nQA 48,244,20\n"
"QA 12,248,16\nQB 244,81,5120\nQA 52,248,20\nQG 4\nQA 16,252,16\nQB 248,81,6144\nQA 56,252,20\nQA 20,228,16\nQB 252,81,7168\nQA 60,228,20\nQG 4\nQA 24,232,16\nQB 228,81,8192\nQA 64,232,20\n"
"QA 28,236,16\nQB 232,81,9216\nQA 68,236,20\nQG 4\nQA 32,240,16\nQB 236,81,10240\nQA 72,240,20\nQA 36,244,16\nQB 240,81,11264\nQA 76,244,20\nQG 4\nQA 0,248,24\nQB 244,81,12288\nQA 40,248,28\n"
"QA 4,252,24\nQB 248,81,13312\nQA 44,252,28\nQG 4\nQA 8,228,24\nQB 252,81,14336\nQA 48,228,28\nQA 12,232,24\nQB 228,81,15360\nQA 52,232,28\nQG 4\nQA 16,236,24\nQAH 22\ns_barrier\nQB 232,81,36864\n"
"QA 56,236,28\nQA 20,240,24\nQB 236,81,37888\nQA 60,240,28\nQG 4\nQA 24,244,24\nQB 240,81,38912\nQA 64,244,28\nQA 28,248,24\nQB 244,81,39936\nQA 68,248,28\nQG 4\nQA 32,252,24\nQB 248,81,40960\n"
"QA 72,252,28\nQA 36,228,24\nQB 252,81,41984\nQA 76,228,28\nQG 4\nQA 0,232,32\nQB 228,81,43008\nQA 40,232,36\nQA 4,236,32\nQB 232,81,44032\nQA 44,236,36\nQG 4\nQA 8,240,32\nQB 236,81,45056\n"
"QA 48,240,36\nQA 12,244,32\nQB 240,81,46080\nQA 52,244,36\nQG 4\nQA 16,248,32\nQB 244,81,47104\nQA 56,248,36\nQA 20,252,32\nQB 248,81,48128\nQA 60,252,36\nQG 4\nQA 24,228,32\nQB 252,81,49152\n"
"QA 64,228,36\nQA 28,232,32\nQB 228,81,50176\nQA 68,232,36\nQG 4\nQA 32,236,32\nQB 232,81,51200\nQA 72,236,36\nQA 36,240,32\nQB 236,81,52224\nQA 76,240,36\nQG 4\nQA 0,244,40\nQB 240,81,53248\n"
"QA 40,244,44\nQA 4,248,40\nQB 244,81,54272\nQA 44,248,44\nQG 4\nQA 8,252,40\nQB 248,81,55296\nQA 48,252,44\nQA 12,228,40\nQB 252,81,56320\nQA 52,228,44\nQG 4\nQA 16,232,40\nQAH 0\ns_barrier\n"
"QB 228,81,57344\nQA 56,232,44\nQA 20,236,40\nQB 232,81,58368\nQA 60,236,44\nQG 4\nQA 24,240,40\nQB 236,81,59392\nQA 64,240,44\nQA 28,244,40\nQB 240,81,60416\nQA 68,244,44\nQG 4\nQA 32,248,40\n"
"QB 244,81,61440\nQA 72,248,44\nQA 36,252,40\nQB 248,82,2048\nQA 76,252,44\nQG 4\nQA 0,228,48\nQB 252,82,3072\nQA 40,228,52\nQA 4,232,48\nQB 228,82,4096\nQA 44,232,52\nQG 4\nQA 8,236,48\n"
"QB 232,82,5120\nQA 48,236,52\nQA 12,240,48\nQB 236,82,6144\nQA 52,240,52\nQG 4\nQA 16,244,48\nQB 240,82,12288\nQA 56,244,52\nQA 0,248,56\nQB 244,82,13312\nQA 40,248,60\nQG 4\nQA 4,252,56\n"
"QB 248,82,14336\nQA 44,252,60\nQA 8,228,56\nQB 252,82,15360\nQA 48,228,60\nQG 4\nQA 12,232,56\nQB 228,82,16384\nQA 52,232,60\nQA 16,236,56\nQB 232,82,22528\nQA 56,236,60\nQG 4\nQA 0,240,64\n"
"QB 236,82,23552\nQA 40,240,68\nQA 4,244,64\nQB 240,82,24576\nQA 44,244,68\nQG 4\nQA 8,248,64\nQB 244,82,25600\nQA 48,248,68\nQA 12,252,64\nQB 248,82,26624\nQA 52,252,68\nQG 4\nQA 16,228,64\n"
"QB 252,81,16384\nQA 56,228,68\nQA 0,232,72\nQB 228,81,17408\nQA 40,232,76\nQG 4\nQA 4,236,72\nQB 232,81,18432\nQA 44,236,76\nQA 8,240,72\nQB 236,81,19456\nQA 48,240,76\nQG 4\nQA 12,244,72\n"
"QB 240,81,20480\nQA 52,244,76\nQA 16,248,72\nQB 244,81,26624\nQA 56,248,76\nQG 4\nQA 0,252,80\nQB 248,81,27648\nQA 40,252,84\nQA 4,228,80\nQB 252,81,28672\nQA 44,228,84\nQG 4\nQA 8,232,80\n"
"QB 228,81,29696\nQA 48,232,84\nQA 12,236,80\nQB 232,81,30720\nQA 52,236,84\nQG 4\nQA 16,240,80\nQB 236,81,62464\nQA 56,240,84\nQA 0,244,88\nQB 240,81,63488\nQA 40,244,92\nQG 4\nQA 4,248,88\n"
"QB 244,81,64512\nQA 44,248,92\nQA 8,252,88\nQB 248,82,0\nQA 48,252,92\nQG 4\nQA 12,228,88\nQB 252,82,1024\nQA 52,228,92\nQA 16,232,88\nQB 228,82,7168\nQA 56,232,92\nQG 4\nQA 20,236,48\n"
"QB 232,82,8192\nQA 60,236,52\nQA 24,240,48\nQB 236,82,9216\nQA 64,240,52\nQG 4\nQA 28,244,48\nQB 240,82,10240\nQF 192,0,1\nQA 68,244,52\nQF 193,2,3\nQF 194,4,5\nQA 32,248,48\nQB 244,82,11264\n"
"QF 195,6,7\nQA 72,248,52\nQG 4\nQA 36,252,48\nQB 248,82,17408\nQA 76,252,52\nQAB 192,44,185,52,0\nQA 20,228,56\nQB 252,82,18432\nQF 196,8,9\nQA 60,228,60\nQG 4\nQF 197,10,11\nQF 198,12,13\n"
"QA 24,232,56\nQB 228,82,19456\nQF 199,14,15\nQA 64,232,60\nQAB 196,44,185,52,1024\nQA 28,236,56\nQB 232,82,20480\nQF 200,40,41\nQA 68,236,60\nQG 4\nQF 201,42,43\nQF 202,44,45\nQA 32,240,56\n"
"QB 236,82,21504\nQF 203,46,47\nQA 72,240,60\nQAB 200,44,185,52,2048\nQA 36,244,56\nQB 240,82,27648\nQF 204,48,49\nQA 76,244,60\nQG 4\nQF 205,50,51\nQF 206,52,53\nQA 20,248,64\nQB 244,82,28672\n"
"QF 207,54,55\nQA 60,248,68\nQAB 204,44,185,52,3072\nQA 24,252,64\nQB 248,82,29696\nQF 208,16,17\nQA 64,252,68\nQG 4\nQF 209,18,19\nQF 210,56,57\nQA 28,228,64\nQB 252,82,30720\nQF 211,58,59\n"
"QA 68,228,68\nQAA 52\nQA 32,232,64\nQB 228,82,31744\nQA 72,232,68\nQG 4\nQA 36,236,64\nQB 232,81,21504\nQA 76,236,68\nQAB 208,44,185,52,0\nQA 20,240,72\nQB 236,81,22528\nQA 60,240,76\nQG 4\n"
"QA 24,244,72\nQB 240,81,23552\nQA 64,244,76\nQA 28,248,72\nQB 244,81,24576\nQA 68,248,76\nQG 4\nQA 32,252,72\nQB 248,81,25600\nQA 72,252,76\nQA 36,228,72\nQB 252,81,31744\nQA 76,228,76\nQG 4\n"
"QA 20,232,80\nQB 228,81,32768\nQA 60,232,84\nQA 24,236,80\nQB 232,81,33792\nQA 64,236,84\nQG 4\nQA 28,240,80\nQB 236,81,34816\nQA 68,240,84\nQA 32,244,80\nQB 240,81,35840\nQA 72,244,84\nQG 4\n"
"QA 36,248,80\nQA 76,248,84\nQA 20,252,88\nQA 60,252,92\nQG 3\nQA 24,228,88\nQA 64,228,92\nQG 2\nQA 28,232,88\nQA 68,232,92\nQG 1\nQA 32,236,88\nQA 72,236,92\nQG 0\nQA 36,240,88\nQA 76,240,92\n"
"QF 212,20,21\nQF 213,22,23\nQF 214,24,25\nQF 215,26,27\nQAB 212,44,185,52,1024\nQF 216,28,29\nQF 217,30,31\nQF 218,32,33\nQF 219,34,35\nQAB 216,44,185,52,2048\nQF 220,60,61\nQF 221,62,63\n"
"QF 222,64,65\nQF 223,66,67\nQAB 220,44,185,52,3072\nQF 224,68,69\nQF 225,70,71\nQF 226,72,73\nQF 227,74,75\nQAA 52\nQAB 224,44,185,52,0\nQF 228,36,37\nQF 229,38,39\nQF 230,76,77\nQF 231,78,79\n"
"QAB 228,44,185,52,1024\nQAH 0\ns_barrier\nQCR 88\ns_bfe_u32 s99, %1, 65536\ns_lshl_b32 s99, s99, 5\ns_bfe_u32 s100, %1, 327683\nQZ 99,100\nQBU 99\nQBP 99\nQZ 99,96\nQEI \nQEJ 92,88,99\nQEK \n"
"s_branch L_done\nL_roleB:\nQDO \nQCG 86\nL_pc26:\ns_lshr_b32 s88, %1, 3\nQBH 88\ns_add_u32 s88, s88, %0\ns_mul_i32 s88, s88, ((L_done-L_roleB2-1024)/127+63)&-64\n"
"s_min_u32 s88, s88, L_done-L_roleB2-1024\ns_add_u32 s88, s88, L_roleB2-L_pc26\nQZ 86,88\nQAX 87\nQCL 92,185\nQFG 90,86,92\nL_roleB2:\nQCV 85,185\nQCW 85\ns_mov_b32 s99, 24576\nQEB 92,84,85,99\n"
"QJ 0,40,185,50,0\nQJ 4,40,185,51,0\nQJ 8,40,185,50,1024\nQJ 12,40,185,51,1024\nQJ 16,40,185,50,2048\nQJ 20,40,185,51,2048\nQJ 24,40,185,50,3072\nQJ 28,40,185,51,3072\nQAA 50\nQJ 32,40,185,50,0\n"
"QAA 51\nQJ 36,40,185,51,0\nQJ 40,40,185,50,1024\nQJ 44,40,185,51,1024\nQJ 48,40,185,50,2048\nQJ 52,40,185,51,2048\nQJ 56,40,185,50,3072\nQJ 60,40,185,51,3072\nQAY 96,97\nQF 97,98,99\nQF 98,100,101\n"
"QF 99,102,103\nQAB 96,64,185,72,0\nQF 100,104,105\nQF 101,106,107\nQF 102,108,109\nQF 103,110,111\nQAB 100,64,185,72,1024\nQF 104,112,113\nQF 105,114,115\nQF 106,116,117\nQF 107,118,119\n"
"QAB 104,64,185,72,2048\nQF 108,120,121\nQF 109,122,123\nQFM 108,64,185,73\nQAA 50\nQJ 64,40,185,50,0\nQAF 124,192,176\nQAF 126,194,176\nQAF 128,196,176\nQAF 130,198,176\nQAF 132,200,176\n"
"QAF 134,202,176\nQAF 136,204,176\nQAU 124,176\nQAU 126,176\nQAU 128,176\nQAU 130,176\nQAU 132,176\nQAU 134,176\nQAU 136,176\nQAP 220,124\nQAP 222,126\nQAP 224,128\nQAP 226,130\nQAP 228,132\nQAA 51\n"
"QJ 68,40,185,51,0\nQAP 230,134\nQAP 232,136\nQAV 220,164\nQAV 222,164\nQAV 224,164\nQAV 226,164\nQAV 228,164\nQAV 230,164\nQAV 232,164\nQX 220,124\nQX 222,126\nQX 224,128\nQX 226,130\nQX 228,132\n"
"QX 230,134\nQX 232,136\nQW 220\nQW 221\nQW 222\nQJ 72,40,185,50,1024\nQW 223\nQW 224\nQW 225\nQW 226\nQW 227\nQW 228\nQW 229\nQW 230\nQW 231\nQW 232\nQW 233\nQAK 220\nQAK 222\nQAK 224\nQAK 226\n"
"QAK 228\nQAK 230\nQAK 232\nQV 220\nQJ 76,40,185,51,1024\nQV 221\nQV 222\nQV 223\nQV 224\nQV 225\nQV 226\nQV 227\nQV 228\nQV 229\nQV 230\nQV 231\nQV 232\nQV 233\nQX 124,220\nQX 126,222\nQX 128,224\n"
"QX 130,226\nQX 132,228\nQX 134,230\nQJ 80,40,185,50,2048\nQX 136,232\nQAF 138,206,176\nQAF 140,208,176\nQAF 142,210,176\nQAF 144,212,176\nQAF 146,214,176\nQAF 148,216,176\nQAF 150,218,176\n"
"QAU 138,176\nQAU 140,176\nQAU 142,176\nQAU 144,176\nQAU 146,176\nQAU 148,176\nQAU 150,176\nQAP 234,138\nQAP 236,140\nQAP 238,142\nQAP 240,144\nQJ 84,40,185,51,2048\nQAP 242,146\nQAP 244,148\n"
"QAP 246,150\nQAV 234,164\nQAV 236,164\nQAV 238,164\nQAV 240,164\nQAV 242,164\nQAV 244,164\nQAV 246,164\nQX 234,138\nQX 236,140\nQX 238,142\nQX 240,144\nQX 242,146\nQX 244,148\nQX 246,150\nQW 234\n"
"QW 235\nQJ 88,40,185,50,3072\nQW 236\nQW 237\nQW 238\nQW 239\nQW 240\nQW 241\nQW 242\nQW 243\nQW 244\nQW 245\nQW 246\nQW 247\nQAK 234\nQAK 236\nQAK 238\nQAK 240\nQAK 242\nQAK 244\nQAK 246\n"
"QJ 92,40,185,51,3072\nQV 234\nQV 235\nQV 236\nQV 237\nQV 238\nQV 239\nQV 240\nQV 241\nQV 242\nQV 243\nQV 244\nQV 245\nQV 246\nQV 247\nQX 138,234\nQX 140,236\nQX 142,238\nQX 144,240\nQX 146,242\n"
"QX 148,244\nQX 150,246\nQAY 124,125\nQF 125,126,127\nQF 126,128,129\nQF 127,130,131\nQAB 124,64,170,72,0\nQF 128,132,133\nQF 129,134,135\nQF 130,136,137\nQF 131,138,139\nQAB 128,64,170,72,1024\n"
"QF 132,140,141\nQF 133,142,143\nQF 134,144,145\nQF 135,146,147\nQAB 132,64,170,72,2048\nQF 136,148,149\nQF 137,150,151\nQFM 136,64,170,73\nQAH 24\ns_barrier\nQB 228,80,0\nQB 232,80,1024\n"
"QB 236,80,2048\nQB 240,80,3072\nQB 244,80,4096\nQB 248,80,5120\nQG 4\nQAE 0,228,0\nQB 252,80,6144\ns_mov_b64 s[56:57], %2\nQAE 20,228,4\ns_add_u32 s56, s56, 72089600\nQAX 57\ns_mov_b32 s58, 2293760\n"
"QBA 59\nQAE 4,232,0\nQB 228,80,7168\ns_mov_b64 s[60:61], %2\nQAE 24,232,4\nQG 4\ns_add_u32 s60, s60, 60817408\nQAX 61\ns_mov_b32 s62, 7340032\nQAE 8,236,0\nQB 232,80,8192\nQBA 63\nQAE 28,236,4\n"
"s_mov_b64 s[64:65], %2\ns_add_u32 s64, s64, 58195968\nQAX 65\nQDL 66\nQAE 12,240,0\nQB 236,80,9216\nQBA 67\nQAE 32,240,4\nQG 4\ns_mov_b64 s[88:89], %3\nQDL 90\nQAE 16,244,0\nQB 240,80,10240\nQBA 91\n"
"QAE 36,244,4\ns_mul_i32 s68, s55, 286720\nQBM 89,49,185\nv_add_u32 v243, 4096, v185\nQA 0,248,8\nQB 244,80,11264\nQEG 74,54\nQA 20,248,12\nQG 4\ns_add_u32 s74, s74, %0\ns_mul_i32 s70, s74, 114688\n"
"QA 4,252,8\nQB 248,80,12288\ns_add_u32 s71, s70, 57344\nQA 24,252,12\nQCJ 83,55\ns_mul_i32 s83, s83, 5120\ns_mul_i32 s72, s74, 40960\nQA 8,228,8\nQB 252,80,13312\nQZ 72,83\nQA 28,228,12\nQG 4\n"
"QBX 84,55\nQFE 83,84\nQA 12,232,8\nQB 228,80,14336\nQBT 73,72,83\nQA 32,232,12\nQAA 73\nQFZ 83,84\nQZ 72,83\nQA 16,236,8\nQB 232,80,15360\ns_mul_i32 s83, s74, 40960\nQA 36,236,12\nQG 4\n"
"s_mul_i32 s84, s55, 160\nQBT 87,83,84\nQA 0,240,16\nQB 236,80,16384\nQEL 86,49\nQA 20,240,20\nQDU 136,185\nv_add_u32 v136, 0, v136\nQA 4,244,16\nQB 240,80,17408\nv_mul_u32_u24 v137, 6554, v136\n"
"QA 24,244,20\nQG 4\nQDA 137\nQDT 136\nQA 8,248,16\nQB 244,80,18432\nv_mul_u32_u24 v232, 1120, v137\nQA 28,248,20\nQDM 232,136\nQDU 136,185\nQA 12,252,16\nQB 248,80,19456\nv_add_u32 v136, 64, v136\n"
"QA 32,252,20\nQG 4\nv_mul_u32_u24 v137, 6554, v136\nQDA 137\nQA 16,228,16\nQAH 16\ns_barrier\nQB 252,80,20480\nQAA 50\nQDT 136\nQA 36,228,20\nQJ 96,40,185,50,0\nv_mul_u32_u24 v233, 1120, v137\n"
"QA 0,232,24\nQB 228,80,21504\nQG 4\nQAA 51\nQDM 233,136\nQA 20,232,28\nQJ 100,40,185,51,0\nQDU 136,185\nQA 4,236,24\nQB 232,80,22528\nv_add_u32 v136, 128, v136\nQA 24,236,28\nQJ 104,40,185,50,1024\n"
"v_mul_u32_u24 v137, 6554, v136\nQA 8,240,24\nQB 236,80,23552\nQG 4\nQDA 137\nQA 28,240,28\nQJ 108,40,185,51,1024\nQDT 136\nQA 12,244,24\nQB 240,80,24576\nv_mul_u32_u24 v234, 1120, v137\nQA 32,244,28\n"
"QJ 112,40,185,50,2048\nQDM 234,136\nQA 16,248,24\nQB 244,80,25600\nQG 4\nQDU 136,185\nQA 36,248,28\nQJ 116,40,185,51,2048\nv_add_u32 v136, 192, v136\nQA 0,252,32\nQB 248,80,26624\n"
"v_mul_u32_u24 v137, 6554, v136\nQA 20,252,36\nQJ 120,40,185,50,3072\nQDA 137\nQA 4,228,32\nQB 252,80,27648\nQDT 136\nQA 24,228,36\nQG 4\nv_mul_u32_u24 v235, 1120, v137\nQDM 235,136\nQA 8,232,32\n"
"QB 228,80,28672\nQDU 136,185\nQA 28,232,36\nQJ 124,40,185,51,3072\nv_add_u32 v136, 256, v136\nQA 12,236,32\nQB 232,80,29696\nQFJ 49\nv_mul_u32_u24 v137, 6554, v136\nQA 32,236,36\nQG 4\nQDA 137\n"
"QDT 136\nQA 16,240,32\nQB 236,80,30720\nv_mul_u32_u24 v236, 1120, v137\nQA 36,240,36\nQL 36,83,48,0\nQDM 236,136\nQA 0,244,40\nQB 240,80,31744\nQBM 237,86,185\nQA 20,244,44\nQG 4\n"
"v_bfe_u32 v242, v185, 4, 4\nv_mul_u32_u24 v242, 160, v242\nQA 4,248,40\nQB 244,80,32768\nv_lshrrev_b32 v136, 8, v185\nQA 24,248,44\nQL 36,83,48,1024\nQCW 136\nQA 8,252,40\nQB 248,80,33792\n"
"v_add3_u32 v242, v242, v136, s86\nQA 28,252,44\nQG 4\nQA 12,228,40\nQB 252,80,34816\nQA 32,228,44\nQL 36,83,48,2048\nQA 16,232,40\nQB 228,80,35840\nQA 36,232,44\nQG 4\nQA 0,236,48\nQB 232,80,36864\n"
"QA 20,236,52\nQL 36,83,48,3072\nQA 4,240,48\nQB 236,80,37888\nQGH 49\nQA 24,240,52\nQG 4\nQAA 48\nQA 8,244,48\nQB 240,80,38912\nQA 28,244,52\nQL 36,83,48,0\nQA 12,248,48\nQB 244,80,39936\nQZ 48,53\n"
"QA 32,248,52\nQG 4\nQA 16,252,48\nQAH 13\ns_barrier\nQCR 88\ns_bfe_u32 s99, %1, 131073\nQBH 99\ns_bfe_u32 s100, %1, 131078\nQZ 99,100\nQBU 99\nQEP 99\nQZ 99,96\nQEI \nQEJ 92,88,99\nQEK \nQFF 84\n"
"QBD \ns_cbranch_vccnz L_ps27\nL_pr27:\nQB 248,80,40960\nQAA 50\nQA 36,252,52\nQJ 0,40,185,50,0\nQA 0,228,56\nQB 252,80,41984\nQG 4\nQAA 51\nQA 20,228,60\nQJ 4,40,185,51,0\nQA 4,232,56\n"
"QB 228,80,43008\nQA 24,232,60\nQJ 8,40,185,50,1024\nQA 8,236,56\nQB 232,80,44032\nQG 4\nQA 28,236,60\nQJ 12,40,185,51,1024\nQA 12,240,56\nQB 236,80,45056\nQA 32,240,60\nQJ 16,40,185,50,2048\n"
"QA 16,244,56\nQB 240,80,46080\nQG 4\nQA 36,244,60\nQJ 20,40,185,51,2048\nQA 0,248,64\nQB 244,80,47104\nQA 20,248,68\nQJ 24,40,185,50,3072\nQA 4,252,64\nQB 248,80,48128\nQA 24,252,68\nQG 4\n"
"QA 8,228,64\nQB 252,80,49152\nQA 28,228,68\nQJ 28,40,185,51,3072\nQA 12,232,64\nQB 228,80,50176\nQBV 49\nQA 32,232,68\nQG 4\nQA 16,236,64\nQB 232,80,51200\nQA 36,236,68\nQL 56,89,68,0\nQA 0,240,72\n"
"QB 236,80,52224\nQA 20,240,76\nQG 4\nQA 4,244,72\nQB 240,80,53248\nQA 24,244,76\nQL 56,89,68,1024\nQA 8,248,72\nQB 244,80,54272\nQA 28,248,76\nQG 4\nQA 12,252,72\nQB 248,80,55296\nQA 32,252,76\n"
"QL 56,89,68,2048\nQA 16,228,72\nQB 252,80,56320\nQA 36,228,76\nQG 4\nQA 0,232,80\nQB 228,80,57344\nQA 20,232,84\nQL 56,89,68,3072\nQA 4,236,80\nQB 232,80,58368\nQCT 49\nQA 24,236,84\nQG 4\nQAA 68\n"
"QA 8,240,80\nQB 236,80,59392\nQA 28,240,84\nQL 56,89,68,0\nQA 12,244,80\nQB 240,80,60416\nQBP 68\nQA 32,244,84\nQG 4\nQA 16,248,80\nQAH 19\ns_barrier\nQB 244,80,61440\nQAA 50\nQA 36,248,84\n"
"QJ 32,40,185,50,0\nQA 0,252,88\nQB 248,80,62464\nQG 4\nQAA 51\nQA 20,252,92\nQJ 36,40,185,51,0\nQA 4,228,88\nQB 252,80,63488\nQA 24,228,92\nQJ 40,40,185,50,1024\nQA 8,232,88\nQB 228,80,64512\nQG 4\n"
"QA 28,232,92\nQJ 44,40,185,51,1024\nQA 12,236,88\nQB 232,81,0\nQA 32,236,92\nQJ 48,40,185,50,2048\nQA 16,240,88\nQB 236,81,1024\nQG 4\nQA 36,240,92\nQJ 52,40,185,51,2048\nQA 0,244,96\nQB 240,81,2048\n"
"QA 20,244,100\nQJ 56,40,185,50,3072\nQA 4,248,96\nQB 244,81,3072\nQG 4\nQA 24,248,100\nQJ 60,40,185,51,3072\nQA 8,252,96\nQB 248,81,4096\nQEX 49\nQA 28,252,100\nQL 56,89,68,0\nQA 12,228,96\n"
"QB 252,81,5120\nQG 4\nQA 32,228,100\nQL 56,89,68,1024\nQA 16,232,96\nQB 228,81,6144\nQA 36,232,100\nQL 56,89,68,2048\nQA 0,236,104\nQB 232,81,7168\nQG 4\nQA 20,236,108\nQL 56,89,68,3072\n"
"QA 4,240,104\nQB 236,81,8192\nQEM 49\nQA 24,240,108\nQAA 68\nQA 8,244,104\nQB 240,81,9216\nQG 4\nQA 28,244,108\nQL 56,89,68,0\nQA 12,248,104\nQB 244,81,10240\nQBP 68\nQA 32,248,108\n"
"QAW 40,64,185,72,0\nQA 16,252,104\nQB 248,81,11264\nQG 4\nQA 36,252,108\nQAW 44,64,185,72,1024\nQA 0,228,112\nQB 252,81,12288\nQA 20,228,116\nQDI 48,64,185,73\nQA 4,232,112\nQB 228,81,13312\nQG 4\n"
"s_add_u32 s72, s72, 20480\ns_add_u32 s73, s73, 20480\nQA 24,232,116\nQAW 50,64,185,72,0\nQA 8,236,112\nQB 232,81,14336\nQA 28,236,116\nQAW 54,64,185,72,1024\nQA 12,240,112\nQB 236,81,15360\nQG 4\n"
"QA 32,240,116\nQDI 58,64,185,73\nQA 16,244,112\nQAH 24\ns_barrier\nQB 240,81,36864\nQAA 50\nQA 36,244,116\nQJ 64,40,185,50,0\nQA 0,248,120\nQB 244,81,37888\nQG 4\nQAA 51\nQA 20,248,124\n"
"QJ 68,40,185,51,0\nQA 4,252,120\nQB 248,81,38912\nQA 24,252,124\nQJ 72,40,185,50,1024\nQA 8,228,120\nQB 252,81,39936\nQG 4\nQA 28,228,124\nQJ 76,40,185,51,1024\nQA 12,232,120\nQB 228,81,40960\n"
"QA 32,232,124\nQJ 80,40,185,50,2048\nQA 16,236,120\nQB 232,81,41984\nQG 4\nQA 36,236,124\nQJ 84,40,185,51,2048\nQA 0,240,0\nQB 236,81,43008\nQA 20,240,4\nQJ 88,40,185,50,3072\nQA 4,244,0\n"
"QB 240,81,44032\nQA 24,244,4\nQG 4\nQA 8,248,0\nQB 244,81,45056\nQA 28,248,4\nQJ 92,40,185,51,3072\nQA 12,252,0\nQB 248,81,46080\nQEN 49\nQA 32,252,4\nQG 4\nQA 16,228,0\nQB 252,81,47104\nQA 36,228,4\n"
"QL 56,89,68,0\nQA 0,232,8\nQB 228,81,48128\nQA 20,232,12\nQG 4\nQA 4,236,8\nQB 232,81,49152\nQA 24,236,12\nQL 56,89,68,1024\nQA 8,240,8\nQB 236,81,50176\nQA 28,240,12\nQG 4\nQA 12,244,8\n"
"QB 240,81,51200\nQA 32,244,12\nQL 56,89,68,2048\nQA 16,248,8\nQB 244,81,52224\nQA 36,248,12\nQG 4\nQA 0,252,16\nQB 248,81,53248\nQA 20,252,20\nQL 56,89,68,3072\nQA 4,228,16\nQB 252,81,54272\n"
"s_add_u32 m0, s49, 45056\nQA 24,228,20\nQG 4\nQAA 68\nQA 8,232,16\nQB 228,81,55296\nQA 28,232,20\nQL 56,89,68,0\nQA 12,236,16\nQB 232,81,56320\nQBP 68\nQA 32,236,20\nQG 4\nQA 16,240,16\nQAH 24\n"
"s_barrier\ns_bfe_u32 s99, %1, 131072\nQBH 99\ns_bfe_u32 s100, %1, 131078\nQZ 99,100\nQBU 99\nQEP 99\nQCZ 92,84,99\nQB 236,81,57344\nQAA 50\nQA 36,240,20\nQJ 96,40,185,50,0\nQA 0,244,24\n"
"QB 240,81,58368\nQG 4\nQAA 51\nQA 20,244,28\nQJ 100,40,185,51,0\nQA 4,248,24\nQB 244,81,59392\nQA 24,248,28\nQJ 104,40,185,50,1024\nQA 8,252,24\nQB 248,81,60416\nQG 4\nQA 28,252,28\n"
"QJ 108,40,185,51,1024\nQA 12,228,24\nQB 252,81,61440\nQA 32,228,28\nQJ 112,40,185,50,2048\nQA 16,232,24\nQB 228,81,62464\nQG 4\nQA 36,232,28\nQJ 116,40,185,51,2048\nQA 0,236,32\nQB 232,81,63488\n"
"QA 20,236,36\nQJ 120,40,185,50,3072\nQA 4,240,32\nQB 236,81,64512\nQA 24,240,36\nQG 4\nQA 8,244,32\nQB 240,82,0\nQA 28,244,36\nQJ 124,40,185,51,3072\nQA 12,248,32\nQB 244,82,1024\nQFL 49\n"
"QA 32,248,36\nQG 4\nQA 16,252,32\nQB 248,82,2048\nQA 36,252,36\nQL 56,89,68,0\nQA 0,228,40\nQB 252,82,3072\nQA 20,228,44\nQG 4\nQA 4,232,40\nQB 228,82,4096\nQA 24,232,44\nQL 56,89,68,1024\n"
"QA 8,236,40\nQB 232,82,5120\nQA 28,236,44\nQG 4\nQA 12,240,40\nQB 236,82,6144\nQA 32,240,44\nQL 56,89,68,2048\nQA 16,244,40\nQB 240,82,7168\nQA 36,244,44\nQG 4\nQA 0,248,48\nQB 244,82,8192\n"
"QA 20,248,52\nQL 56,89,68,3072\nQA 4,252,48\nQB 248,82,9216\nQDY 49\nQA 24,252,52\nQG 4\nQAA 68\nQA 8,228,48\nQB 252,82,10240\nQA 28,228,52\nQL 56,89,68,0\nQA 12,232,48\nQB 228,82,11264\nQBP 68\n"
"QA 32,232,52\nQG 4\nQA 16,236,48\nQAH 19\ns_barrier\nQAH 13\nQFF 84\nQBD \ns_cbranch_vccnz L_ps28\nL_pr28:\nQB 232,82,12288\nQAH 13\nQA 36,236,52\nQJ 0,60,185,70,0\nQU 96,40\nQA 0,240,56\n"
"QB 236,82,13312\nQG 4\nQY 97,40\nQA 20,240,60\nQJ 4,60,185,71,0\nQU 98,41\nQA 4,244,56\nQB 240,82,14336\nQY 99,41\nQA 24,244,60\nQJ 8,60,185,70,1024\nQU 100,42\nQA 8,248,56\nQB 244,82,15360\nQG 4\n"
"QY 101,42\nQA 28,248,60\nQJ 12,60,185,71,1024\nQU 102,43\nQA 12,252,56\nQB 248,82,16384\nQY 103,43\nQA 32,252,60\nQJ 16,60,185,70,2048\nQU 104,44\nQA 16,228,56\nQB 252,82,17408\nQG 4\nQY 105,44\n"
"QA 36,228,60\nQJ 20,60,185,71,2048\nQU 106,45\nQA 0,232,64\nQB 228,82,18432\nQY 107,45\nQA 20,232,68\nQJ 24,60,185,70,3072\nQU 108,46\nQA 4,236,64\nQB 232,82,19456\nQY 109,46\nQA 24,236,68\nQG 4\n"
"QU 110,47\nQY 111,47\nQA 8,240,64\nQB 236,82,20480\nQU 112,48\nQA 28,240,68\nQJ 28,60,185,71,3072\nQY 113,48\nQA 12,244,64\nQB 240,82,21504\nQGF 49\nQU 114,49\nQA 32,244,68\nQG 4\nQY 115,49\n"
"QU 116,50\nQA 16,248,64\nQB 244,82,22528\nQY 117,50\nQA 36,248,68\nQL 56,89,68,0\nQU 118,51\nQA 0,252,72\nQB 248,82,23552\nQY 119,51\nQA 20,252,76\nQG 4\nQU 120,52\nQY 121,52\nQA 4,228,72\n"
"QB 252,82,24576\nQU 122,53\nQA 24,228,76\nQL 56,89,68,1024\nQY 123,53\nQA 8,232,72\nQB 228,82,25600\nQU 124,54\nQA 28,232,76\nQG 4\nQY 125,54\nQU 126,55\nQA 12,236,72\nQB 232,82,26624\nQY 127,55\n"
"QA 32,236,76\nQL 56,89,68,2048\nQU 128,56\nQA 16,240,72\nQB 236,82,27648\nQY 129,56\nQA 36,240,76\nQG 4\nQU 130,57\nQY 131,57\nQA 0,244,80\nQB 240,82,28672\nQU 132,58\nQA 20,244,84\nQL 56,89,68,3072\n"
"QY 133,58\nQA 4,248,80\nQB 244,82,29696\nQDN 49\nQU 134,59\nQA 24,248,84\nQG 4\nQAA 68\nQY 135,59\nQA 8,252,80\nQB 248,82,30720\nQA 28,252,84\nQL 56,89,68,0\nQA 12,228,80\nQB 252,82,31744\nQBP 68\n"
"QA 32,228,84\nQG 4\nQA 16,232,80\nQAH 18\ns_barrier\nQB 228,81,16384\nQAA 70\nQA 36,232,84\nQJ 32,60,185,70,0\nQA 0,236,88\nQB 232,81,17408\nQG 4\nQAA 71\nQA 20,236,92\nQJ 36,60,185,71,0\n"
"QA 4,240,88\nQB 236,81,18432\nQA 24,240,92\nQJ 40,60,185,70,1024\nQA 8,244,88\nQB 240,81,19456\nQG 4\nQA 28,244,92\nQJ 44,60,185,71,1024\nQA 12,248,88\nQB 244,81,20480\nQA 32,248,92\n"
"QJ 48,60,185,70,2048\nQA 16,252,88\nQB 248,81,21504\nQG 4\nQA 36,252,92\nQJ 52,60,185,71,2048\nQA 0,228,96\nQB 252,81,22528\nQA 20,228,100\nQJ 56,60,185,70,3072\nQA 4,232,96\nQB 228,81,23552\n"
"QA 24,232,100\nQG 4\nQA 8,236,96\nQB 232,81,24576\nQA 28,236,100\nQJ 60,60,185,71,3072\nQA 12,240,96\nQB 236,81,25600\nQDD 49\nQA 32,240,100\nQG 4\nQA 16,244,96\nQB 240,81,26624\nQA 36,244,100\n"
"QL 56,89,68,0\nQA 0,248,104\nQB 244,81,27648\nQA 20,248,108\nQG 4\nQA 4,252,104\nQB 248,81,28672\nQA 24,252,108\nQL 56,89,68,1024\nQA 8,228,104\nQB 252,81,29696\nQA 28,228,108\nQG 4\nQA 12,232,104\n"
"QB 228,81,30720\nQA 32,232,108\nQL 56,89,68,2048\nQA 16,236,104\nQB 232,81,31744\nQA 36,236,108\nQG 4\nQA 0,240,112\nQB 236,81,32768\nQA 20,240,116\nQL 56,89,68,3072\nQA 4,244,112\nQB 240,81,33792\n"
"QGG 49\nQA 24,244,116\nQG 4\nQAA 68\nQA 8,248,112\nQB 244,81,34816\nQA 28,248,116\nQL 56,89,68,0\nQA 12,252,112\nQB 248,81,35840\nQBP 68\nQA 32,252,116\nQG 4\nQA 16,228,112\nQA 36,228,116\n"
"QA 0,232,120\nQA 20,232,124\nQG 3\nQA 4,236,120\nQA 24,236,124\nQG 2\nQA 8,240,120\nQA 28,240,124\nQG 1\nQA 12,244,120\nQA 32,244,124\nQG 0\nQA 16,248,120\nQA 36,248,124\nQAH 18\ns_barrier\n"
"QB 228,80,0\nQB 232,80,1024\nQB 236,80,2048\nQB 240,80,3072\nQB 244,80,4096\nQB 248,80,5120\nQG 4\nQA 96,228,0\nQB 252,80,6144\nQGZ 49\nQA 116,228,4\nQL 56,89,68,0\nQA 100,232,0\nQB 228,80,7168\n"
"QG 4\nQA 120,232,4\nQL 56,89,68,1024\nQA 104,236,0\nQB 232,80,8192\nQA 124,236,4\nQL 56,89,68,2048\nQA 108,240,0\nQB 236,80,9216\nQG 4\nQA 128,240,4\nQL 56,89,68,3072\nQA 112,244,0\nQB 240,80,10240\n"
"QFI 49\nQA 132,244,4\nQAA 68\nQA 96,248,8\nQB 244,80,11264\nQG 4\nQA 116,248,12\nQL 56,89,68,0\nQA 100,252,8\nQB 248,80,12288\nQBP 68\nQA 120,252,12\nQAA 70\nQA 104,228,8\nQB 252,80,13312\nQG 4\n"
"QA 124,228,12\nQJ 64,60,185,70,0\nQA 108,232,8\nQB 228,80,14336\nQAA 71\nQA 128,232,12\nQJ 68,60,185,71,0\nQA 112,236,8\nQB 232,80,15360\nQG 4\nQA 132,236,12\nQJ 72,60,185,70,1024\nQA 96,240,16\n"
"QB 236,80,16384\nQA 116,240,20\nQJ 76,60,185,71,1024\nQA 100,244,16\nQB 240,80,17408\nQG 4\nQA 120,244,20\nQJ 80,60,185,70,2048\nQA 104,248,16\nQB 244,80,18432\nQA 124,248,20\nQJ 84,60,185,71,2048\n"
"QA 108,252,16\nQB 248,80,19456\nQG 4\nQA 128,252,20\nQJ 88,60,185,70,3072\nQA 112,228,16\nQJ 92,60,185,71,3072\nQAH 18\ns_barrier\nQCG 100\nL_pc29:\ns_lshr_b32 s99, %1, 2\nQGB 99\n"
"s_bitcmp1_b32 %0, 0\nQGC 99\nQER 99\ns_min_u32 s99, s99, L_done-L_pc29-512\nQZ 100,99\nQAX 101\nQES 92,185\nQGD 100,90,92\nQB 252,80,20480\nQAA 70\nQA 132,228,20\nQJ 96,60,185,70,0\nQA 96,232,24\n"
"QB 228,80,21504\nQG 4\nQAA 71\nQA 116,232,28\nQJ 100,60,185,71,0\nQA 100,236,24\nQB 232,80,22528\nQA 120,236,28\nQJ 104,60,185,70,1024\nQA 104,240,24\nQB 236,80,23552\nQG 4\nQA 124,240,28\n"
"QJ 108,60,185,71,1024\nQA 108,244,24\nQB 240,80,24576\nQA 128,244,28\nQJ 112,60,185,70,2048\nQA 112,248,24\nQB 244,80,25600\nQG 4\nQA 132,248,28\nQJ 116,60,185,71,2048\nQA 96,252,32\nQB 248,80,26624\n"
"QA 116,252,36\nQJ 120,60,185,70,3072\nQA 100,228,32\nQB 252,80,27648\nQA 120,228,36\nQG 4\nQA 104,232,32\nQB 228,80,28672\nQA 124,232,36\nQJ 124,60,185,71,3072\nQA 108,236,32\nQB 232,80,29696\n"
"QFJ 49\nQA 128,236,36\nQG 4\nQA 112,240,32\nQB 236,80,30720\nQA 132,240,36\nQL 56,89,68,0\nQA 96,244,40\nQB 240,80,31744\nQA 116,244,44\nQG 4\nQA 100,248,40\nQB 244,80,32768\nQA 120,248,44\n"
"QL 56,89,68,1024\nQA 104,252,40\nQB 248,80,33792\nQA 124,252,44\nQG 4\nQA 108,228,40\nQB 252,80,34816\nQA 128,228,44\nQL 56,89,68,2048\nQA 112,232,40\nQB 228,80,35840\nQA 132,232,44\nQG 4\n"
"QA 96,236,48\nQB 232,80,36864\nQA 116,236,52\nQL 56,89,68,3072\nQA 100,240,48\nQB 236,80,37888\nQGH 49\nQA 120,240,52\nQG 4\nQAA 68\nQA 104,244,48\nQB 240,80,38912\nQA 124,244,52\nQL 56,89,68,0\n"
"QA 108,248,48\nQB 244,80,39936\nQBP 68\nQA 128,248,52\nQG 4\nQA 112,252,48\nQAH 14\ns_barrier\nQB 248,80,40960\nQAA 70\nQA 132,252,52\nQJ 0,60,185,70,0\nQA 96,228,56\nQB 252,80,41984\nQG 4\nQAA 71\n"
"QA 116,228,60\nQJ 4,60,185,71,0\nQA 100,232,56\nQB 228,80,43008\nQA 120,232,60\nQJ 8,60,185,70,1024\nQA 104,236,56\nQB 232,80,44032\nQG 4\nQA 124,236,60\nQJ 12,60,185,71,1024\nQA 108,240,56\n"
"QB 236,80,45056\nQA 128,240,60\nQJ 16,60,185,70,2048\nQA 112,244,56\nQB 240,80,46080\nQG 4\nQA 132,244,60\nQJ 20,60,185,71,2048\nQA 96,248,64\nQB 244,80,47104\nQA 116,248,68\nQJ 24,60,185,70,3072\n"
"QA 100,252,64\nQB 248,80,48128\nQA 120,252,68\nQG 4\nQA 104,228,64\nQB 252,80,49152\nQA 124,228,68\nQJ 28,60,185,71,3072\nQA 108,232,64\nQB 228,80,50176\nQBV 49\nQA 128,232,68\nQG 4\nQA 112,236,64\n"
"QB 232,80,51200\nQA 132,236,68\nQL 56,89,68,0\nQA 96,240,72\nQB 236,80,52224\nQA 116,240,76\nQG 4\nQA 100,244,72\nQB 240,80,53248\nQA 120,244,76\nQL 56,89,68,1024\nQA 104,248,72\nQB 244,80,54272\n"
"QA 124,248,76\nQG 4\nQA 108,252,72\nQB 248,80,55296\nQA 128,252,76\nQL 56,89,68,2048\nQA 112,228,72\nQB 252,80,56320\nQA 132,228,76\nQG 4\nQA 96,232,80\nQB 228,80,57344\nQA 116,232,84\n"
"QL 56,89,68,3072\nQA 100,236,80\nQB 232,80,58368\nQCT 49\nQA 120,236,84\nQG 4\nQAA 68\nQA 104,240,80\nQB 236,80,59392\nQA 124,240,84\nQL 56,89,68,0\nQA 108,244,80\nQB 240,80,60416\nQBP 68\n"
"QA 128,244,84\nQG 4\nQA 112,248,80\nQAH 18\ns_barrier\nQB 244,80,61440\nQAA 70\nQA 132,248,84\nQJ 32,60,185,70,0\nQA 96,252,88\nQB 248,80,62464\nQG 4\nQAA 71\nQA 116,252,92\nQJ 36,60,185,71,0\n"
"QA 100,228,88\nQB 252,80,63488\nQA 120,228,92\nQJ 40,60,185,70,1024\nQA 104,232,88\nQB 228,80,64512\nQG 4\nQA 124,232,92\nQJ 44,60,185,71,1024\nQA 108,236,88\nQB 232,81,0\nQA 128,236,92\n"
"QJ 48,60,185,70,2048\nQA 112,240,88\nQB 236,81,1024\nQG 4\nQA 132,240,92\nQJ 52,60,185,71,2048\nQA 96,244,96\nQB 240,81,2048\nQA 116,244,100\nQJ 56,60,185,70,3072\nQA 100,248,96\nQB 244,81,3072\n"
"QA 120,248,100\nQG 4\nQA 104,252,96\nQB 248,81,4096\nQA 124,252,100\nQJ 60,60,185,71,3072\nQA 108,228,96\nQB 252,81,5120\nQEX 49\nQA 128,228,100\nQG 4\nQA 112,232,96\nQB 228,81,6144\nQA 132,232,100\n"
"QL 56,89,68,0\nQA 96,236,104\nQB 232,81,7168\nQA 116,236,108\nQG 4\nQA 100,240,104\nQB 236,81,8192\nQA 120,240,108\nQL 56,89,68,1024\nQA 104,244,104\nQB 240,81,9216\nQA 124,244,108\nQG 4\n"
"QA 108,248,104\nQB 244,81,10240\nQA 128,248,108\nQL 56,89,68,2048\nQA 112,252,104\nQB 248,81,11264\nQA 132,252,108\nQG 4\nQA 96,228,112\nQB 252,81,12288\nQA 116,228,116\nQL 56,89,68,3072\n"
"QA 100,232,112\nQB 228,81,13312\nQEM 49\nQA 120,232,116\nQG 4\nQAA 68\nQA 104,236,112\nQB 232,81,14336\nQA 124,236,116\nQL 56,89,68,0\nQA 108,240,112\nQB 236,81,15360\nQBP 68\nQA 128,240,116\nQG 4\n"
"QA 112,244,112\nQAH 18\ns_barrier\nQB 240,81,36864\nQAA 70\nQA 132,244,116\nQJ 64,60,185,70,0\nQA 96,248,120\nQB 244,81,37888\nQG 4\nQAA 71\nQA 116,248,124\nQJ 68,60,185,71,0\nQA 100,252,120\n"
"QB 248,81,38912\nQA 120,252,124\nQJ 72,60,185,70,1024\nQA 104,228,120\nQB 252,81,39936\nQG 4\nQA 124,228,124\nQJ 76,60,185,71,1024\nQA 108,232,120\nQB 228,81,40960\nQA 128,232,124\n"
"QJ 80,60,185,70,2048\nQA 112,236,120\nQB 232,81,41984\nQG 4\nQA 132,236,124\nQJ 84,60,185,71,2048\nQA 96,240,0\nQB 236,81,43008\nQA 116,240,4\nQJ 88,60,185,70,3072\nQA 100,244,0\nQB 240,81,44032\n"
"QA 120,244,4\nQG 4\nQA 104,248,0\nQB 244,81,45056\nQA 124,248,4\nQJ 92,60,185,71,3072\nQA 108,252,0\nQB 248,81,46080\nQEN 49\nQA 128,252,4\nQG 4\nQA 112,228,0\nQB 252,81,47104\nQA 132,228,4\n"
"QL 56,89,68,0\nQA 96,232,8\nQB 228,81,48128\nQA 116,232,12\nQG 4\nQA 100,236,8\nQB 232,81,49152\nQA 120,236,12\nQL 56,89,68,1024\nQA 104,240,8\nQB 236,81,50176\nQA 124,240,12\nQG 4\nQA 108,244,8\n"
"QB 240,81,51200\nQA 128,244,12\nQL 56,89,68,2048\nQA 112,248,8\nQB 244,81,52224\nQA 132,248,12\nQG 4\nQA 96,252,16\nQB 248,81,53248\nQA 116,252,20\nQL 56,89,68,3072\nQA 100,228,16\nQB 252,81,54272\n"
"s_add_u32 m0, s49, 45056\nQA 120,228,20\nQG 4\nQAA 68\nQA 104,232,16\nQB 228,81,55296\nQA 124,232,20\nQL 56,89,68,0\nQA 108,236,16\nQB 232,81,56320\nQBP 68\nQA 128,236,20\nQG 4\nQA 112,240,16\n"
"QAH 18\ns_barrier\nQB 236,81,57344\nQAA 70\nQA 132,240,20\nQJ 96,60,185,70,0\nQA 96,244,24\nQB 240,81,58368\nQG 4\nQAA 71\nQA 116,244,28\nQJ 100,60,185,71,0\nQA 100,248,24\nQB 244,81,59392\n"
"QA 120,248,28\nQJ 104,60,185,70,1024\nQA 104,252,24\nQB 248,81,60416\nQG 4\nQA 124,252,28\nQJ 108,60,185,71,1024\nQA 108,228,24\nQB 252,81,61440\nQA 128,228,28\nQJ 112,60,185,70,2048\nQA 112,232,24\n"
"QB 228,81,62464\nQG 4\nQA 132,232,28\nQJ 116,60,185,71,2048\nQA 96,236,32\nQB 232,81,63488\nQA 116,236,36\nQJ 120,60,185,70,3072\nQA 100,240,32\nQB 236,81,64512\nQA 120,240,36\nQG 4\nQA 104,244,32\n"
"QB 240,82,0\nQA 124,244,36\nQJ 124,60,185,71,3072\nQA 108,248,32\nQB 244,82,1024\nQFL 49\nQA 128,248,36\nQG 4\nQA 112,252,32\nQB 248,82,2048\nQA 132,252,36\nQL 56,89,68,0\nQA 96,228,40\n"
"QB 252,82,3072\nQA 116,228,44\nQG 4\nQA 100,232,40\nQB 228,82,4096\nQA 120,232,44\nQL 56,89,68,1024\nQA 104,236,40\nQB 232,82,5120\nQA 124,236,44\nQG 4\nQA 108,240,40\nQB 236,82,6144\nQA 128,240,44\n"
"QL 56,89,68,2048\nQA 112,244,40\nQB 240,82,7168\nQA 132,244,44\nQG 4\nQA 96,248,48\nQB 244,82,8192\nQA 116,248,52\nQL 56,89,68,3072\nQA 100,252,48\nQB 248,82,9216\nQDY 49\nQA 120,252,52\nQG 4\n"
"QAA 68\nQA 104,228,48\nQB 252,82,10240\nQA 124,228,52\nQL 56,89,68,0\nQA 108,232,48\nQB 228,82,11264\nQBP 68\nQA 128,232,52\nQG 4\nQA 112,236,48\nQAH 18\ns_barrier\nQB 232,82,12288\nQAA 70\n"
"QA 132,236,52\nQJ 0,60,185,70,0\nQA 96,240,56\nQB 236,82,13312\nQG 4\nQAA 71\nQA 116,240,60\nQJ 4,60,185,71,0\nQA 100,244,56\nQB 240,82,14336\nQA 120,244,60\nQJ 8,60,185,70,1024\nQA 104,248,56\n"
"QB 244,82,15360\nQG 4\nQA 124,248,60\nQJ 12,60,185,71,1024\nQA 108,252,56\nQB 248,82,16384\nQA 128,252,60\nQJ 16,60,185,70,2048\nQA 112,228,56\nQB 252,82,17408\nQG 4\nQA 132,228,60\n"
"QJ 20,60,185,71,2048\nQA 96,232,64\nQB 228,82,18432\nQA 116,232,68\nQJ 24,60,185,70,3072\nQA 100,236,64\nQB 232,82,19456\nQA 120,236,68\nQG 4\nQA 104,240,64\nQB 236,82,20480\nQA 124,240,68\n"
"QJ 28,60,185,71,3072\nQA 108,244,64\nQB 240,82,21504\nQGF 49\nQA 128,244,68\nQG 4\nQA 112,248,64\nQB 244,82,22528\nQA 132,248,68\nQL 56,89,68,0\nQA 96,252,72\nQB 248,82,23552\nQA 116,252,76\nQG 4\n"
"QA 100,228,72\nQB 252,82,24576\nQA 120,228,76\nQL 56,89,68,1024\nQA 104,232,72\nQB 228,82,25600\nQA 124,232,76\nQG 4\nQA 108,236,72\nQB 232,82,26624\nQA 128,236,76\nQL 56,89,68,2048\nQA 112,240,72\n"
"QB 236,82,27648\nQA 132,240,76\nQG 4\nQA 96,244,80\nQB 240,82,28672\nQA 116,244,84\nQL 56,89,68,3072\nQA 100,248,80\nQB 244,82,29696\nQDN 49\nQA 120,248,84\nQG 4\nQAA 68\nQA 104,252,80\n"
"QB 248,82,30720\nQA 124,252,84\nQL 56,89,68,0\nQA 108,228,80\nQB 252,82,31744\nQBP 68\nQA 128,228,84\nQG 4\nQA 112,232,80\nQAH 18\ns_barrier\nQCG 100\nL_pc30:\ns_lshr_b32 s99, %1, 2\nQGB 99\n"
"s_bitcmp1_b32 %0, 0\nQGC 99\nQER 99\ns_min_u32 s99, s99, L_done-L_pc30-512\nQZ 100,99\nQAX 101\nQES 92,185\nQGD 100,90,92\nQB 228,81,16384\nQAA 70\nQA 132,232,84\nQJ 32,60,185,70,0\nQA 96,236,88\n"
"QB 232,81,17408\nQG 4\nQAA 71\nQA 116,236,92\nQJ 36,60,185,71,0\nQA 100,240,88\nQB 236,81,18432\nQA 120,240,92\nQJ 40,60,185,70,1024\nQA 104,244,88\nQB 240,81,19456\nQG 4\nQA 124,244,92\n"
"QJ 44,60,185,71,1024\nQA 108,248,88\nQB 244,81,20480\nQA 128,248,92\nQJ 48,60,185,70,2048\nQA 112,252,88\nQB 248,81,21504\nQG 4\nQA 132,252,92\nQJ 52,60,185,71,2048\nQA 96,228,96\nQB 252,81,22528\n"
"QA 116,228,100\nQJ 56,60,185,70,3072\nQA 100,232,96\nQB 228,81,23552\nQA 120,232,100\nQG 4\nQA 104,236,96\nQB 232,81,24576\nQA 124,236,100\nQJ 60,60,185,71,3072\nQA 108,240,96\nQB 236,81,25600\n"
"QDD 49\nQA 128,240,100\nQG 4\nQA 112,244,96\nQB 240,81,26624\nQA 132,244,100\nQL 56,89,68,0\nQA 96,248,104\nQB 244,81,27648\nQA 116,248,108\nQG 4\nQA 100,252,104\nQB 248,81,28672\nQA 120,252,108\n"
"QL 56,89,68,1024\nQA 104,228,104\nQB 252,81,29696\nQA 124,228,108\nQG 4\nQA 108,232,104\nQB 228,81,30720\nQA 128,232,108\nQL 56,89,68,2048\nQA 112,236,104\nQB 232,81,31744\nQA 132,236,108\nQG 4\n"
"QA 96,240,112\nQB 236,81,32768\nQA 116,240,116\nQL 56,89,68,3072\nQA 100,244,112\nQB 240,81,33792\nQGG 49\nQA 120,244,116\nQG 4\nQAA 68\nQA 104,248,112\nQB 244,81,34816\nQA 124,248,116\n"
"QL 56,89,68,0\nQA 108,252,112\nQB 248,81,35840\nQBP 68\nQA 128,252,116\nQG 4\nQA 112,228,112\nQAH 19\ns_barrier\nQB 252,80,0\nQAA 70\nQA 132,228,116\nQJ 64,60,185,70,0\nQA 96,232,120\nQB 228,80,1024\n"
"QG 4\nQAA 71\nQA 116,232,124\nQJ 68,60,185,71,0\nQA 100,236,120\nQB 232,80,2048\nQA 120,236,124\nQJ 72,60,185,70,1024\nQA 104,240,120\nQB 236,80,3072\nQG 4\nQA 124,240,124\nQJ 76,60,185,71,1024\n"
"QA 108,244,120\nQB 240,80,4096\nQA 128,244,124\nQJ 80,60,185,70,2048\nQA 112,248,120\nQB 244,80,5120\nQG 4\nQA 132,248,124\nQJ 84,60,185,71,2048\nQA 96,252,0\nQB 248,80,6144\nQA 116,252,4\n"
"QJ 88,60,185,70,3072\nQA 100,228,0\nQB 252,80,7168\nQA 120,228,4\nQG 4\nQA 104,232,0\nQB 228,80,8192\nQA 124,232,4\nQJ 92,60,185,71,3072\nQA 108,236,0\nQB 232,80,9216\nQA 128,236,4\nQG 4\n"
"QA 112,240,0\nQB 236,80,10240\nQA 132,240,4\nbuffer_load_dwordx4 v[60:63], v232, s[88:91], s87 offen\nQA 96,244,8\nQB 240,80,11264\nQA 116,244,12\nQG 4\nQA 100,248,8\nQB 244,80,12288\nQA 120,248,12\n"
"buffer_load_dwordx4 v[64:67], v233, s[88:91], s87 offen\nQA 104,252,8\nQB 248,80,13312\nQA 124,252,12\nQG 4\nQA 108,228,8\nQB 252,80,14336\nQA 128,228,12\n"
"buffer_load_dwordx4 v[68:71], v234, s[88:91], s87 offen\nQA 112,232,8\nQB 228,80,15360\nQA 132,232,12\nQG 4\nQA 96,236,16\nQB 232,80,16384\nQA 116,236,20\n"
"buffer_load_dwordx4 v[72:75], v235, s[88:91], s87 offen\nQA 100,240,16\nQB 236,80,17408\nQA 120,240,20\nQG 4\nQA 104,244,16\nQB 240,80,18432\nQA 124,244,20\n"
"buffer_load_dwordx4 v[76:79], v236, s[88:91], s87 offen\nQA 108,248,16\nQB 244,80,19456\nQA 128,248,20\nQG 4\nQA 112,252,16\nQAH 18\ns_barrier\nQB 248,80,20480\nQAA 70\nQA 132,252,20\n"
"QJ 96,60,185,70,0\nQA 96,228,24\nQB 252,80,21504\nQAA 71\nQA 116,228,28\nQG 4\nQA 100,232,24\nQB 228,80,22528\nQA 120,232,28\nQJ 100,60,185,71,0\nQA 104,236,24\nQB 232,80,23552\nQA 124,236,28\nQG 4\n"
"QA 108,240,24\nQB 236,80,24576\nQA 128,240,28\nQJ 104,60,185,70,1024\nQA 112,244,24\nQB 240,80,25600\nQA 132,244,28\nQG 4\nQA 96,248,32\nQB 244,80,26624\nQA 116,248,36\nQJ 108,60,185,71,1024\n"
"QA 100,252,32\nQB 248,80,27648\nQA 120,252,36\nQG 4\nQA 104,228,32\nQB 252,80,28672\nQA 124,228,36\nQJ 112,60,185,70,2048\nQA 108,232,32\nQB 228,80,29696\nQA 128,232,36\nQG 4\nQA 112,236,32\n"
"QB 232,80,30720\nQA 132,236,36\nQJ 116,60,185,71,2048\nQA 96,240,40\nQB 236,80,31744\nQA 116,240,44\nQG 4\nQA 100,244,40\nQB 240,80,32768\nQA 120,244,44\nQJ 120,60,185,70,3072\nQA 104,248,40\n"
"QB 244,80,33792\nQA 124,248,44\nQG 4\nQA 108,252,40\nQB 248,80,34816\nQA 128,252,44\nQJ 124,60,185,71,3072\nQA 112,228,40\nQB 252,80,35840\nQA 132,228,44\nQG 4\nQA 96,232,48\nQB 228,80,36864\n"
"QA 116,232,52\nQA 100,236,48\nQB 232,80,37888\nQA 120,236,52\nQG 4\nQA 104,240,48\nQB 236,80,38912\nQA 124,240,52\nQA 108,244,48\nQB 240,80,39936\nQA 128,244,52\nQG 4\nQA 112,248,48\nQAH 13\n"
"s_barrier\nQB 244,80,40960\nQAA 70\nQA 132,248,52\nQJ 0,60,185,70,0\nQA 96,252,56\nQB 248,80,41984\nQAA 71\nQA 116,252,60\nQG 4\nQA 100,228,56\nQB 252,80,43008\nQA 120,228,60\nQJ 4,60,185,71,0\n"
"QA 104,232,56\nQB 228,80,44032\nQA 124,232,60\nQG 4\nQA 108,236,56\nQB 232,80,45056\nQA 128,236,60\nQJ 8,60,185,70,1024\nQA 112,240,56\nQB 236,80,46080\nQA 132,240,60\nQG 4\nQA 96,244,64\n"
"QB 240,80,47104\nQA 116,244,68\nQJ 12,60,185,71,1024\nQA 100,248,64\nQB 244,80,48128\nQA 120,248,68\nQG 4\nQA 104,252,64\nQB 248,80,49152\nQA 124,252,68\nQJ 16,60,185,70,2048\nQA 108,228,64\n"
"QB 252,80,50176\nQA 128,228,68\nQG 4\nQA 112,232,64\nQB 228,80,51200\nQA 132,232,68\nQJ 20,60,185,71,2048\nQA 96,236,72\nQB 232,80,52224\nQA 116,236,76\nQG 4\nQA 100,240,72\nQB 236,80,53248\n"
"QA 120,240,76\nQJ 24,60,185,70,3072\nQA 104,244,72\nQB 240,80,54272\nQA 124,244,76\nQG 4\nQA 108,248,72\nQB 244,80,55296\nQA 128,248,76\nQJ 28,60,185,71,3072\nQA 112,252,72\nQB 248,80,56320\n"
"QA 132,252,76\nQG 4\nQA 96,228,80\nQB 252,80,57344\nQA 116,228,84\nQA 100,232,80\nQB 228,80,58368\nQA 120,232,84\nQG 4\nQA 104,236,80\nQB 232,80,59392\nQA 124,236,84\nQA 108,240,80\nQB 236,80,60416\n"
"QA 128,240,84\nQG 4\nQA 112,244,80\nQAH 8\ns_barrier\nQB 240,80,61440\nQAA 70\nQA 132,244,84\nQJ 32,60,185,70,0\nQA 96,248,88\nQB 244,80,62464\nQAA 71\nQA 116,248,92\nQG 4\nQAH 9\nQU 192,60\n"
"QA 100,252,88\nQB 248,80,63488\nQY 193,60\nQA 120,252,92\nQJ 36,60,185,71,0\nQA 104,228,88\nQB 252,80,64512\nQU 194,61\nQA 124,228,92\nQG 4\nQY 195,61\nQU 196,62\nQA 108,232,88\nQB 228,81,0\n"
"QY 197,62\nQA 128,232,92\nQJ 40,60,185,70,1024\nQA 112,236,88\nQB 232,81,1024\nQU 198,63\nQA 132,236,92\nQG 4\nQY 199,63\nQU 200,64\nQA 96,240,96\nQB 236,81,2048\nQY 201,64\nQA 116,240,100\n"
"QJ 44,60,185,71,1024\nQA 100,244,96\nQB 240,81,3072\nQU 202,65\nQA 120,244,100\nQG 4\nQY 203,65\nQU 204,66\nQA 104,248,96\nQB 244,81,4096\nQY 205,66\nQA 124,248,100\nQU 206,67\nQY 207,67\n"
"QA 108,252,96\nQB 248,81,5120\nQU 208,68\nQA 128,252,100\nQG 4\nQY 209,68\nQU 210,69\nQA 112,228,96\nQB 252,81,6144\nQY 211,69\nQA 132,228,100\nQU 212,70\nQY 213,70\nQA 96,232,104\nQB 228,81,7168\n"
"QU 214,71\nQA 116,232,108\nQG 4\nQY 215,71\nQU 216,72\nQA 100,236,104\nQB 232,81,8192\nQY 217,72\nQA 120,236,108\nQU 218,73\nQY 219,73\nQA 104,240,104\nQB 236,81,9216\nQU 220,74\nQA 124,240,108\n"
"QG 4\nQY 221,74\nQU 222,75\nQA 108,244,104\nQB 240,81,10240\nQY 223,75\nQA 128,244,108\nQU 224,76\nQY 225,76\nQA 112,248,104\nQB 244,81,11264\nQU 226,77\nQA 132,248,108\nQG 4\nQY 227,77\nQU 228,78\n"
"QA 96,252,112\nQB 248,81,12288\nQY 229,78\nQA 116,252,116\nQU 230,79\nQY 231,79\nQA 100,228,112\nQB 252,81,13312\ns_mov_b64 s[88:89], %8\nQA 120,228,116\nQG 4\nQDL 90\nQBA 91\nQA 104,232,112\n"
"QB 228,81,14336\nQA 124,232,116\nQA 108,236,112\nQB 232,81,15360\nQA 128,236,116\nQG 4\nQA 112,240,112\nQAH 4\ns_barrier\ns_bfe_u32 s99, %1, 131072\ns_lshl_b32 s99, s99, 4\n"
"s_bfe_u32 s100, %1, 262148\nQZ 99,100\nQBU 99\nQBP 99\nQCZ 92,84,99\nQB 236,81,36864\nQA 132,240,116\nQA 96,244,120\nQB 240,81,37888\nQA 116,244,124\nQG 4\nQA 100,248,120\nQB 244,81,38912\n"
"QA 120,248,124\nQA 104,252,120\nQB 248,81,39936\nQA 124,252,124\nQG 4\nQA 108,228,120\nQB 252,81,40960\nQA 128,228,124\nQA 112,232,120\nQB 228,81,41984\nQA 132,232,124\nQG 4\nQA 96,236,0\n"
"QB 232,81,43008\nQA 116,236,4\nQA 100,240,0\nQB 236,81,44032\nQA 120,240,4\nQG 4\nQA 104,244,0\nQB 240,81,45056\nQA 124,244,4\nQA 108,248,0\nQB 244,81,46080\nQA 128,248,4\nQG 4\nQA 112,252,0\n"
"QB 248,81,47104\nQA 132,252,4\nQA 96,228,8\nQB 252,81,48128\nQA 116,228,12\nQG 4\nQA 100,232,8\nQB 228,81,49152\nQA 120,232,12\nQA 104,236,8\nQB 232,81,50176\nQA 124,236,12\nQG 4\nQA 108,240,8\n"
"QB 236,81,51200\nQA 128,240,12\nQA 112,244,8\nQB 240,81,52224\nQA 132,244,12\nQG 4\nQA 96,248,16\nQB 244,81,53248\nQA 116,248,20\nQA 100,252,16\nQB 248,81,54272\nQA 120,252,20\nQG 4\nQA 104,228,16\n"
"QB 252,81,55296\nQA 124,228,20\nQA 108,232,16\nQB 228,81,56320\nQA 128,232,20\nQG 4\nQA 112,236,16\nQAH 1\ns_barrier\nQAH 0\nv_cmp_ne_u32 vcc, 2, v84\nQDC 85\nQBE \ns_cbranch_vccnz L_no6\n"
"QAW 40,44,185,52,0\nQAW 44,44,185,52,1024\nQAW 48,44,185,52,2048\nQAW 52,44,185,52,3072\nQAW 56,44,243,52,0\ns_add_u32 s52, s52, 40960\nQAW 60,44,185,52,0\nQAW 64,44,185,52,1024\n"
"QAW 68,44,185,52,2048\nQAW 72,44,185,52,3072\nQAW 76,44,243,52,0\ns_add_u32 s52, s52, 40960\ns_mov_b32 s85, 1\nL_no6:\nQB 232,81,57344\nQA 132,236,20\nQA 96,240,24\nQB 236,81,58368\nQA 116,240,28\n"
"QG 4\nQA 100,244,24\nQB 240,81,59392\nQA 120,244,28\nQA 104,248,24\nQB 244,81,60416\nQA 124,248,28\nQG 4\nQA 108,252,24\nQB 248,81,61440\nQA 128,252,28\nQA 112,228,24\nQB 252,81,62464\nQA 132,228,28\n"
"QG 4\nQA 96,232,32\nQB 228,81,63488\nQA 116,232,36\nQA 100,236,32\nQB 232,81,64512\nQA 120,236,36\nQG 4\nQA 104,240,32\nQB 236,82,0\nQA 124,240,36\nQA 108,244,32\nQB 240,82,1024\nQA 128,244,36\nQG 4\n"
"QA 112,248,32\nQA 132,248,36\nQA 96,252,40\nQA 116,252,44\nQG 3\nQA 100,228,40\nQA 120,228,44\nQG 2\nQA 104,232,40\nQA 124,232,44\nQG 1\nQA 108,236,40\nQA 128,236,44\nQG 0\nQA 112,240,40\n"
"QA 132,240,44\nQBC 136,96\nQBC 137,97\nQBC 138,98\nQBC 139,99\nQBC 140,100\nQBC 141,101\nQBC 142,102\nQBC 143,103\nQBC 144,104\nQBC 145,105\nQBC 146,106\nQBC 147,107\nQBC 148,108\nQBC 149,109\n"
"QBC 150,110\nQBC 151,111\nQBC 152,112\nQBC 153,113\nQBC 154,114\nQBC 155,115\nQBC 156,116\nQBC 157,117\nQBC 158,118\nQBC 159,119\nQBC 160,120\nQBC 161,121\nQBC 162,122\nQBC 163,123\nQBC 164,124\n"
"QBC 165,125\nQBC 166,126\nQBC 167,127\nQBC 168,128\nQBC 169,129\nQBC 170,130\nQBC 171,131\nQBC 172,132\nQBC 173,133\nQBC 174,134\nQBC 175,135\nQW 136\nQW 137\nQW 138\nQW 139\nQW 140\nQW 141\nQW 142\n"
"QW 143\nQW 144\nQW 145\nQW 146\nQW 147\nQW 148\nQW 149\nQW 150\nQW 151\nQW 152\nQW 153\nQW 154\nQW 155\nQW 156\nQW 157\nQW 158\nQW 159\nQW 160\nQW 161\nQW 162\nQW 163\nQW 164\nQW 165\nQW 166\nQW 167\n"
"QW 168\nQW 169\nQW 170\nQW 171\nQW 172\nQW 173\nQW 174\nQW 175\ns_bfe_u32 s99, %1, 131072\ns_lshl_b32 s99, s99, 4\ns_bfe_u32 s100, %1, 262148\nQZ 99,100\nQBU 99\nQBP 99\nQCZ 92,84,99\nQAK 136\n"
"QAK 138\nQAK 140\nQAK 142\nQAK 144\nQAK 146\nQAK 148\nQAK 150\nQAK 152\nQAK 154\nQAK 156\nQAK 158\nQAK 160\nQAK 162\nQAK 164\nQAK 166\nQAK 168\nQAK 170\nQAK 172\nQAK 174\nQV 136\nQV 137\nQV 138\n"
"QV 139\nQV 140\nQV 141\nQV 142\nQV 143\nQV 144\nQV 145\nQV 146\nQV 147\nQV 148\nQV 149\nQV 150\nQV 151\nQV 152\nQV 153\nQV 154\nQV 155\nQV 156\nQV 157\nQV 158\nQV 159\nQV 160\nQV 161\nQV 162\nQV 163\n"
"QV 164\nQV 165\nQV 166\nQV 167\nQV 168\nQV 169\nQV 170\nQV 171\nQV 172\nQV 173\nQV 174\nQV 175\nQX 96,136\nQX 98,138\nQX 100,140\nQX 102,142\nQX 104,144\nQX 106,146\nQX 108,148\nQX 110,150\n"
"QX 112,152\nQX 114,154\nQX 116,156\nQX 118,158\nQX 120,160\nQX 122,162\nQX 124,164\nQX 126,166\nQX 128,168\nQX 130,170\nQX 132,172\nQX 134,174\ns_cmp_eq_u32 s85, 1\ns_cbranch_scc1 L_got6\nQAH 0\n"
"v_cmp_ne_u32 vcc, 2, v84\nQBD \ns_cbranch_vccnz L_ps31\nL_pr31:\nQAW 40,44,185,52,0\nQAW 44,44,185,52,1024\nQAW 48,44,185,52,2048\nQAW 52,44,185,52,3072\nQAW 56,44,243,52,0\n"
"s_add_u32 s52, s52, 40960\nQAW 60,44,185,52,0\nQAW 64,44,185,52,1024\nQAW 68,44,185,52,2048\nQAW 72,44,185,52,3072\nQAW 76,44,243,52,0\ns_add_u32 s52, s52, 40960\nL_got6:\nQAH 0\nQU 136,40\n"
"QY 137,40\nQU 138,41\nQY 139,41\nQU 140,42\nQY 141,42\nQU 142,43\nQY 143,43\nQU 144,44\nQY 145,44\nQU 146,45\nQY 147,45\nQU 148,46\nQY 149,46\nQU 150,47\nQY 151,47\nQU 152,48\nQY 153,48\nQU 154,49\n"
"QY 155,49\nQU 156,50\nQY 157,50\nQU 158,51\nQY 159,51\nQU 160,52\nQY 161,52\nQU 162,53\nQY 163,53\nQU 164,54\nQY 165,54\nQU 166,55\nQY 167,55\nQU 168,56\nQY 169,56\nQU 170,57\nQY 171,57\nQU 172,58\n"
"QY 173,58\nQU 174,59\nQY 175,59\nQAI 0,136\nQAI 2,138\nQAI 4,140\nQAI 6,142\nQAI 8,144\nQAI 10,146\nQAI 12,148\nQAI 14,150\nQAI 20,152\nQAI 22,154\nQAI 24,156\nQAI 26,158\nQAI 28,160\nQAI 30,162\n"
"QAI 32,164\nQAI 34,166\nQAI 16,168\nQAI 18,170\nQAI 36,172\nQAI 38,174\nQU 136,60\nQY 137,60\nQU 138,61\nQY 139,61\nQU 140,62\nQY 141,62\nQU 142,63\nQY 143,63\nQU 144,64\nQY 145,64\nQU 146,65\n"
"QY 147,65\nQU 148,66\nQY 149,66\nQU 150,67\nQY 151,67\nQU 152,68\nQY 153,68\nQU 154,69\nQY 155,69\nQU 156,70\nQY 157,70\nQU 158,71\nQY 159,71\nQU 160,72\nQY 161,72\nQU 162,73\nQY 163,73\nQU 164,74\n"
"QY 165,74\nQU 166,75\nQY 167,75\nQU 168,76\nQY 169,76\nQU 170,77\nQY 171,77\nQU 172,78\nQY 173,78\nQU 174,79\nQY 175,79\nQAI 0,136\nQAI 2,138\nQAI 4,140\nQAI 6,142\nQAI 8,144\nQAI 10,146\nQAI 12,148\n"
"QAI 14,150\nQAI 20,152\nQAI 22,154\nQAI 24,156\nQAI 26,158\nQAI 28,160\nQAI 30,162\nQAI 32,164\nQAI 34,166\nQAI 16,168\nQAI 18,170\nQAI 36,172\nQAI 38,174\nQX 96,0\nQX 98,2\nQAY 96,97\nQF 97,98,99\n"
"QCD 96,242,0\nQX 100,4\nQX 102,6\nQAY 100,101\nQF 101,102,103\nQCD 100,242,32\nQX 104,8\nQX 106,10\nQAY 104,105\nQF 105,106,107\nQCD 104,242,64\nQX 108,12\nQX 110,14\nQAY 108,109\nQF 109,110,111\n"
"QCD 108,242,96\nQX 112,16\nQX 114,18\nQAY 112,113\nQF 113,114,115\nQCD 112,242,128\nQX 116,20\nQX 118,22\nQAY 116,117\nQF 117,118,119\nQCD 116,242,2560\nQX 120,24\nQX 122,26\nQAY 120,121\n"
"QF 121,122,123\nQCD 120,242,2592\nQX 124,28\nQX 126,30\nQAY 124,125\nQF 125,126,127\nQCD 124,242,2624\nQX 128,32\nQX 130,34\nQAY 128,129\nQF 129,130,131\nQCD 128,242,2656\nQX 132,36\nQX 134,38\n"
"QAY 132,133\nQF 133,134,135\nQCD 132,242,2688\nQH 40,237,0\nQH 44,237,1024\nQH 48,237,2048\nQH 52,237,3072\nQH 56,237,4096\nQG 4\nQU 136,40\nQY 137,40\nQU 138,41\nQY 139,41\nQU 140,42\nQY 141,42\n"
"QU 142,43\nQY 143,43\nQAI 136,192\nQAI 138,194\nQAI 140,196\nQAI 142,198\nQF 40,136,137\nQF 41,138,139\nQF 42,140,141\nQF 43,142,143\nbuffer_store_dwordx4 v[40:43], v232, s[88:91], s87 offen\nQG 3\n"
"QU 136,44\nQY 137,44\nQU 138,45\nQY 139,45\nQU 140,46\nQY 141,46\nQU 142,47\nQY 143,47\nQAI 136,200\nQAI 138,202\nQAI 140,204\nQAI 142,206\nQF 44,136,137\nQF 45,138,139\nQF 46,140,141\nQF 47,142,143\n"
"buffer_store_dwordx4 v[44:47], v233, s[88:91], s87 offen\nQG 2\nQU 136,48\nQY 137,48\nQU 138,49\nQY 139,49\nQU 140,50\nQY 141,50\nQU 142,51\nQY 143,51\nQAI 136,208\nQAI 138,210\nQAI 140,212\n"
"QAI 142,214\nQF 48,136,137\nQF 49,138,139\nQF 50,140,141\nQF 51,142,143\nbuffer_store_dwordx4 v[48:51], v234, s[88:91], s87 offen\nQG 1\nQU 136,52\nQY 137,52\nQU 138,53\nQY 139,53\nQU 140,54\n"
"QY 141,54\nQU 142,55\nQY 143,55\nQAI 136,216\nQAI 138,218\nQAI 140,220\nQAI 142,222\nQF 52,136,137\nQF 53,138,139\nQF 54,140,141\nQF 55,142,143\n"
"buffer_store_dwordx4 v[52:55], v235, s[88:91], s87 offen\nQG 0\nQU 136,56\nQY 137,56\nQU 138,57\nQY 139,57\nQU 140,58\nQY 141,58\nQU 142,59\nQY 143,59\nQAI 136,224\nQAI 138,226\nQAI 140,228\n"
"QAI 142,230\nQF 56,136,137\nQF 57,138,139\nQF 58,140,141\nQF 59,142,143\nbuffer_store_dwordx4 v[56:59], v236, s[88:91], s87 offen\ns_branch L_done\nL_ps18:\nQDB 101\nL_pl18:\nQDP \nQCV 165,185\n"
"QCW 165\ns_mov_b32 s99, 6144\nQEB 92,164,165,99\nQCS 101\nQBZ 101\nQAH 0\ns_cbranch_scc1 L_pr18\nQHH 164\nQDR \ns_cbranch_vccnz L_pl18\ns_branch L_pr18\nL_ps19:\nQDB 101\nL_pl19:\nQDP \nQCV 165,185\n"
"QCW 165\ns_mov_b32 s99, 6144\nQEB 92,164,165,99\nQCS 101\nQBZ 101\nQAH 0\ns_cbranch_scc1 L_pr19\nQHH 164\nQDR \ns_cbranch_vccnz L_pl19\ns_branch L_pr19\nL_ps22:\nQDB 101\nL_pl22:\nQDP \nQCV 85,185\n"
"QCW 85\ns_mov_b32 s99, 8192\nQEB 92,84,85,99\nQCS 101\nQBZ 101\nQAH 0\ns_cbranch_scc1 L_pr22\nQHH 84\nQDR \ns_cbranch_vccnz L_pl22\ns_branch L_pr22\nL_ps23:\nQDB 101\nL_pl23:\nQDP \nQGY 99,54\n"
"s_min_u32 s99, s99, s54\nQBU 99\ns_add_u32 s99, s99, 10240\nQCZ 92,84,99\nQCS 101\nQBZ 101\nQAH 0\ns_cbranch_scc1 L_pr23\nv_cmp_ne_u32 vcc, 32, v84\nQDR \ns_cbranch_vccnz L_pl23\ns_branch L_pr23\n"
"L_ps27:\nQDB 101\nL_pl27:\nQDP \nQCV 85,185\nQCW 85\ns_mov_b32 s99, 24576\nQEB 92,84,85,99\nQCS 101\nQBZ 101\nQAH 0\ns_cbranch_scc1 L_pr27\nQFF 84\nQDR \ns_cbranch_vccnz L_pl27\ns_branch L_pr27\n"
"L_ps28:\nQDB 101\nL_pl28:\nQDP \ns_bfe_u32 s99, %1, 131072\nQBH 99\ns_bfe_u32 s100, %1, 131078\nQZ 99,100\nQBU 99\nQEP 99\nQCZ 92,84,99\nQCS 101\nQBZ 101\nQAH 0\ns_cbranch_scc1 L_pr28\nQFF 84\nQDR \n"
"s_cbranch_vccnz L_pl28\ns_branch L_pr28\nL_ps31:\nQDB 101\nL_pl31:\nQDP \ns_bfe_u32 s99, %1, 131072\ns_lshl_b32 s99, s99, 4\ns_bfe_u32 s100, %1, 262148\nQZ 99,100\nQBU 99\nQBP 99\nQCZ 92,84,99\n"
"QCS 101\nQBZ 101\nQAH 0\ns_cbranch_scc1 L_pr31\nv_cmp_ne_u32 vcc, 2, v84\nQDR \ns_cbranch_vccnz L_pl31\ns_branch L_pr31\nL_done:\n.purgem QA\n.purgem QB\n.purgem QC\n.purgem QD\n.purgem QE\n"
".purgem QF\n.purgem QG\n.purgem QH\n.purgem QI\n.purgem QJ\n.purgem QK\n.purgem QL\n.purgem QM\n.purgem QN\n.purgem QO\n.purgem QP\n.purgem QR\n.purgem QS\n.purgem QT\n.purgem QU\n.purgem QV\n"
".purgem QW\n.purgem QX\n.purgem QY\n.purgem QZ\n.purgem QAA\n.purgem QAB\n.purgem QAC\n.purgem QAD\n.purgem QAE\n.purgem QAF\n.purgem QAG\n.purgem QAH\n.purgem QAI\n.purgem QAJ\n.purgem QAK\n"
".purgem QAL\n.purgem QAM\n.purgem QAN\n.purgem QAO\n.purgem QAP\n.purgem QAR\n.purgem QAS\n.purgem QAT\n.purgem QAU\n.purgem QAV\n.purgem QAW\n.purgem QAX\n.purgem QAY\n.purgem QAZ\n.purgem QBA\n"
".purgem QBB\n.purgem QBC\n.purgem QBD\n.purgem QBE\n.purgem QBF\n.purgem QBG\n.purgem QBH\n.purgem QBI\n.purgem QBJ\n.purgem QBK\n.purgem QBL\n.purgem QBM\n.purgem QBN\n.purgem QBO\n.purgem QBP\n"
".purgem QBR\n.purgem QBS\n.purgem QBT\n.purgem QBU\n.purgem QBV\n.purgem QBW\n.purgem QBX\n.purgem QBY\n.purgem QBZ\n.purgem QCA\n.purgem QCB\n.purgem QCC\n.purgem QCD\n.purgem QCE\n.purgem QCF\n"
".purgem QCG\n.purgem QCH\n.purgem QCI\n.purgem QCJ\n.purgem QCK\n.purgem QCL\n.purgem QCM\n.purgem QCN\n.purgem QCO\n.purgem QCP\n.purgem QCR\n.purgem QCS\n.purgem QCT\n.purgem QCU\n.purgem QCV\n"
".purgem QCW\n.purgem QCX\n.purgem QCY\n.purgem QCZ\n.purgem QDA\n.purgem QDB\n.purgem QDC\n.purgem QDD\n.purgem QDE\n.purgem QDF\n.purgem QDG\n.purgem QDH\n.purgem QDI\n.purgem QDJ\n.purgem QDK\n"
".purgem QDL\n.purgem QDM\n.purgem QDN\n.purgem QDO\n.purgem QDP\n.purgem QDR\n.purgem QDS\n.purgem QDT\n.purgem QDU\n.purgem QDV\n.purgem QDW\n.purgem QDX\n.purgem QDY\n.purgem QDZ\n.purgem QEA\n"
".purgem QEB\n.purgem QEC\n.purgem QED\n.purgem QEE\n.purgem QEF\n.purgem QEG\n.purgem QEH\n.purgem QEI\n.purgem QEJ\n.purgem QEK\n.purgem QEL\n.purgem QEM\n.purgem QEN\n.purgem QEO\n.purgem QEP\n"
".purgem QER\n.purgem QES\n.purgem QET\n.purgem QEU\n.purgem QEV\n.purgem QEW\n.purgem QEX\n.purgem QEY\n.purgem QEZ\n.purgem QFA\n.purgem QFB\n.purgem QFC\n.purgem QFD\n.purgem QFE\n.purgem QFF\n"
".purgem QFG\n.purgem QFH\n.purgem QFI\n.purgem QFJ\n.purgem QFK\n.purgem QFL\n.purgem QFM\n.purgem QFN\n.purgem QFO\n.purgem QFP\n.purgem QFR\n.purgem QFS\n.purgem QFT\n.purgem QFU\n.purgem QFV\n"
".purgem QFW\n.purgem QFX\n.purgem QFY\n.purgem QFZ\n.purgem QGA\n.purgem QGB\n.purgem QGC\n.purgem QGD\n.purgem QGE\n.purgem QGF\n.purgem QGG\n.purgem QGH\n.purgem QGI\n.purgem QGJ\n.purgem QGK\n"
".purgem QGL\n.purgem QGM\n.purgem QGN\n.purgem QGO\n.purgem QGP\n.purgem QGR\n.purgem QGS\n.purgem QGT\n.purgem QGU\n.purgem QGV\n.purgem QGW\n.purgem QGX\n.purgem QGY\n.purgem QGZ\n.purgem QHA\n"
".purgem QHB\n.purgem QHC\n.purgem QHD\n.purgem QHE\n.purgem QHF\n.purgem QHG\n.purgem QHH\n"
  :: "s"(wid), "s"(blockIdx.x), "s"(ws), "s"(x), "s"(ckv), "s"(wq), "s"(wuk), "s"(wuv), "s"(out), "s"(w1), "s"(w2), "s"(w3), "s"(w4), "s"(wo) : "v0","v1","v2","v3","v4","v5","v6","v7","v8","v9","v10","v11","v12","v13","v14","v15","v16","v17","v18","v19","v20","v21","v22","v23","v24","v25","v26","v27","v28","v29","v30","v31","v32","v33","v34","v35","v36","v37","v38","v39","v40","v41","v42","v43","v44","v45","v46","v47","v48","v49","v50","v51","v52","v53","v54","v55","v56","v57","v58","v59","v60","v61","v62","v63","v64","v65","v66","v67","v68","v69","v70","v71","v72","v73","v74","v75","v76","v77","v78","v79","v80","v81","v82","v83","v84","v85","v86","v87","v88","v89","v90","v91","v92","v93","v94","v95","v96","v97","v98","v99","v100","v101","v102","v103","v104","v105","v106","v107","v108","v109","v110","v111","v112","v113","v114","v115","v116","v117","v118","v119","v120","v121","v122","v123","v124","v125","v126","v127","v128","v129","v130","v131","v132","v133","v134","v135","v136","v137","v138","v139","v140","v141","v142","v143","v144","v145","v146","v147","v148","v149","v150","v151","v152","v153","v154","v155","v156","v157","v158","v159","v160","v161","v162","v163","v164","v165","v166","v167","v168","v169","v170","v171","v172","v173","v174","v175","v176","v177","v178","v179","v180","v181","v182","v183","v184","v185","v186","v187","v188","v189","v190","v191","v192","v193","v194","v195","v196","v197","v198","v199","v200","v201","v202","v203","v204","v205","v206","v207","v208","v209","v210","v211","v212","v213","v214","v215","v216","v217","v218","v219","v220","v221","v222","v223","v224","v225","v226","v227","v228","v229","v230","v231","v232","v233","v234","v235","v236","v237","v238","v239","v240","v241","v242","v243","v244","v245","v246","v247","v248","v249","v250","v251","v252","v253","v254","v255","a0","a1","a2","a3","a4","a5","a6","a7","a8","a9","a10","a11","a12","a13","a14","a15","a16","a17","a18","a19","a20","a21","a22","a23","a24","a25","a26","a27","a28","a29","a30","a31","a32","a33","a34","a35","a36","a37","a38","a39","a40","a41","a42","a43","a44","a45","a46","a47","a48","a49","a50","a51","a52","a53","a54","a55","a56","a57","a58","a59","a60","a61","a62","a63","a64","a65","a66","a67","a68","a69","a70","a71","a72","a73","a74","a75","a76","a77","a78","a79","a80","a81","a82","a83","a84","a85","a86","a87","a88","a89","a90","a91","a92","a93","a94","a95","a96","a97","a98","a99","a100","a101","a102","a103","a104","a105","a106","a107","a108","a109","a110","a111","a112","a113","a114","a115","a116","a117","a118","a119","a120","a121","a122","a123","a124","a125","a126","a127","a128","a129","a130","a131","a132","a133","a134","a135","a136","a137","a138","a139","a140","a141","a142","a143","a144","a145","a146","a147","a148","a149","a150","a151","a152","a153","a154","a155","a156","a157","a158","a159","a160","a161","a162","a163","a164","a165","a166","a167","a168","a169","a170","a171","a172","a173","a174","a175","a176","a177","a178","a179","a180","a181","a182","a183","a184","a185","a186","a187","a188","a189","a190","a191","a192","a193","a194","a195","a196","a197","a198","a199","a200","a201","a202","a203","a204","a205","a206","a207","a208","a209","a210","a211","a212","a213","a214","a215","a216","a217","a218","a219","a220","a221","a222","a223","a224","a225","a226","a227","a228","a229","a230","a231","a232","a233","a234","a235","a236","a237","a238","a239","a240","a241","a242","a243","a244","a245","a246","a247","a248","a249","a250","a251","a252","a253","a254","a255","s36","s37","s38","s39","s40","s41","s42","s43","s44","s45","s46","s47","s48","s49","s50","s51","s52","s53","s54","s55","s56","s57","s58","s59","s60","s61","s62","s63","s64","s65","s66","s67","s68","s69","s70","s71","s72","s73","s74","s75","s76","s77","s78","s79","s80","s81","s82","s83","s84","s85","s86","s87","s88","s89","s90","s91","s92","s93","s94","s95","s96","s97","s98","s99","s100","s101","vcc","scc","memory");
}
extern "C" void kernel_launch(void* const* d_in, const int* in_sizes, int n_in, void* d_out, int out_size, void* d_ws, size_t ws_size, hipStream_t st){
  char* ws=(char*)d_ws;
  (void)hipFuncSetAttribute((const void*)k_mega,hipFuncAttributeMaxDynamicSharedMemorySize,160*1024);
  k_mega<<<256,256,160*1024,st>>>(ws,(const char*)d_in[0],(const char*)d_in[1],(const char*)d_in[2],(const char*)d_in[3],(const char*)d_in[4],(char*)d_out,
    (const char*)d_in[7],(const char*)d_in[8],(const char*)d_in[9],(const char*)d_in[10],(const char*)d_in[5]);
}
```
